```python
import torch

def _softmax(x, axis=-1):
    return torch.softmax(x, dim=axis)
def _gelu(x):
    return torch.nn.functional.gelu(x, approximate="tanh")
def _silu(x):
    return torch.nn.functional.silu(x)
def _sigmoid(x):
    return torch.sigmoid(x)
def _layernorm(x, axis=-1, eps=1e-5):
    m = x.mean(dim=axis, keepdim=True)
    v = x.var(dim=axis, keepdim=True, unbiased=False)
    return (x - m) / torch.sqrt(v + eps)
def _scatter_add(tgt, idx, src, axis=0):
    return tgt.clone().index_add_(axis, idx, src)
def _segment_sum(src, seg, n):
    out = torch.zeros((n,) + src.shape[1:], dtype=src.dtype)
    return out.index_add_(0, seg, src)
def _pad(x, pw):
    flat = tuple(v for pair in reversed(pw) for v in pair)
    return torch.nn.functional.pad(x, flat)

kernel_name = 'interleave_layers__ssm_conv_h3__dense_activation_relu__prompt'

def setup_inputs():

    _w = (lambda a: (lambda t: t.to(torch.bfloat16) if t.is_floating_point() else t)(torch.as_tensor(a)))
    def _A_setup():
        B, S, D = 1, 2048, 448
        ks = 4
        return {
            "q": _w((lambda *s: torch.randn(*s, dtype=torch.bfloat16) * 0.05)(B, S, D)),
            "k": _w((lambda *s: torch.randn(*s, dtype=torch.bfloat16) * 0.8)(B, S, D)),
            "v": _w((lambda *s: torch.randn(*s, dtype=torch.bfloat16) * 0.8)(B, S, D)),
            "shift_k": _w((lambda *s: torch.randn(*s, dtype=torch.bfloat16) * 0.8)(D, ks) * 0.5),
            "ssm_k": _w((lambda *s: torch.randn(*s, dtype=torch.bfloat16) * 0.8)(D, S) * (1.0 / S ** 0.5)),
            "ssm_D": _w((lambda *s: torch.randn(*s, dtype=torch.bfloat16) * 0.8)(D)),
        }
    _A_in = _A_setup()

    def _B_setup():
        B, S, D, Dff = 1, 2048, 448, 1792
        return {
            "x": _w((lambda *s: torch.randn(*s, dtype=torch.bfloat16) * 0.05)(B, S, D)),
            "w1": _w((lambda *s: torch.randn(*s, dtype=torch.bfloat16) * 0.1)(D, Dff)),
            "w2": _w((lambda *s: torch.randn(*s, dtype=torch.bfloat16) * 0.1)(Dff, D)),
        }
    _B_in = _B_setup()

    return {
        "A_q": _A_in["q"],
        "A_k": _A_in["k"],
        "A_v": _A_in["v"],
        "A_shift_k": _A_in["shift_k"],
        "A_ssm_k": _A_in["ssm_k"],
        "A_ssm_D": _A_in["ssm_D"],
        "B_x": _B_in["x"],
        "B_w1": _B_in["w1"],
        "B_w2": _B_in["w2"],
    }

def reference(A_q, A_k, A_v, A_shift_k, A_ssm_k, A_ssm_D, B_x, B_w1, B_w2):
    def _A_ref(q, k, v, shift_k, ssm_k, ssm_D):
        B, S, D = q.shape
        ks = shift_k.shape[-1]
        k_pad = _pad(k, ((0, 0), (ks - 1, 0), (0, 0)))
        k_shift = torch.zeros_like(k)
        for t in range(ks):
            k_shift = k_shift + k_pad[:, t:t + S, :] * shift_k[:, ks - 1 - t][None, None, :]
        kv = k_shift * v
        L = 2 * S
        kv_pad = _pad(kv, ((0, 0), (0, S), (0, 0)))
        kf_pad = _pad(ssm_k, ((0, 0), (0, S)))
        kv_f = (lambda x, axes=(-2,-1): torch.fft.rfft2(x.float(), dim=axes))(kv_pad[:, None, :, :], axes=(1, 2))
        kf_f = (lambda x, axes=(-2,-1): torch.fft.rfft2(x.float(), dim=axes))(kf_pad[:, None, :], axes=(1, 2))
        kf_f = (lambda x, *d: x.permute(*d))(kf_f, 1, 2, 0)
        y_f = kv_f * kf_f[None, :, :, :]
        y = (lambda x, s, axes=(-2,-1): torch.fft.irfft2(x, s=s, dim=axes).to(torch.bfloat16))(y_f, (1, L), axes=(1, 2))[:, 0, :S, :]
        y = y + kv * ssm_D[None, None, :]
        return q * y

    def _B_ref(x, w1, w2):
        h = torch.einsum("bsd,df->bsf", x, w1)
        zero = torch.zeros_like(h)
        h = torch.maximum(h, zero)
        return torch.einsum("bsf,fd->bsd", h, w2)

    x = A_q
    x = x + _A_ref(_layernorm(x, axis=-1), A_k, A_v, A_shift_k, A_ssm_k, A_ssm_D)
    x = x + _B_ref(_layernorm(x, axis=-1), B_w1, B_w2)
    x = x + _A_ref(_layernorm(x, axis=-1), A_k, A_v, A_shift_k, A_ssm_k, A_ssm_D)
    x = x + _B_ref(_layernorm(x, axis=-1), B_w1, B_w2)
    return x
```

```cpp
#include <hip/hip_runtime.h>
#include <hip/hip_bf16.h>
#include <stdint.h>

typedef unsigned short u16;
constexpr int S = 2048, D = 448, F = 1792;

__device__ __forceinline__ float bf2f(u16 b) { return __uint_as_float(((unsigned)b) << 16); }
__device__ __forceinline__ u16 f2bf(float f) { __hip_bfloat16 h = __float2bfloat16(f); return *reinterpret_cast<u16*>(&h); }
__device__ __forceinline__ float rb(float f) { return bf2f(f2bf(f)); }

__global__ void k_kv(const u16* k, const u16* v, const u16* sk, u16* kvT) {
    int idx = blockIdx.x * blockDim.x + threadIdx.x;
    if (idx >= S * D) return;
    int s = idx / D, d = idx % D;
    float acc = 0.f;
    for (int t = 0; t < 4; ++t) {
        int ss = s + t - 3;
        float kk = ss >= 0 ? bf2f(k[ss * D + d]) : 0.f;
        acc = rb(acc + rb(kk * bf2f(sk[d * 4 + 3 - t])));
    }
    kvT[d * S + s] = f2bf(acc * bf2f(v[idx]));
}
__global__ void k_conv(const u16* kvT, const u16* hk, const u16* Dv, u16* y) {
    __shared__ float skv[S], sh[S];
    int d = blockIdx.x;
    for (int i = threadIdx.x; i < S; i += blockDim.x) { skv[i] = bf2f(kvT[d * S + i]); sh[i] = bf2f(hk[d * S + i]); }
    __syncthreads();
    for (int s = threadIdx.x; s < S; s += blockDim.x) {
        float acc = 0.f;
        for (int t = 0; t <= s; ++t) acc += skv[t] * sh[s - t];
        y[s * D + d] = f2bf(rb(acc) + rb(skv[s] * bf2f(Dv[d])));
    }
}
__device__ float wave_sum(float v) { for (int o = 32; o > 0; o >>= 1) v += __shfl_xor(v, o); return v; }
__global__ void k_lng(const u16* x, const u16* y, u16* xo, u16* xn) {
    int row = blockIdx.x; int lane = threadIdx.x;
    float xv[7];
    float s = 0.f;
    for (int i = 0; i < 7; ++i) { xv[i] = bf2f(x[row * D + lane + 64 * i]); s += xv[i]; }
    float m = wave_sum(s) / D;
    float q = 0.f;
    for (int i = 0; i < 7; ++i) { float t = xv[i] - m; q += t * t; }
    float var = wave_sum(q) / D;
    float mb = rb(m), sd = rb(sqrtf(rb(rb(var) + 1e-5f)));
    s = 0.f;
    for (int i = 0; i < 7; ++i) {
        float ln = rb(rb(xv[i] - mb) / sd);
        float a = rb(ln * bf2f(y[row * D + lane + 64 * i]));
        xv[i] = rb(xv[i] + a);
        xo[row * D + lane + 64 * i] = f2bf(xv[i]);
        s += xv[i];
    }
    m = wave_sum(s) / D;
    q = 0.f;
    for (int i = 0; i < 7; ++i) { float t = xv[i] - m; q += t * t; }
    var = wave_sum(q) / D;
    mb = rb(m); sd = rb(sqrtf(rb(rb(var) + 1e-5f)));
    for (int i = 0; i < 7; ++i) xn[row * D + lane + 64 * i] = f2bf(rb(xv[i] - mb) / sd);
}
__global__ void k_gemm(const u16* A, const u16* B, u16* C, const u16* res, int M, int N, int K, int mode) {
    __shared__ float sA[16][17], sB[16][17];
    int tx = threadIdx.x & 15, ty = threadIdx.x >> 4;
    int m0 = blockIdx.y * 16, n0 = blockIdx.x * 16;
    float acc = 0.f;
    for (int k0 = 0; k0 < K; k0 += 16) {
        sA[ty][tx] = bf2f(A[(m0 + ty) * K + k0 + tx]);
        sB[ty][tx] = bf2f(B[(k0 + ty) * N + n0 + tx]);
        __syncthreads();
        for (int k = 0; k < 16; ++k) acc += sA[ty][k] * sB[k][tx];
        __syncthreads();
    }
    int o = (m0 + ty) * N + n0 + tx;
    if (mode == 0) C[o] = f2bf(fmaxf(rb(acc), 0.f));
    else C[o] = f2bf(bf2f(res[o]) + rb(acc));
}

extern "C" void kernel_launch(void* const* d_in, const int* in_sizes, int n_in,
                              void* d_out, int out_size, void* d_ws, size_t ws_size,
                              hipStream_t stream) {
    const u16* q = (const u16*)d_in[0]; const u16* k = (const u16*)d_in[1]; const u16* v = (const u16*)d_in[2];
    const u16* sk = (const u16*)d_in[3]; const u16* hk = (const u16*)d_in[4]; const u16* Dv = (const u16*)d_in[5];
    const u16* w1 = (const u16*)d_in[7]; const u16* w2 = (const u16*)d_in[8];
    u16* ws = (u16*)d_ws;
    u16* kvT = ws; u16* y = kvT + S * D; u16* x1 = y + S * D; u16* xn = x1 + S * D; u16* x2 = xn + S * D; u16* x3 = x2 + S * D; u16* h = x3 + S * D;
    u16* out = (u16*)d_out;
    k_kv<<<(S * D + 255) / 256, 256, 0, stream>>>(k, v, sk, kvT);
    k_conv<<<D, 256, 0, stream>>>(kvT, hk, Dv, y);
    k_lng<<<S, 64, 0, stream>>>(q, y, x1, xn);
    k_gemm<<<dim3(F / 16, S / 16), 256, 0, stream>>>(xn, w1, h, nullptr, S, F, D, 0);
    k_gemm<<<dim3(D / 16, S / 16), 256, 0, stream>>>(h, w2, x2, x1, S, D, F, 1);
    k_lng<<<S, 64, 0, stream>>>(x2, y, x3, xn);
    k_gemm<<<dim3(F / 16, S / 16), 256, 0, stream>>>(xn, w1, h, nullptr, S, F, D, 0);
    k_gemm<<<dim3(D / 16, S / 16), 256, 0, stream>>>(h, w2, out, x3, S, D, F, 1);
}
```

```cpp
#include <hip/hip_runtime.h>
#include <stdint.h>

typedef unsigned short u16;
typedef unsigned u32;
typedef u32 v4u __attribute__((ext_vector_type(4)));
typedef u32 v2u __attribute__((ext_vector_type(2)));
typedef float f32x2 __attribute__((ext_vector_type(2)));
typedef float f32x4 __attribute__((ext_vector_type(4)));
typedef float f32x16 __attribute__((ext_vector_type(16)));
typedef __bf16 bf16x8 __attribute__((ext_vector_type(8)));
typedef __bf16 bf16x2 __attribute__((ext_vector_type(2)));
typedef short s16x4 __attribute__((ext_vector_type(4)));
typedef short s16x8 __attribute__((ext_vector_type(8)));
#define LAS __attribute__((address_space(3)))
#define GAS __attribute__((address_space(1)))
#define DI __device__ __forceinline__
#define RLX_AGENT __ATOMIC_RELAXED, __HIP_MEMORY_SCOPE_AGENT

constexpr int S = 2048, D = 448, F = 1792;
constexpr float EPS = 1e-5f;
constexpr u32 OFF_FLAGS = 0;
constexpr u32 OFF_ST    = 16384;
constexpr u32 CLR_BYTES = OFF_ST + 64 * 7 * 512;
constexpr u32 OFF_CS    = 262144;
constexpr u32 OFF_ST0   = OFF_CS + 8192;
constexpr u32 OFF_KVR   = OFF_ST0 + 16384;
constexpr u32 TSZ       = S * D * 2;
constexpr u32 OFF_X0T   = OFF_KVR + TSZ;
constexpr u32 OFF_X1    = OFF_X0T + TSZ;
constexpr u32 OFF_YT    = OFF_X1 + TSZ;
constexpr u32 OFF_X3    = OFF_YT + TSZ;
constexpr u32 OFF_H     = OFF_X3 + TSZ;
constexpr u32 WS_BYTES  = OFF_H + 2 * 64 * 112 * 1024;
constexpr int RSRC_W3 = 0x00020000;
constexpr int AUX_SC1 = 16;

DI float bflo(u32 x) { return __uint_as_float(x << 16); }
DI float bfhi(u32 x) { return __uint_as_float(x & 0xffff0000u); }
DI u32 pk(float a, float b) { f32x2 v = {a, b}; return __builtin_bit_cast(u32, __builtin_convertvector(v, bf16x2)); }
DI float rbf(float a) { return bflo(pk(a, a) & 0xffffu); }
DI __amdgpu_buffer_rsrc_t mkrs(const void* p, u32 bytes) { return __builtin_amdgcn_make_buffer_rsrc((void*)p, 0, (int)bytes, RSRC_W3); }
DI f32x16 mfma32(v4u a, v4u b, f32x16 c) { return __builtin_amdgcn_mfma_f32_32x32x16_bf16(__builtin_bit_cast(bf16x8, a), __builtin_bit_cast(bf16x8, b), c, 0, 0, 0); }
DI f32x4 mfma16(v4u a, v4u b, f32x4 c) { return __builtin_amdgcn_mfma_f32_16x16x32_bf16(__builtin_bit_cast(bf16x8, a), __builtin_bit_cast(bf16x8, b), c, 0, 0, 0); }
DI v2u trd(LAS char* p) { return __builtin_bit_cast(v2u, __builtin_amdgcn_ds_read_tr16_b64_v4i16((LAS s16x4*)p)); }
DI v4u trd2(LAS char* p, int second) { v2u a = trd(p), b = trd(p + second); return v4u{a.x, a.y, b.x, b.y}; }
DI float dot2(u32 a, u32 b, float c) { return __builtin_amdgcn_fdot2_f32_bf16(__builtin_bit_cast(bf16x2, a), __builtin_bit_cast(bf16x2, b), c, false); }
DI void ln_scalars(float s1, float s2, float& mb, float& r) {
    const float m = s1 * (1.0f / D);
    float var = fmaxf(s2 * (1.0f / D) - m * m, 0.f);
    mb = rbf(m);
    r = 1.0f / rbf(sqrtf(rbf(rbf(var) + EPS)));
}

__global__ void __launch_bounds__(256) k0_prep(const u16* __restrict__ x0, const u16* __restrict__ k, const u16* __restrict__ v,
                                               const u16* __restrict__ sk, const u16* __restrict__ w1, unsigned char* __restrict__ ws) {
    __shared__ __attribute__((aligned(16))) u32 sm[4096];
    const int b = blockIdx.x, tid = threadIdx.x;
    if (b < 224) {
        const int g = b % 7, rb = b / 7, rp = tid >> 3, c8 = tid & 7;
        const int row = rb * 64 + 2 * rp, ch0 = g * 64 + c8 * 8;
        const __amdgpu_buffer_rsrc_t rk = mkrs(k, TSZ);
        v4u kr[5];
#pragma unroll
        for (int i = 0; i < 5; ++i) kr[i] = __builtin_amdgcn_raw_buffer_load_b128(rk, ((row - 3 + i) * D + ch0) * 2, 0, 0);
        const v4u v0 = *(const v4u*)(v + row * D + ch0), v1 = *(const v4u*)(v + (row + 1) * D + ch0);
        const v4u a0 = *(const v4u*)(x0 + row * D + ch0), a1 = *(const v4u*)(x0 + (row + 1) * D + ch0);
        v4u wq[4];
#pragma unroll
        for (int i = 0; i < 4; ++i) wq[i] = *(const v4u*)(sk + ch0 * 4 + i * 8);
#pragma unroll
        for (int e = 0; e < 8; ++e) {
            const u32 wa = wq[e >> 1][(e & 1) * 2], wb = wq[e >> 1][(e & 1) * 2 + 1];
            const float w0 = bflo(wa), w1_ = bfhi(wa), w2_ = bflo(wb), w3 = bfhi(wb);
            float kk[5];
#pragma unroll
            for (int i = 0; i < 5; ++i) kk[i] = (e & 1) ? bfhi(kr[i][e >> 1]) : bflo(kr[i][e >> 1]);
            const float s0 = kk[0] * w3 + kk[1] * w2_ + kk[2] * w1_ + kk[3] * w0;
            const float s1 = kk[1] * w3 + kk[2] * w2_ + kk[3] * w1_ + kk[4] * w0;
            const float va = (e & 1) ? bfhi(v0[e >> 1]) : bflo(v0[e >> 1]);
            const float vb = (e & 1) ? bfhi(v1[e >> 1]) : bflo(v1[e >> 1]);
            const int ch = c8 * 8 + e;
            const int dk = 31 - rp;
            sm[ch * 32 + (((dk >> 2) ^ c8) << 2) + (dk & 3)] = pk(rbf(s1) * vb, rbf(s0) * va);
            const u32 xa = (e & 1) ? (a0[e >> 1] >> 16) : (a0[e >> 1] & 0xffffu);
            const u32 xb = (e & 1) ? (a1[e >> 1] & 0xffff0000u) : (a1[e >> 1] << 16);
            sm[2048 + ch * 32 + (((rp >> 2) ^ c8) << 2) + (rp & 3)] = xa | xb;
        }
        __syncthreads();
#pragma unroll
        for (int i = 0; i < 2; ++i) {
            const int p = tid + 256 * i, ch = p >> 3, p8 = p & 7;
            const v4u kvv = *(const v4u*)&sm[ch * 32 + ((p8 ^ (ch >> 3)) << 2)];
            const v4u xv = *(const v4u*)&sm[2048 + ch * 32 + ((p8 ^ (ch >> 3)) << 2)];
            *(v4u*)(ws + OFF_KVR + ((g * 64 + ch) * S + (1984 - 64 * rb) + p8 * 8) * 2) = kvv;
            *(v4u*)(ws + OFF_X0T + ((g * 64 + ch) * S + 64 * rb + p8 * 8) * 2) = xv;
        }
    } else {
        const int q = b - 224;
        {
            const int row = q * 64 + (tid >> 2), part = tid & 3;
            v4u xr[14];
#pragma unroll
            for (int i = 0; i < 14; ++i) xr[i] = *(const v4u*)(x0 + row * D + (i * 4 + part) * 8);
            float s = 0.f;
#pragma unroll
            for (int i = 0; i < 14; ++i)
#pragma unroll
                for (int e = 0; e < 4; ++e) s += bflo(xr[i][e]) + bfhi(xr[i][e]);
            s += __shfl_xor(s, 1); s += __shfl_xor(s, 2);
            const float m = s * (1.0f / D);
            float q2 = 0.f;
#pragma unroll
            for (int i = 0; i < 14; ++i)
#pragma unroll
                for (int e = 0; e < 4; ++e) { float a = bflo(xr[i][e]) - m, c = bfhi(xr[i][e]) - m; q2 += a * a + c * c; }
            q2 += __shfl_xor(q2, 1); q2 += __shfl_xor(q2, 2);
            const float var = q2 * (1.0f / D);
            if (part == 0) *(f32x2*)(ws + OFF_ST0 + row * 8) = f32x2{rbf(m), 1.0f / rbf(sqrtf(rbf(rbf(var) + EPS)))};
        }
        {
            const int rg = tid >> 3, c8 = tid & 7;
            float cs[8] = {0, 0, 0, 0, 0, 0, 0, 0};
            if (c8 < 7) {
#pragma unroll
                for (int i = 0; i < 14; ++i) {
                    const v4u w = *(const v4u*)(w1 + (rg * 14 + i) * F + q * 56 + c8 * 8);
#pragma unroll
                    for (int e = 0; e < 4; ++e) { cs[2 * e] += bflo(w[e]); cs[2 * e + 1] += bfhi(w[e]); }
                }
            }
            float* sf = (float*)sm;
#pragma unroll
            for (int e = 0; e < 8; ++e) sf[rg * 65 + c8 * 8 + e] = cs[e];
            __syncthreads();
            if (tid < 56) {
                float s = 0.f;
                for (int i = 0; i < 32; ++i) s += sf[i * 65 + tid];
                *(float*)(ws + OFF_CS + (q * 56 + tid) * 4) = s;
            }
        }
        for (u32 o = (q * 256 + tid) * 16; o < CLR_BYTES; o += 32 * 256 * 16) *(v4u*)(ws + o) = v4u{0, 0, 0, 0};
    }
}

constexpr int CV_HE = 0;
constexpr int CV_HO = 4160 + 64;
constexpr int CV_KV = CV_HO + 4160 + 64;
constexpr int CV_CH = CV_KV + 4608;
constexpr int CV_EX = 2 * CV_CH;
constexpr int CV_LDS = CV_EX + 16384;

__global__ void __launch_bounds__(256, 1) k1_conv(const u16* __restrict__ hk, const u16* __restrict__ Dv, unsigned char* __restrict__ ws) {
    __shared__ __attribute__((aligned(16))) char smem[CV_LDS];
    LAS char* L = (LAS char*)smem;
    const int b = blockIdx.x, tid = threadIdx.x, lane = tid & 63;
    const int w = __builtin_amdgcn_readfirstlane(tid >> 6);
    const int cp = (b & 7) * 28 + (b >> 3);
    const __amdgpu_buffer_rsrc_t rh = mkrs(hk, TSZ);
#pragma unroll
    for (int c = 0; c < 2; ++c) {
        const int ch = 2 * cp + c;
        const v4u e = __builtin_amdgcn_raw_buffer_load_b128(rh, (ch * S + tid * 8) * 2, 0, 0);
        const u32 nx = __builtin_amdgcn_raw_buffer_load_b32(rh, (ch * S + tid * 8 + 8) * 2, 0, 0);
        const v4u kv = *(const v4u*)(ws + OFF_KVR + (ch * S + tid * 8) * 2);
        v4u o;
        o.x = __builtin_amdgcn_alignbit(e.y, e.x, 16); o.y = __builtin_amdgcn_alignbit(e.z, e.y, 16);
        o.z = __builtin_amdgcn_alignbit(e.w, e.z, 16); o.w = __builtin_amdgcn_alignbit(nx, e.w, 16);
        *(LAS v4u*)(L + c * CV_CH + CV_HE + 32 + tid * 16) = e;
        *(LAS v4u*)(L + c * CV_CH + CV_HO + 32 + tid * 16) = o;
        *(LAS v4u*)(L + c * CV_CH + CV_KV + tid * 16) = kv;
        if (tid == 0) {
            *(LAS v4u*)(L + c * CV_CH + CV_HE) = v4u{0, 0, 0, 0}; *(LAS v4u*)(L + c * CV_CH + CV_HE + 16) = v4u{0, 0, 0, 0};
            *(LAS v4u*)(L + c * CV_CH + CV_HO) = v4u{0, 0, 0, 0}; *(LAS v4u*)(L + c * CV_CH + CV_HO + 16) = v4u{0, 0, 0, e.x << 16};
        }
        if (tid < 32) *(LAS v4u*)(L + c * CV_CH + CV_KV + 4096 + tid * 16) = v4u{0, 0, 0, 0};
    }
    const int c = w >> 1, p = w & 1, ch = 2 * cp + c;
    const int i16 = lane & 15, q = lane >> 4;
    v2u x0v[4]; f32x4 st0[4][2];
#pragma unroll
    for (int t = 0; t < 4; ++t) {
        const int s = 256 * (4 * p + t) + 16 * i16 + 4 * q;
        x0v[t] = *(const v2u*)(ws + OFF_X0T + (ch * S + s) * 2);
        st0[t][0] = *(const f32x4*)(ws + OFF_ST0 + s * 8); st0[t][1] = *(const f32x4*)(ws + OFF_ST0 + s * 8 + 16);
    }
    const float Dch = bflo(Dv[ch]);
    __syncthreads();
    const int o0 = i16 + 1 + 32 * p + 8 * q;
    LAS char* ab = L + c * CV_CH + ((o0 & 1) ? CV_HO : CV_HE) + (o0 >> 1) * 4;
    v4u A[32];
#pragma unroll
    for (int t = 0; t < 32; ++t) {
        LAS u32* pp = (LAS u32*)(ab + t * 128);
        A[t] = v4u{pp[0], pp[1], pp[2], pp[3]};
    }
    LAS char* bb = L + c * CV_CH + CV_KV + (2032 - 16 * i16 + 32 * p + 8 * q) * 2;
    f32x4 acc[8];
#pragma unroll
    for (int i = 0; i < 8; ++i) acc[i] = f32x4{0, 0, 0, 0};
#pragma unroll
    for (int om = -3; om <= 28; ++om) {
        const v4u B = *(LAS v4u*)(bb - om * 128);
#pragma unroll
        for (int cb = 0; cb < 8; ++cb)
            if (4 * cb - om >= 0) acc[cb] = mfma16(A[4 * cb - om], B, acc[cb]);
    }
    LAS char* ex = L + CV_EX;
    f32x4 fin[4];
    if (p == 0) {
#pragma unroll
        for (int t = 0; t < 4; ++t) { *(LAS f32x4*)(ex + w * 4096 + t * 1024 + lane * 16) = acc[4 + t]; fin[t] = acc[t]; }
    } else {
#pragma unroll
        for (int t = 0; t < 4; ++t) { *(LAS f32x4*)(ex + w * 4096 + t * 1024 + lane * 16) = acc[t]; fin[t] = acc[4 + t]; }
    }
    __syncthreads();
#pragma unroll
    for (int t = 0; t < 4; ++t) {
        fin[t] += *(LAS f32x4*)(ex + (w ^ 1) * 4096 + t * 1024 + lane * 16);
        const int s = 256 * (4 * p + t) + 16 * i16 + 4 * q;
        const v2u kr = *(LAS v2u*)(L + c * CV_CH + CV_KV + (2044 - s) * 2);
        const float kvs[4] = {bfhi(kr.y), bflo(kr.y), bfhi(kr.x), bflo(kr.x)};
        const float xs[4] = {bflo(x0v[t].x), bfhi(x0v[t].x), bflo(x0v[t].y), bfhi(x0v[t].y)};
        const float mm[4] = {st0[t][0][0], st0[t][0][2], st0[t][1][0], st0[t][1][2]};
        const float rr[4] = {st0[t][0][1], st0[t][0][3], st0[t][1][1], st0[t][1][3]};
        float y[4], x1[4];
#pragma unroll
        for (int e = 0; e < 4; ++e) {
            y[e] = rbf(fin[t][e] + kvs[e] * Dch);
            x1[e] = xs[e] + (xs[e] - mm[e]) * rr[e] * y[e];
        }
        const int hg = s >> 5, off = hg * 28672 + ch * 64 + (s & 31) * 2;
        *(v2u*)(ws + OFF_YT + off) = v2u{pk(y[0], y[1]), pk(y[2], y[3])};
        *(v2u*)(ws + OFF_X1 + off) = v2u{pk(x1[0], x1[1]), pk(x1[2], x1[3])};
    }
}

constexpr int L_X = 0;
constexpr int L_W = 57344;
constexpr int L_M = L_W + 98304;
constexpr int L_Y = L_M;
constexpr int L_SP = L_M;
constexpr int L_S2 = L_M + 4096;
constexpr int L_TOT = L_M + 8192;
constexpr int L_CS = L_M + 4096 + 2048 + 1024;
constexpr unsigned SPIN_CAP = 1u << 16;

DI void dma16(u32 ldsaddr, u32 voff, __amdgpu_buffer_rsrc_t rs, u32 soff) {
    asm volatile("s_mov_b32 m0, %0\n\ts_nop 0\n\tbuffer_load_dwordx4 %1, %2, %3 offen lds" :: "s"(ldsaddr), "v"(voff), "s"(rs), "s"(soff) : "memory", "m0");
}
#define WAIT_VM(n) asm volatile("s_waitcnt vmcnt(%0)" :: "i"(n) : "memory")
#define WAIT_LGKM0() asm volatile("s_waitcnt lgkmcnt(0)" ::: "memory")
DI void bar() { asm volatile("s_waitcnt lgkmcnt(0)\n\ts_barrier" ::: "memory"); }

DI void wait_flags(GAS u32* f, int lane, GAS u32* tmo) {
    for (unsigned spins = 0;; ++spins) {
        u32 x = 1u;
        if (lane < 7) x = __hip_atomic_load(f + lane, RLX_AGENT);
        if (__all(x == 1u)) break;
        if (spins > SPIN_CAP) { if (lane == 0) __hip_atomic_store(tmo, 1u, RLX_AGENT); break; }
        __builtin_amdgcn_s_sleep(1);
    }
    __builtin_amdgcn_fence(__ATOMIC_ACQUIRE, "wavefront");
}

struct Ctx {
    LAS char* L;
    __amdgpu_buffer_rsrc_t rws;
    GAS u32* flags;
    int lane, w, j, grp;
    u32 trl;
    int kb0;
    int rot2;
};

template <bool TR, int HALF, int MLP>
DI void g1(const Ctx& c, const v4u (&W1)[2][28]) {
    f32x16 acc[2];
#pragma unroll
    for (int i = 0; i < 16; ++i) { acc[0][i] = 0.f; acc[1][i] = 0.f; }
    float s1 = 0.f, s2 = 0.f;
    const u32 lp = TR ? c.trl : (u32)c.lane * 16u;
#pragma unroll
    for (int sg = 0; sg < 4; ++sg) {
        LAS char* xb = c.L + L_X + HALF * 28672 + ((c.kb0 + sg) & 3) * 7168 + lp;
#pragma unroll
        for (int r = 0; r < 7; ++r) {
            const int i = sg * 7 + r;
            const v4u B = TR ? trd2(xb + r * 1024, 512) : *(LAS v4u*)(xb + r * 1024);
            acc[0] = mfma32(W1[0][i], B, acc[0]);
            acc[1] = mfma32(W1[1][i], B, acc[1]);
            if (sg == 0) {
#pragma unroll
                for (int e = 0; e < 4; ++e) { s1 = dot2(B[e], 0x3f803f80u, s1); s2 = dot2(B[e], B[e], s2); }
            }
        }
        if (sg == 0) *(LAS f32x2*)(c.L + L_SP + HALF * 2048 + (c.lane & 31) * 64 + (c.w * 2 + (c.lane >> 5)) * 8) = f32x2{s1, s2};
    }
    bar();
    float t1 = 0.f, t2 = 0.f;
#pragma unroll
    for (int i = 0; i < 4; ++i) {
        const f32x4 v = *(LAS f32x4*)(c.L + L_SP + HALF * 2048 + (c.lane & 31) * 64 + i * 16);
        t1 += v[0] + v[2]; t2 += v[1] + v[3];
    }
    float mb, r;
    ln_scalars(t1, t2, mb, r);
    const int hg = c.grp * 2 + HALF;
    const u32 hoff = OFF_H + ((MLP * 64 + hg) * 112 + 16 * c.j + 4 * c.w) * 1024;
#pragma unroll
    for (int ft = 0; ft < 2; ++ft) {
#pragma unroll
        for (int s = 0; s < 2; ++s) {
            v4u o;
#pragma unroll
            for (int g = 0; g < 2; ++g) {
                const f32x4 cs = *(LAS f32x4*)(c.L + L_CS + (c.w * 64 + ft * 32 + (2 * s + g) * 8 + (c.lane >> 5) * 4) * 4);
                float h[4];
#pragma unroll
                for (int e = 0; e < 4; ++e) h[e] = fmaxf((acc[ft][8 * s + 4 * g + e] - mb * cs[e]) * r, 0.f);
                o[2 * g] = pk(h[0], h[1]); o[2 * g + 1] = pk(h[2], h[3]);
            }
            __builtin_amdgcn_raw_buffer_store_b128(o, c.rws, c.lane * 16, hoff + (2 * ft + s) * 1024, AUX_SC1);
        }
    }
    WAIT_VM(0);
    bar();
    if (c.w == 0 && c.lane == 0) __hip_atomic_store(c.flags + (MLP * 64 + hg) * 8 + c.j, 1u, RLX_AGENT);
}

constexpr int HR = 14;
template <int HALF, int MLP>
DI void g2(const Ctx& c, const v4u (&W2)[2][16], u32 w2l, float (&o)[8]) {
    const int hg = c.grp * 2 + HALF;
    wait_flags(c.flags + (MLP * 64 + hg) * 8, c.lane, c.flags + 2048);
    const u32 hoff = OFF_H + ((MLP * 64 + hg) * 112 + 28 * c.w) * 1024;
    v4u hb[28];
    auto ld = [&](int i) { int ks = i + c.rot2; ks = ks >= 28 ? ks - 28 : ks; return __builtin_amdgcn_raw_buffer_load_b128(c.rws, c.lane * 16, hoff + ks * 1024, AUX_SC1); };
#pragma unroll
    for (int i = 0; i < HR; ++i) hb[i] = ld(i);
    __builtin_amdgcn_sched_barrier(0);
    f32x16 acc[2];
#pragma unroll
    for (int i = 0; i < 16; ++i) { acc[0][i] = 0.f; acc[1][i] = 0.f; }
#pragma unroll
    for (int i = 0; i < 28; ++i) {
        v4u a0, a1;
        if (i < 16) { a0 = W2[0][i]; a1 = W2[1][i]; }
        else {
            LAS char* p = c.L + L_W + ((28 + i) % 12) * 2048;
            a0 = trd2(p + w2l, 1024); a1 = trd2(p + (w2l ^ 64u), 1024);
        }
        acc[0] = mfma32(a0, hb[i], acc[0]);
        acc[1] = mfma32(a1, hb[i], acc[1]);
        if (i + HR < 28) hb[i + HR] = ld(i + HR);
        __builtin_amdgcn_sched_barrier(0);
    }
    LAS char* red = c.L + L_X + HALF * 28672;
#pragma unroll
    for (int pidx = 1; pidx < 4; ++pidx) {
        LAS char* dst = red + ((pidx ^ c.w) * 3 + pidx - 1) * 2048 + c.lane * 16;
        const f32x16& a = acc[pidx >> 1];
        const int r0 = (pidx & 1) * 8;
        *(LAS f32x4*)dst = f32x4{a[r0], a[r0 + 1], a[r0 + 2], a[r0 + 3]};
        *(LAS f32x4*)(dst + 1024) = f32x4{a[r0 + 4], a[r0 + 5], a[r0 + 6], a[r0 + 7]};
    }
    bar();
#pragma unroll
    for (int e = 0; e < 8; ++e) o[e] = acc[0][e];
#pragma unroll
    for (int sl = 0; sl < 3; ++sl) {
        LAS char* src = red + (c.w * 3 + sl) * 2048 + c.lane * 16;
        const f32x4 u = *(LAS f32x4*)src, v = *(LAS f32x4*)(src + 1024);
#pragma unroll
        for (int e = 0; e < 4; ++e) { o[e] += u[e]; o[4 + e] += v[e]; }
    }
}

__global__ void __launch_bounds__(256, 1) k2_mlp(const u16* __restrict__ w1, const u16* __restrict__ w2, unsigned char* __restrict__ ws, u16* __restrict__ out) {
    extern __shared__ __attribute__((aligned(16))) char smem[];
    Ctx c;
    c.L = (LAS char*)smem;
    const int b = blockIdx.x, tid = threadIdx.x;
    c.lane = tid & 63; c.w = __builtin_amdgcn_readfirstlane(tid >> 6);
    { const int xcd = b & 7, idx = b >> 3; c.grp = xcd * 4 + idx / 7; c.j = idx % 7; }
    const int gi = c.grp & 3;
    c.rws = mkrs(ws, WS_BYTES);
    c.flags = (GAS u32*)ws;
    const int lane = c.lane, w = c.w, j = c.j;
    const int hh = lane >> 5, tb = (lane >> 4) & 1, qq = (lane >> 2) & 3, pp = lane & 3;
    c.trl = (4 * hh + qq) * 64 + tb * 32 + pp * 8;
    c.kb0 = (w + gi) & 3;
    c.rot2 = (4 * j + 7 * gi) % 28;
    const u32 lbase = (u32)(uintptr_t)c.L;

#pragma unroll
    for (int i = 0; i < 14; ++i) dma16(lbase + L_X + (w * 14 + i) * 1024, lane * 16, c.rws, OFF_X1 + c.grp * 57344 + (w * 14 + i) * 1024);
#pragma unroll
    for (int i = 0; i < 2; ++i) dma16(lbase + L_Y + (w * 2 + i) * 1024, lane * 16, c.rws, OFF_YT + (c.grp * 2 + (w >> 1)) * 28672 + j * 4096 + ((w & 1) * 2 + i) * 1024);
    const float csv = *(const float*)(ws + OFF_CS + (256 * j + tid) * 4);

    const __amdgpu_buffer_rsrc_t rw1 = mkrs(w1, D * F * 2), rw2 = mkrs(w2, D * F * 2);
    const u32 swz = (u32)(((lane & 7) ^ (((lane >> 4) & 1) << 2)) * 16);
    const u32 vo1 = (lane >> 3) * (F * 2) + swz, vo2 = (lane >> 3) * (D * 2) + swz;
    const u32 wring = lbase + L_W + w * 24576;
    auto issue = [&](int n) {
        const u32 dst = wring + (n % 12) * 2048;
        if (n < 28) {
            const int ks = ((c.kb0 + n / 7) & 3) * 7 + n % 7;
            const u32 so = ks * 16 * (F * 2) + (256 * j + 64 * w) * 2;
            dma16(dst, vo1, rw1, so); dma16(dst + 1024, vo1, rw1, so + 8 * (F * 2));
        } else {
            int ks = n - 28 + c.rot2; ks = ks >= 28 ? ks - 28 : ks;
            const u32 so = (28 * w + ks) * 16 * (D * 2) + 128 * j;
            dma16(dst, vo2, rw2, so); dma16(dst + 1024, vo2, rw2, so + 8 * (D * 2));
        }
    };
    const int rr = 4 * hh + qq;
    const u32 w1l = w * 24576 + rr * 128 + (((rr >> 1) & 1) * 64) + tb * 32 + pp * 8;
    const u32 w2l = w * 24576 + rr * 128 + ((((rr >> 1) & 1) ^ (w >> 1)) * 64) + (tb ^ (w & 1)) * 32 + pp * 8;
#pragma unroll
    for (int n = 0; n < 12; ++n) issue(n);
    v4u W1[2][28], W2[2][16];
#pragma unroll
    for (int n = 0; n < 44; ++n) {
        WAIT_VM(22);
        LAS char* p = c.L + L_W + (n % 12) * 2048;
        if (n < 28) { W1[0][n] = trd2(p + w1l, 1024); W1[1][n] = trd2(p + (w1l ^ 64u), 1024); }
        else { W2[0][n - 28] = trd2(p + w2l, 1024); W2[1][n - 28] = trd2(p + (w2l ^ 64u), 1024); }
        WAIT_LGKM0();
        __builtin_amdgcn_sched_barrier(0);
        issue(n + 12);
    }
    WAIT_VM(0);
    bar();
    v4u xres[2], yf[2];
#pragma unroll
    for (int h = 0; h < 2; ++h) {
        xres[h] = trd2(c.L + L_X + h * 28672 + (4 * j + w) * 1024 + c.trl, 512);
        yf[h] = trd2(c.L + L_Y + h * 4096 + w * 1024 + c.trl, 512);
    }
    bar();
    *(LAS float*)(c.L + L_CS + tid * 4) = csv;
    bar();

    g1<true, 0, 0>(c, W1);
    g1<true, 1, 0>(c, W1);

    float x2[2][8];
    auto post1 = [&](auto HC, float (&o)[8]) {
        constexpr int H = decltype(HC)::value;
        float s1 = 0.f, s2 = 0.f;
#pragma unroll
        for (int e = 0; e < 8; ++e) {
            const float xr = (e & 1) ? bfhi(xres[H][e >> 1]) : bflo(xres[H][e >> 1]);
            const float t = rbf(xr + rbf(o[e]));
            x2[H][e] = t; s1 += t; s2 += t * t;
        }
        *(LAS f32x2*)(c.L + L_S2 + H * 1024 * 0 + (lane & 31) * 64 + (w * 2 + hh) * 8) = f32x2{s1, s2};
        bar();
        if (w == 0) {
            float t1 = 0.f, t2 = 0.f;
#pragma unroll
            for (int i = 0; i < 4; ++i) {
                const f32x4 v = *(LAS f32x4*)(c.L + L_S2 + (lane & 31) * 64 + i * 16);
                t1 += v[0] + v[2]; t2 += v[1] + v[3];
            }
            if (lane < 32)
                __builtin_amdgcn_raw_buffer_store_b128(v4u{__float_as_uint(t1), 1u, __float_as_uint(t2), 1u}, c.rws, lane * 16,
                                                       OFF_ST + ((c.grp * 2 + H) * 7 + j) * 512, AUX_SC1);
        }
    };
    auto post2 = [&](auto HC) {
        constexpr int H = decltype(HC)::value;
        const int hg = c.grp * 2 + H;
        float t1, t2;
        for (unsigned spins = 0;; ++spins) {
            asm volatile("" ::: "memory");
            v4u a[4];
#pragma unroll
            for (int i = 0; i < 4; ++i) {
                const int m = hh * 4 + i;
                a[i] = __builtin_amdgcn_raw_buffer_load_b128(c.rws, (m < 7 ? m : 6) * 512 + (lane & 31) * 16, OFF_ST + hg * 3584, AUX_SC1);
            }
            bool ok = true; t1 = 0.f; t2 = 0.f;
#pragma unroll
            for (int i = 0; i < 4; ++i) {
                ok &= (a[i].y == 1u) & (a[i].w == 1u);
                const bool use = hh * 4 + i < 7;
                t1 += use ? __uint_as_float(a[i].x) : 0.f; t2 += use ? __uint_as_float(a[i].z) : 0.f;
            }
            if (__all(ok)) break;
            if (spins > SPIN_CAP) { if (lane == 0) __hip_atomic_store(c.flags + 2048, 2u, RLX_AGENT); break; }
            __builtin_amdgcn_s_sleep(1);
        }
        t1 += __shfl_xor(t1, 32); t2 += __shfl_xor(t2, 32);
        float mb, r;
        ln_scalars(t1, t2, mb, r);
        float x3[8];
#pragma unroll
        for (int e = 0; e < 8; ++e) {
            const float y = (e & 1) ? bfhi(yf[H][e >> 1]) : bflo(yf[H][e >> 1]);
            x3[e] = x2[H][e] + (x2[H][e] - mb) * r * y;
        }
        xres[H] = v4u{pk(x3[0], x3[1]), pk(x3[2], x3[3]), pk(x3[4], x3[5]), pk(x3[6], x3[7])};
        __builtin_amdgcn_raw_buffer_store_b128(xres[H], c.rws, lane * 16, OFF_X3 + (hg * 28 + 4 * j + w) * 1024, AUX_SC1);
        WAIT_VM(0);
        bar();
        if (w == 0 && lane == 0) __hip_atomic_store(c.flags + 1024 + hg * 8 + j, 1u, RLX_AGENT);
    };
    auto getx3 = [&](auto HC) {
        constexpr int H = decltype(HC)::value;
        const int hg = c.grp * 2 + H;
        wait_flags(c.flags + 1024 + hg * 8, lane, c.flags + 2048);
        v4u t[7];
#pragma unroll
        for (int i = 0; i < 7; ++i) t[i] = __builtin_amdgcn_raw_buffer_load_b128(c.rws, lane * 16, OFF_X3 + (hg * 28 + w * 7 + i) * 1024, AUX_SC1);
#pragma unroll
        for (int i = 0; i < 7; ++i) *(LAS v4u*)(c.L + L_X + H * 28672 + (w * 7 + i) * 1024 + lane * 16) = t[i];
        bar();
    };
    auto fin = [&](auto HC, float (&o)[8]) {
        constexpr int H = decltype(HC)::value;
        float x4[8];
#pragma unroll
        for (int e = 0; e < 8; ++e) {
            const float xr = (e & 1) ? bfhi(xres[H][e >> 1]) : bflo(xres[H][e >> 1]);
            x4[e] = xr + rbf(o[e]);
        }
        u16* po = out + (c.grp * 64 + H * 32 + (lane & 31)) * D + 64 * j + 16 * w + 4 * hh;
        *(v2u*)po = v2u{pk(x4[0], x4[1]), pk(x4[2], x4[3])};
        *(v2u*)(po + 8) = v2u{pk(x4[4], x4[5]), pk(x4[6], x4[7])};
    };
    using H0 = std::integral_constant<int, 0>; using H1 = std::integral_constant<int, 1>;
    float o[8];
    g2<0, 0>(c, W2, w2l, o); post1(H0{}, o);
    g2<1, 0>(c, W2, w2l, o); post1(H1{}, o);
    post2(H0{});
    post2(H1{});
    getx3(H0{}); g1<false, 0, 1>(c, W1);
    getx3(H1{}); g1<false, 1, 1>(c, W1);
    g2<0, 1>(c, W2, w2l, o); fin(H0{}, o);
    g2<1, 1>(c, W2, w2l, o); fin(H1{}, o);
}

extern "C" void kernel_launch(void* const* d_in, const int* in_sizes, int n_in,
                              void* d_out, int out_size, void* d_ws, size_t ws_size,
                              hipStream_t stream) {
    const u16* x0 = (const u16*)d_in[0]; const u16* k = (const u16*)d_in[1]; const u16* v = (const u16*)d_in[2];
    const u16* sk = (const u16*)d_in[3]; const u16* hk = (const u16*)d_in[4]; const u16* Dv = (const u16*)d_in[5];
    const u16* w1 = (const u16*)d_in[7]; const u16* w2 = (const u16*)d_in[8];
    unsigned char* ws = (unsigned char*)d_ws;
    (void)hipFuncSetAttribute((const void*)k2_mlp, hipFuncAttributeMaxDynamicSharedMemorySize, L_TOT);
    k0_prep<<<256, 256, 0, stream>>>(x0, k, v, sk, w1, ws);
    k1_conv<<<224, 256, 0, stream>>>(hk, Dv, ws);
    k2_mlp<<<224, 256, L_TOT, stream>>>(w1, w2, ws, (u16*)d_out);
}
```

```cpp
#include <hip/hip_runtime.h>
#include <stdint.h>
#include <type_traits>

typedef unsigned short u16;
typedef unsigned u32;
typedef u32 v4u __attribute__((ext_vector_type(4)));
typedef u32 v2u __attribute__((ext_vector_type(2)));
typedef float f32x2 __attribute__((ext_vector_type(2)));
typedef float f32x4 __attribute__((ext_vector_type(4)));
typedef float f32x16 __attribute__((ext_vector_type(16)));
typedef __bf16 bf16x8 __attribute__((ext_vector_type(8)));
typedef __bf16 bf16x2 __attribute__((ext_vector_type(2)));
typedef short s16x4 __attribute__((ext_vector_type(4)));
typedef short s16x2 __attribute__((ext_vector_type(2)));
typedef unsigned short u16x2 __attribute__((ext_vector_type(2)));
#define LAS __attribute__((address_space(3)))
#define GAS __attribute__((address_space(1)))
#define DI __device__ __forceinline__
#define RLX_AGENT __ATOMIC_RELAXED, __HIP_MEMORY_SCOPE_AGENT
#define SB() __builtin_amdgcn_sched_barrier(0)
#define NOUNROLL _Pragma("unroll 1")
#define MEMBAR() asm volatile("" ::: "memory")
template <class T> __device__ __forceinline__ void LV(T& x) { asm volatile("" : "+v"(x)); }
template <class T> __device__ __forceinline__ void LA(T& x) { asm volatile("" : "+a"(x)); }
#define ANY(x) (__builtin_amdgcn_ballot_w64(x) != 0ull)

constexpr int S = 2048, D = 448, F = 1792;
constexpr float EPS = 1e-5f;
constexpr u32 TSZ = S * D * 2;
constexpr u32 OFF_EP  = 0;
constexpr u32 OFF_FL  = 4096;
constexpr u32 OFF_ST0 = OFF_FL + 16384;
constexpr u32 OFF_CSG = OFF_ST0 + 16384;
constexpr u32 OFF_ST  = OFF_CSG + 16384;
constexpr u32 OFF_KVG = 1048576;
constexpr u32 OFF_X0G = OFF_KVG + 2 * TSZ;
constexpr u32 OFF_X1  = OFF_X0G + 2 * TSZ;
constexpr u32 OFF_YT  = OFF_X1 + TSZ;
constexpr u32 OFF_X3  = OFF_YT + TSZ;
constexpr u32 OFF_H   = OFF_X3 + TSZ;
constexpr u32 WS_BYTES = OFF_H + 64 * 112 * 1024;
static_assert(OFF_ST + 64 * 24 * 512 <= OFF_KVG, "ws map");
#define STAMP(i) do {} while (0)
#define STAMP2(i) do {} while (0)
#define STAMP3(i) do {} while (0)
constexpr int RSRC_W3 = 0x00020000;
constexpr int AUX_SC1 = 16;
constexpr unsigned SPIN_CAP = 1u << 14;
constexpr u32 SGN = 0x80008000u;

template <int I, int N, class Fn> DI void sfor(Fn&& fn) { if constexpr (I < N) { fn(std::integral_constant<int, I>{}); sfor<I + 1, N>(fn); } }

DI float bflo(u32 x) { return __uint_as_float(x << 16); }
DI float bfhi(u32 x) { return __uint_as_float(x & 0xffff0000u); }
DI u32 pk(float a, float b) { f32x2 v = {a, b}; return __builtin_bit_cast(u32, __builtin_convertvector(v, bf16x2)); }
DI float rbf(float a) { return bflo(pk(a, a)); }
DI __amdgpu_buffer_rsrc_t mkrs(const void* p, u32 bytes) { return __builtin_amdgcn_make_buffer_rsrc((void*)p, 0, (int)bytes, RSRC_W3); }
DI f32x4 mfma16(v4u a, v4u b, f32x4 c) { return __builtin_amdgcn_mfma_f32_16x16x32_bf16(__builtin_bit_cast(bf16x8, a), __builtin_bit_cast(bf16x8, b), c, 0, 0, 0); }
DI f32x16 mfma32(v4u a, v4u b, f32x16 c) { return __builtin_amdgcn_mfma_f32_32x32x16_bf16(__builtin_bit_cast(bf16x8, a), __builtin_bit_cast(bf16x8, b), c, 0, 0, 0); }
DI f32x16 zero16() { f32x16 z; for (int i = 0; i < 16; ++i) z[i] = 0.f; return z; }
DI v2u trd(LAS char* p) { return __builtin_bit_cast(v2u, __builtin_amdgcn_ds_read_tr16_b64_v4i16((LAS s16x4*)p)); }
DI v4u trd2(LAS char* p, int second) { v2u a = trd(p), b = trd(p + second); return v4u{a.x, a.y, b.x, b.y}; }
DI float dot2(u32 a, u32 b, float c) { return __builtin_amdgcn_fdot2_f32_bf16(__builtin_bit_cast(bf16x2, a), __builtin_bit_cast(bf16x2, b), c, false); }
DI v4u ld128(__amdgpu_buffer_rsrc_t rs, u32 vo, u32 so) { return __builtin_amdgcn_raw_buffer_load_b128(rs, vo, so, AUX_SC1); }
template <bool SC1, int OFF = 0> DI v4u ld128_asm(__amdgpu_buffer_rsrc_t rs, u32 vo, u32 so) {
    v4u r;
    if constexpr (SC1) asm volatile("buffer_load_dwordx4 %0, %1, %2, %3 offen offset:%4 sc1" : "=v"(r) : "v"(vo), "s"(rs), "s"(so), "i"(OFF) : "memory");
    else asm volatile("buffer_load_dwordx4 %0, %1, %2, %3 offen offset:%4" : "=v"(r) : "v"(vo), "s"(rs), "s"(so), "i"(OFF) : "memory");
    return r;
}
template <int OFF = 0> DI void st128(v4u v, __amdgpu_buffer_rsrc_t rs, u32 vo, u32 so) {
    asm volatile("buffer_store_dwordx4 %0, %1, %2, %3 offen offset:%4 sc1\n\ts_nop 1" :: "v"(v), "v"(vo), "s"(rs), "s"(so), "i"(OFF) : "memory");
}
DI int lane_id() { int l; asm volatile("v_mbcnt_lo_u32_b32 %0, -1, 0\n\tv_mbcnt_hi_u32_b32 %0, -1, %0" : "=v"(l)); return l; }
DI float hsum(float a) {
    const u32 v = __float_as_uint(a);
    auto t = __builtin_amdgcn_permlane32_swap(v, v, false, false);
    return __uint_as_float(t[0]) + __uint_as_float(t[1]);
}
DI float ln_sd(float var) { return rbf(__builtin_amdgcn_sqrtf(rbf(rbf(var) + EPS))); }
DI void ln_scalars(float s1, float s2, float& m, float& r) {
    m = s1 * (1.0f / D);
    r = __builtin_amdgcn_rsqf(fmaxf(__builtin_fmaf(-m, m, s2 * (1.0f / D)), 0.f) + EPS);
}
DI u32 xor_or(u32 a, u32 b, u32 c) { return __builtin_amdgcn_bitop3_b32(a, b, c, 0xBE); }
DI u32 tag_bad(const v4u& g, u32 tA, u32 tB, u32 acc) { return xor_or(g.w, tB, xor_or(g.y, tA, acc)); }
template <bool SC1> DI void dma16(u32 ldsaddr, u32 voff, __amdgpu_buffer_rsrc_t rs, u32 soff) {
    if constexpr (SC1) asm volatile("s_mov_b32 m0, %0\n\ts_nop 0\n\tbuffer_load_dwordx4 %1, %2, %3 offen sc1 lds" :: "s"(ldsaddr), "v"(voff), "s"(rs), "s"(soff) : "memory", "m0");
    else asm volatile("s_mov_b32 m0, %0\n\ts_nop 0\n\tbuffer_load_dwordx4 %1, %2, %3 offen lds" :: "s"(ldsaddr), "v"(voff), "s"(rs), "s"(soff) : "memory", "m0");
}
template <int LI> DI void dma_pair(u32 ldsbase, u32 vo0, u32 vo1, __amdgpu_buffer_rsrc_t rs, u32 soff) {
    asm volatile("s_add_u32 m0, %0, %5\n\ts_nop 0\n\tbuffer_load_dwordx4 %1, %3, %4 offen lds\n\tbuffer_load_dwordx4 %2, %3, %4 offen offset:1024 lds"
                 :: "s"(ldsbase), "v"(vo0), "v"(vo1), "s"(rs), "s"(soff), "i"(LI) : "memory", "m0", "scc");
}
DI void touch(u32 junk, u32 voff, __amdgpu_buffer_rsrc_t rs, u32 soff) {
    asm volatile("s_mov_b32 m0, %0\n\ts_nop 0\n\tbuffer_load_dword %1, %2, %3 offen lds" :: "s"(junk), "v"(voff), "s"(rs), "s"(soff) : "memory", "m0");
}
#define WAIT_VM(n) asm volatile("s_waitcnt vmcnt(%0)" :: "i"(n) : "memory")
#define WAIT_LGKM(n) asm volatile("s_waitcnt lgkmcnt(%0)" :: "i"(n) : "memory")
DI void bar() { asm volatile("s_waitcnt lgkmcnt(0)\n\ts_barrier" ::: "memory"); }
DI int wrap7(int x) { return x >= 7 ? x - 7 : x; }
template <int N, int K> DI void ilv() { if constexpr (N > 0) { __builtin_amdgcn_sched_group_barrier(0x8, 1, 0); __builtin_amdgcn_sched_group_barrier(0x366, K, 0); ilv<N - 1, K>(); } }

constexpr int L_X = 0;
constexpr int L_W = 57344;
constexpr int L_M = L_W + 98304;
constexpr int L_SP = L_M;
constexpr int L_CS = L_M + 2048;
constexpr int L_S2 = L_M + 3072;
constexpr int L_JK = L_M + 4096;
constexpr int L_TOT = L_M + 8192;
constexpr int T_K = 0, T_V = 9216, T_X = 17408, T_S = L_JK;
constexpr int CV_B = 25600;
constexpr int CV_HE = 0, CV_HO = 4160, CV_KV = CV_HO + 4160, CV_CH = CV_KV + 4608;
constexpr int CV_ST = 16384;
static_assert(CV_B + 2 * CV_CH <= L_W, "lds map");

struct Ctx {
    LAS char* L;
    __amdgpu_buffer_rsrc_t rws, rw1, rw2;
    GAS u32* tmo;
    int w, j, grp, kb0, gi;
    u32 lbase, tagA, tagB;
};
DI Ctx fresh(const Ctx& c0) { Ctx c = c0; asm volatile("" : "+s"(c.w), "+s"(c.j), "+s"(c.grp), "+s"(c.kb0), "+s"(c.gi)); return c; }
DI bool spin_fail(const Ctx& c, unsigned& spins, u32 code) {
    if (++spins > SPIN_CAP) { __hip_atomic_store(c.tmo, code, RLX_AGENT); return true; }
    __builtin_amdgcn_s_sleep(1);
    return false;
}
DI u32 xtr_of(int lane) { return (u32)(((lane >> 5) * 4 + ((lane >> 2) & 3)) * 64 + ((lane >> 4) & 1) * 32 + (lane & 3) * 8); }
DI u32 wl1_of(const Ctx& c, int lane) {
    const int rr = (lane >> 5) * 4 + ((lane >> 2) & 3);
    return (u32)(L_W + c.w * 24576 + rr * 128 + ((rr >> 1) & 1) * 64 + ((lane >> 4) & 1) * 32 + (lane & 3) * 8);
}
DI u32 wl2_of(const Ctx& c, int lane) { return wl1_of(c, lane) ^ (u32)((c.w >> 1) * 64 + (c.w & 1) * 32); }
DI int member(const Ctx& c, int m) {
    if (m == 0) return c.j;
    int t = m - 1 + 2 * c.gi; t = t >= 6 ? t - 6 : t; t = t >= 6 ? t - 6 : t;
    return wrap7(c.j + 1 + t);
}
typedef v4u W1T[28][2];
typedef v4u W2T[16][2];
typedef f32x16 AccT[2];

DI constexpr int k_mem(int f) { return f < 4 ? 0 : 1 + (f - 4) % 6; }
DI constexpr int k_sub(int f) { return f < 4 ? f : (f - 4) / 6; }
DI constexpr int w2_step_of(int n) { return n < 40 ? 2 * (n - 28) : (n < 44 ? n - 16 : 2 * (n - 44) + 1); }
template <int N> DI u32 w_soff(const Ctx& c) {
    if constexpr (N < 28) return (N % 7 + 7 * ((N / 7 + c.kb0) & 3)) * 16 * (F * 2) + (256 * c.j + 64 * c.w) * 2;
    else return (256 * member(c, k_mem(w2_step_of(N))) + 64 * c.w + 16 * k_sub(w2_step_of(N))) * (D * 2) + 128 * c.j;
}
template <int N> DI u32 w_voff(int lane) { return (lane >> 3) * ((N < 28 ? F : D) * 2) + ((lane & 7) ^ (((lane >> 4) & 1) << 2)) * 16; }
struct WSt { u32 ring, vo0, vo1; };
template <int N> DI WSt w_st(const Ctx& c, int lane) { WSt t; t.ring = c.lbase + L_W + c.w * 24576; t.vo0 = w_voff<N>(lane); t.vo1 = t.vo0 + 8 * ((N < 28 ? F : D) * 2) - 1024; return t; }
template <int N> DI void w_dma(const Ctx& c, const WSt& t, u32 so) { dma_pair<(N % 12) * 2048>(t.ring, t.vo0, t.vo1, N < 28 ? c.rw1 : c.rw2, so); }
DI v4u w_frag(const Ctx& c, u32 wl, int slot, int t) { return trd2(c.L + (wl ^ (u32)(t * 64)) + slot * 2048, 1024); }
template <int N> DI void w_read1(const Ctx& c, u32 wl, W1T& W1, W2T& W2) {
#pragma unroll
    for (int t = 0; t < 2; ++t) {
        if constexpr (N < 28) W1[N][t] = w_frag(c, wl, N % 12, t);
        else W2[N - 28][t] = w_frag(c, wl, N % 12, t);
    }
}
template <int N> DI void w_neg(W2T& W2) {
#pragma unroll
    for (int t = 0; t < 2; ++t) { LV(W2[N - 28][t]); W2[N - 28][t].z ^= SGN; W2[N - 28][t].w ^= SGN; asm volatile("" : "+v"(W2[N - 28][t])); }
}
template <int N0, int N1> DI void w_read(const Ctx& c0, W1T& W1, W2T& W2) {
    const Ctx c = fresh(c0);
    const u32 wl = wl1_of(c, lane_id());
    sfor<N0, N1>([&](auto NC) { w_read1<decltype(NC)::value>(c, wl, W1, W2); });
}

constexpr int PS0 = 0, PS1 = 33;
template <int N0, int N1> DI void pin_a(W1T& W1) {
    if constexpr (N0 < N1) { asm volatile("" : "+a"(W1[N0][0]), "+a"(W1[N0][1])); pin_a<N0 + 1, N1>(W1); }
}
struct G1Out { AccT acc; float mb, r; };
DI constexpr bool reg_start(unsigned long long end, int f) { return f == 0 || ((end >> (f - 1)) & 1ull); }
DI constexpr int reg_len(unsigned long long end, int f) { int n = 1; while (!reg_start(end, f - n + 1)) ++n; return n; }
struct Ad {
    u32 l16;
    u32 ca;
    u32 hst;
    u32 x3o;
    u32 sto;
    u32 so[7];
};
constexpr u32 ST_HG = 3 * 4096;
DI u32 tok32(const Ad& a) { return a.l16 & 496u; }

template <bool TR, int HALF, int S0, unsigned END, class Side>
DI void g1_main(const Ctx& c, Ad& a, const W1T& W1, AccT& acc, Side&& side) {
    const u32 lp = TR ? xtr_of(a.l16 >> 4) : a.l16;
    int kb = c.kb0; asm volatile("" : "+s"(kb));
    u32 la = 0;
    auto ldb = [&](int f) {
        if (f % 7 == 0) la = lp + ((f / 7 + kb) & 3) * 7168;
        LAS char* p = c.L + la + (L_X + HALF * 28672 + (f % 7) * 1024);
        if constexpr (TR) return trd2(p, 512);
        else return *(LAS v4u*)p;
    };
    v4u B[28];
    B[0] = ldb(0); B[1] = ldb(1); B[2] = ldb(2);
    SB();
    sfor<0, 28>([&](auto FC) {
        constexpr int f = decltype(FC)::value;
        if constexpr (f % 4 == 0) STAMP2(S0 + f / 4);
        if constexpr (reg_start(END, f)) asm volatile("" : "+v"(a.l16), "+v"(a.ca));
        acc[0] = mfma32(W1[f][0], B[f], f == 0 ? zero16() : acc[0]);
        acc[1] = mfma32(W1[f][1], B[f], f == 0 ? zero16() : acc[1]);
        if constexpr (f + 3 < 28) B[f + 3] = ldb(f + 3);
        side(FC);
        if constexpr ((END >> f) & 1u) { ilv<2 * reg_len(END, f), 5>(); SB(); }
    });
}
struct St1 { u32 ad; v4u fr[2]; float s1, s2; };
template <int HALF, int K> DI void st1_read(const Ctx& c, St1& s) { s.fr[K & 1] = trd2(c.L + s.ad + (L_X + HALF * 28672 + K * 1024), 512); }
template <int K, int E0, int E1> DI void st1_acc(St1& s) {
#pragma unroll
    for (int e = E0; e < E1; ++e) { if (e == E0) LV(s.fr[K & 1]); const u32 x = s.fr[K & 1][e >> 1]; if (e & 1) s.s2 = dot2(x, x, s.s2); else s.s1 = dot2(x, 0x3f803f80u, s.s1); }
}
DI void part_put(const Ctx& c, const Ad& a, int off, float s1, float s2) { *(LAS f32x2*)(c.L + off + tok32(a) * 2 + c.w * 8) = f32x2{hsum(s1), hsum(s2)}; }
DI f32x2 part_get(const Ctx& c, const Ad& a, int off) {
    const f32x4 u = *(LAS f32x4*)(c.L + off + tok32(a) * 2), v = *(LAS f32x4*)(c.L + off + tok32(a) * 2 + 16);
    return f32x2{(u[0] + u[2]) + (v[0] + v[2]), (u[1] + u[3]) + (v[1] + v[3])};
}
DI void frag_sums(const v4u& p, float& s1, float& s2) {
    s1 = 0.f; s2 = 0.f;
#pragma unroll
    for (int e = 0; e < 4; ++e) { s1 = dot2(p[e], 0x3f803f80u, s1); s2 = dot2(p[e], p[e], s2); }
}
template <int HALF, int KIND> DI void stat_pub(const Ctx& c, const Ad& a, int off, int who) {
    if (c.w == who) {
        f32x2 t = part_get(c, a, off);
        const bool z = c.j == 6 && a.l16 >= 512u;
        st128(v4u{z ? 0u : __float_as_uint(t[0]), c.tagA, z ? 0u : __float_as_uint(t[1]), c.tagB}, c.rws, z ? a.l16 : tok32(a), a.sto + HALF * ST_HG + KIND * 4096 + c.j * 512);
    }
}
struct Gath { v4u g[4]; };
template <int HALF, int KIND, int I> DI void gath_ld(const Ctx& c, const Ad& a, Gath& g) {
    g.g[I] = ld128(c.rws, a.l16 + I * 1024, a.sto + HALF * ST_HG + KIND * 4096);
}
DI bool gath_bad(const Ctx& c, const Gath& g) {
    u32 d = 0;
#pragma unroll
    for (int i = 0; i < 4; ++i) d = tag_bad(g.g[i], c.tagA, c.tagB, d);
    return ANY(d != 0u);
}
template <int HALF, int KIND> DI void gath_wait(const Ctx& c, const Ad& a, Gath& g) {
    if (gath_bad(c, g)) {
        unsigned spins = 0;
        NOUNROLL do { MEMBAR(); sfor<0, 4>([&](auto IC) { gath_ld<HALF, KIND, decltype(IC)::value>(c, a, g); }); } while (gath_bad(c, g) && !spin_fail(c, spins, 2u));
    }
}
DI void gath_ln(const Gath& g, float& m, float& r) {
    const float t1 = (__uint_as_float(g.g[0].x) + __uint_as_float(g.g[1].x)) + (__uint_as_float(g.g[2].x) + __uint_as_float(g.g[3].x));
    const float t2 = (__uint_as_float(g.g[0].z) + __uint_as_float(g.g[1].z)) + (__uint_as_float(g.g[2].z) + __uint_as_float(g.g[3].z));
    ln_scalars(hsum(t1), hsum(t2), m, r);
}
DI f32x4 epi_cs(const Ctx& c, const Ad& a, int E) { return *(LAS f32x4*)(c.L + a.ca + E * 32); }
template <int HALF, int MLP, int E>
DI void g1_epi(const Ctx& c, const Ad& a, G1Out& o, f32x4& cs, v4u& fr) {
    constexpr int t = E >> 2, r0 = 4 * (E & 3), g = E & 1;
    constexpr bool neg = (g ^ MLP) != 0;
    float h[4];
    LV(cs); LA(o.acc[t]);
#pragma unroll
    for (int e = 0; e < 4; ++e) h[e] = __builtin_fmaf(-o.mb, cs[e], o.acc[t][r0 + e]);
#pragma unroll
    for (int e = 0; e < 2; ++e) {
        const u32 p = __builtin_bit_cast(u32, __builtin_elementwise_max(__builtin_bit_cast(s16x2, pk(h[2 * e], h[2 * e + 1])), s16x2{0, 0}));
        fr[2 * g + e] = neg ? (p | SGN) : p;
    }
    if (g == 0) LV(fr);
}
template <int HALF, int K> DI void h_store(const Ctx& c, const Ad& a, const v4u& fr) { st128<K * 1024>(fr, c.rws, a.l16, a.hst + HALF * 114688); }

#define NW0_ 0
#define NT0_ 0
constexpr int NT0 = NT0_;
#define NW1_ 2
constexpr int NW1 = NW1_;
#define KP_ 4
constexpr int KP = KP_;
#define TLBW_ 0
constexpr int TLBW = TLBW_;
constexpr int NW0 = NW0_;
#define EPE 8
constexpr int HR = 10, GL = 14, GC = 20;
template <int MLP> DI bool h_bad(const v4u& a, const v4u& b) {
    const u32 lo = MLP == 0 ? (a.x | a.y | b.x | b.y) : ~(a.x & a.y & b.x & b.y);
    const u32 hi = MLP == 0 ? ~(a.z & a.w & b.z & b.w) : (a.z | a.w | b.z | b.w);
    return ((lo | hi) & SGN) != 0u;
}
DI u32 h_base(const Ctx& c, int half) { return OFF_H + ((c.grp * 2 + half) * 112 + 4 * c.w) * 1024; }
template <bool ASM> DI v2u gate_ld(const Ctx& c, const Ad& a, int half) {
    const u32 vo = min(a.l16, 96u) * 1024, so = a.hst - c.j * 16384 + half * 114688 + 4;
    if constexpr (ASM) { v2u r; asm volatile("buffer_load_dwordx2 %0, %1, %2, %3 offen sc1" : "=v"(r) : "v"(vo), "s"(c.rws), "s"(so) : "memory"); return r; }
    else return __builtin_amdgcn_raw_buffer_load_b64(c.rws, vo, so, AUX_SC1);
}
template <int MLP> DI bool gate_bad(v2u x) { return ((MLP == 0 ? (x.x | ~x.y) : (~x.x | x.y)) & SGN) != 0u; }
template <int MLP> DI void gate_wait(const Ctx& c, const Ad& a, int half, v2u x) {
    if (ANY(gate_bad<MLP>(x))) {
        unsigned spins = 0;
        NOUNROLL do { MEMBAR(); x = gate_ld<false>(c, a, half); } while (ANY(gate_bad<MLP>(x)) && !spin_fail(c, spins, 1u));
    }
}
template <int N> DI v4u h_next(const Ctx& c, Ad& a) {
    constexpr int f = N >= 28 ? N - 28 : N;
    if constexpr (N >= 32 && N < 38) a.so[k_mem(f)] += 114688u;
    v4u r = __builtin_amdgcn_raw_buffer_load_b128(c.rws, a.l16 + k_sub(f) * 1024, a.so[k_mem(f)], AUX_SC1);
    if constexpr (N >= 50) a.so[k_mem(f)] -= 114688u;
    return r;
}
template <int MLP, int F_> DI void h_check(const Ctx& c0, v4u& a, v4u& b) {
    if (__builtin_expect(ANY(h_bad<MLP>(a, b)), 0)) {
        const Ctx c = fresh(c0);
        constexpr int half = F_ >= 28, f = F_ - 28 * half;
        const u32 vo = lane_id() * 16 + k_sub(f) * 1024, s0 = h_base(c, half) + member(c, k_mem(f)) * 16384, s1 = h_base(c, half) + member(c, k_mem(f + 1)) * 16384;
        unsigned spins = 0;
        NOUNROLL do {
            MEMBAR();
            a = ld128(c.rws, vo, s0); b = ld128(c.rws, vo, s1);
        } while (ANY(h_bad<MLP>(a, b)) && !spin_fail(c, spins, 3u));
    }
}
template <int HALF, int P> DI void red_write(const Ctx& c, const Ad& a, const AccT& acc) {
    LAS char* dst = c.L + (a.l16 + (P ^ c.w) * 6144) + (L_X + (1 - HALF) * 28672 + (P - 1) * 2048);
    const f32x16& x = acc[P >> 1];
    constexpr int r0 = (P & 1) * 8;
    *(LAS f32x4*)dst = f32x4{x[r0], x[r0 + 1], x[r0 + 2], x[r0 + 3]};
    *(LAS f32x4*)(dst + 1024) = f32x4{x[r0 + 4], x[r0 + 5], x[r0 + 6], x[r0 + 7]};
}
template <int HALF> DI void red_read(const Ctx& c, const Ad& a, f32x4 (&t)[6]) {
    LAS char* src = c.L + (a.l16 + c.w * 6144) + (L_X + (1 - HALF) * 28672);
#pragma unroll
    for (int i = 0; i < 6; ++i) t[i] = *(LAS f32x4*)(src + i * 1024);
}
template <int K> DI void post_half(const f32x4& o, float r, v4u& xres) {
#pragma unroll
    for (int e = 0; e < 2; ++e) xres[2 * K + e] = pk(__builtin_fmaf(o[2 * e], r, bflo(xres[2 * K + e])), __builtin_fmaf(o[2 * e + 1], r, bfhi(xres[2 * K + e])));
}
template <int HALF> DI void out_store(const Ctx& c, const Ad& a, const v4u& x, __amdgpu_buffer_rsrc_t rout) {
    auto r0 = __builtin_amdgcn_permlane32_swap(x.x, x.z, false, false);
    auto r1 = __builtin_amdgcn_permlane32_swap(x.y, x.w, false, false);
    const v4u v = {r0[0], r1[0], r0[1], r1[1]};
    const u32 vo = tok32(a) * (D / 8) + (a.l16 >> 9) * 16, so = (u32)((c.grp * 64 + HALF * 32) * D + 64 * c.j + 16 * c.w) * 2;
    asm volatile("buffer_store_dwordx4 %0, %1, %2, %3 offen\n\ts_nop 1" :: "v"(v), "v"(vo), "s"(rout), "s"(so) : "memory");
}
template <int HALF, int MLP> DI v4u xres_ld(const Ctx& c, const Ad& a) {
    if constexpr (MLP == 0) return trd2(c.L + (xtr_of(a.l16 >> 4) + (4 * c.j + c.w) * 1024) + (L_X + HALF * 28672), 512);
    else return *(LAS v4u*)(c.L + (a.l16 + (4 * c.j + c.w) * 1024) + (L_X + HALF * 28672));
}
template <int HALF> DI v4u y_ld(const Ctx& c, const Ad& a) { return trd2(c.L + (xtr_of(a.l16 >> 4) + c.w * 1024) + (L_X + HALF * 28672 + 24576), 512); }
DI void x3_make(v4u& x, const v4u& yf, float m, float r) {
    const float n = -m * r;
#pragma unroll
    for (int e = 0; e < 4; ++e) {
        const float p = bflo(x[e]), q = bfhi(x[e]);
        x[e] = pk(__builtin_fmaf(__builtin_fmaf(p, r, n), bflo(yf[e]), p), __builtin_fmaf(__builtin_fmaf(q, r, n), bfhi(yf[e]), q));
    }
}
template <int HALF> DI void x3_store(const Ctx& c, const Ad& a, const v4u& x) { st128(x, c.rws, a.l16, a.x3o + HALF * 28672); }
template <int HALF, int I> DI v4u x3_ld1(const Ctx& c, const Ad& a) {
    return ld128(c.rws, a.l16 + (I & 3) * 1024, a.x3o + (6 * c.w - 4 * c.j + (I & 4)) * 1024 + HALF * 28672);
}
DI bool x3_bad(const v4u (&t)[7]) {
    u32 mx = 0;
#pragma unroll
    for (int i = 0; i < 7; ++i) mx = max(max(mx, max(t[i].x, t[i].y)), max(t[i].z, t[i].w));
    return ANY(mx == ~0u);
}
template <int HALF> DI void x3_wait(const Ctx& c, const Ad& a, v4u (&t)[7]) {
    if (x3_bad(t)) {
        unsigned spins = 0;
        NOUNROLL do { MEMBAR(); sfor<0, 7>([&](auto IC) { t[decltype(IC)::value] = x3_ld1<HALF, decltype(IC)::value>(c, a); }); } while (x3_bad(t) && !spin_fail(c, spins, 4u));
    }
}
template <int HALF, int I> DI void x3_put1(const Ctx& c, const Ad& a, const v4u& t) { *(LAS v4u*)(c.L + (a.l16 + c.w * 7168) + (L_X + HALF * 28672 + I * 1024)) = t; }

template <int MLP>
DI void g2_pair(const Ctx& c, Ad& a, const W2T& W2, v4u (&hb)[56], G1Out& gb, f32x4 (&csq)[3], float r0, v4u (&xres)[2], __amdgpu_buffer_rsrc_t out) {
    const u32 wl0 = wl2_of(c, a.l16 >> 4), wl1 = wl0 ^ 64u;
    AccT acc[2];
    v2u gate = {0, 0};
    v4u wl[2], fr, yf;
    f32x4 o[2], u[2];
    float s1, s2;
    Gath gs;
    const float ra_ = MLP ? -r0 : r0, rb_ = MLP ? -gb.r : gb.r;
    auto wres = [&](int slot, int k) { return trd2(c.L + (k ? wl1 : wl0) + slot * 2048, 1024); };
    auto fetch = [&](auto NC) {
        constexpr int N = decltype(NC)::value;
        if constexpr (N < 56) {
            if constexpr (N >= 28 && N < 32) hb[N] = *(LAS v4u*)(c.L + (a.l16 + c.w * 4096) + (L_X + (N - 28) * 1024));
            else hb[N] = h_next<N>(c, a);
        }
    };
    auto rd = [&](int i) { return *(LAS f32x4*)(c.L + (a.l16 + c.w * 6144) + (L_X + 28672 + i * 1024)); };
    auto own = [&](int k) { return f32x4{acc[0][0][4 * k], acc[0][0][4 * k + 1], acc[0][0][4 * k + 2], acc[0][0][4 * k + 3]}; };
    wl[0] = wres(8, 0); wl[1] = wres(8, 1);
    SB();
    sfor<0, 28>([&](auto PC) {
        constexpr int F_ = 2 * decltype(PC)::value, H = F_ >= 28, f = F_ - 28 * H;
        constexpr bool res = f < 24;
        constexpr int r0_ = f < 24 ? f >> 1 : f - 12;
        if constexpr (F_ == 24 || F_ == 30 || (MLP == 0 && F_ == 38)) { bar(); SB(); }
        if constexpr (F_ % 4 == 0) STAMP2((MLP ? PS1 : PS0) + 15 + F_ / 4);
        asm volatile("" : "+v"(a.l16), "+v"(a.ca));
        acc[H][0] = mfma32(W2[r0_][0], hb[F_], f == 0 ? zero16() : acc[H][0]);
        acc[H][1] = mfma32(W2[r0_][1], hb[F_], f == 0 ? zero16() : acc[H][1]);
        fetch(std::integral_constant<int, F_ + (F_ < EPE ? HR - 2 : HR)>{});
        fetch(std::integral_constant<int, F_ + 1 + (F_ < EPE ? HR - 2 : HR)>{});
        if constexpr (F_ == EPE - 2) { fetch(std::integral_constant<int, F_ + HR>{}); fetch(std::integral_constant<int, F_ + HR + 1>{}); }
        if constexpr (EPE == 16 && F_ < 16) {
            constexpr int E = F_ / 2;
            g1_epi<1, MLP, E>(c, a, gb, csq[E % 3], fr);
            if constexpr (E & 1) { *(LAS v4u*)(c.L + (a.l16 + c.w * 4096) + (L_X + (E / 2) * 1024)) = fr; h_store<1, E / 2>(c, a, fr); }
            if constexpr (E < 6) csq[(E + 2) % 3] = epi_cs(c, a, E + 2);
        }
        if constexpr (EPE == 8 && F_ < 8) {
            g1_epi<1, MLP, F_>(c, a, gb, csq[F_ % 3], fr); g1_epi<1, MLP, F_ + 1>(c, a, gb, csq[(F_ + 1) % 3], fr);
            *(LAS v4u*)(c.L + (a.l16 + c.w * 4096) + (L_X + (F_ / 2) * 1024)) = fr; h_store<1, F_ / 2>(c, a, fr);
            if constexpr (F_ < 6) { csq[(F_ + 2) % 3] = epi_cs(c, a, F_ + 2); csq[F_ % 3] = epi_cs(c, a, F_ + 3); }
        }
        acc[H][0] = mfma32(res ? wl[0] : W2[r0_ + 1][0], hb[F_ + 1], acc[H][0]);
        acc[H][1] = mfma32((res || f == 26) ? wl[1] : W2[r0_ + 1][1], hb[F_ + 1], acc[H][1]);
        if constexpr (f < 22) { wl[0] = wres((9 + (f >> 1)) % 12, 0); wl[1] = wres((9 + (f >> 1)) % 12, 1); }
        if constexpr (f == 24) wl[1] = *(LAS v4u*)(c.L + (a.l16 + c.w * 1024) + L_JK);
        if constexpr (F_ == 26) { wl[0] = wres(8, 0); wl[1] = wres(8, 1); }
        if constexpr (MLP == 0 && (F_ == 24 || F_ == 26))
            dma16<true>(c.lbase + L_X + (F_ - 24) / 2 * 28672 + 24576 + c.w * 1024, a.l16, c.rws, OFF_YT + (c.grp * 2 + (F_ - 24) / 2) * 28672 + c.j * 4096 + c.w * 1024);
        if constexpr (F_ == GL) gate = gate_ld<false>(c, a, 1);
        if constexpr (F_ == 22) xres[1] = xres_ld<1, MLP>(c, a);
        if constexpr (F_ == 28) { red_write<0, 1>(c, a, acc[0]); red_write<0, 2>(c, a, acc[0]); red_write<0, 3>(c, a, acc[0]); }
        if constexpr (F_ == 30) { u[0] = rd(0); u[1] = rd(2); o[0] = rd(4); }
        if constexpr (F_ == 32) { LA(acc[0][0]); LV(o[0]); LV(u[0]); LV(u[1]); o[0] = (o[0] + own(0)) + (u[0] + u[1]); post_half<0>(o[0], ra_, xres[0]); LV(xres[0]); u[0] = rd(1); u[1] = rd(3); o[0] = rd(5); }
        if constexpr (F_ == 34) { LA(acc[0][0]); LV(o[0]); LV(u[0]); LV(u[1]); o[0] = (o[0] + own(1)) + (u[0] + u[1]); post_half<1>(o[0], ra_, xres[0]); LV(xres[0]); }
        if constexpr (MLP == 0 && F_ == 36) { frag_sums(xres[0], s1, s2); part_put(c, a, L_S2, s1, s2); }
        if constexpr (MLP == 1 && F_ == 36) out_store<0>(c, a, xres[0], out);
        if constexpr (MLP == 0 && F_ >= 52) { gath_ld<0, 0, F_ - 52>(c, a, gs); gath_ld<0, 0, F_ - 51>(c, a, gs); }
        if constexpr (MLP == 0 && F_ == 54) yf = y_ld<0>(c, a);
        ilv<4, 5>();
        if constexpr (F_ + 2 < 56 && f + 2 != 28 && f + 2 >= 4) h_check<MLP, F_ + 2>(c, hb[F_ + 2], hb[F_ + 3]);
        if constexpr (F_ == GC) gate_wait<MLP>(c, a, 1, gate);
        if constexpr (MLP == 0 && F_ == 38) stat_pub<0, 0>(c, a, L_S2, 0);
        SB();
    });
    static_assert(GC + 2 + HR <= 32 && (GC & 1) == 0 && (HR & 1) == 0, "half B's first foreign fragment is requested behind the gate");
    STAMP2((MLP ? PS1 : PS0) + 29);
    red_write<1, 1>(c, a, acc[1]); red_write<1, 2>(c, a, acc[1]); red_write<1, 3>(c, a, acc[1]);
    bar();
    f32x4 t[6];
    red_read<1>(c, a, t);
    o[0] = (f32x4{acc[1][0][0], acc[1][0][1], acc[1][0][2], acc[1][0][3]} + t[0]) + (t[2] + t[4]);
    o[1] = (f32x4{acc[1][0][4], acc[1][0][5], acc[1][0][6], acc[1][0][7]} + t[1]) + (t[3] + t[5]);
    post_half<0>(o[0], rb_, xres[1]); post_half<1>(o[1], rb_, xres[1]);
    if constexpr (MLP == 0) {
        frag_sums(xres[1], s1, s2); part_put(c, a, L_S2, s1, s2);
        bar();
        stat_pub<1, 0>(c, a, L_S2, 1);
        float m, r;
        gath_wait<0, 0>(c, a, gs);
        gath_ln(gs, m, r);
        x3_make(xres[0], yf, m, r);
        x3_store<0>(c, a, xres[0]);
        frag_sums(xres[0], s1, s2); part_put(c, a, L_SP, s1, s2);
        yf = y_ld<1>(c, a);
        bar();
        stat_pub<0, 1>(c, a, L_SP, 2);
        sfor<0, 4>([&](auto IC) { gath_ld<1, 0, decltype(IC)::value>(c, a, gs); });
        gath_wait<1, 0>(c, a, gs);
        gath_ln(gs, m, r);
        x3_make(xres[1], yf, m, r);
        x3_store<1>(c, a, xres[1]);
        frag_sums(xres[1], s1, s2); part_put(c, a, L_S2, s1, s2);
    } else out_store<1>(c, a, xres[1], out);
}

__global__ void __launch_bounds__(256, 1) fused_kernel(const u16* __restrict__ x0, const u16* __restrict__ kin, const u16* __restrict__ vin,
                                                       const u16* __restrict__ sk, const u16* __restrict__ hk, const u16* __restrict__ Dv,
                                                       const u16* __restrict__ w1, const u16* __restrict__ w2,
                                                       unsigned char* __restrict__ ws, u16* __restrict__ out) {
    extern __shared__ __attribute__((aligned(16))) char smem[];
    Ctx c;
    c.L = (LAS char*)smem;
    c.lbase = (u32)(uintptr_t)c.L;
    const int b = blockIdx.x, tid = threadIdx.x;
    c.w = __builtin_amdgcn_readfirstlane(tid >> 6);
    c.rws = mkrs(ws, WS_BYTES);
    c.tmo = (GAS u32*)(ws + 256);
    STAMP(0);
    STAMP3(0);

    if (b < 32) {
        const v2u ep = __builtin_amdgcn_raw_buffer_load_b64(c.rws, 0, OFF_EP, AUX_SC1);
        const int row = b * 64 + (tid >> 2), part = tid & 3;
        v4u xr[14];
#pragma unroll
        for (int i = 0; i < 14; ++i) xr[i] = *(const v4u*)(x0 + row * D + (i * 4 + part) * 8);
        MEMBAR();
        {
            unsigned long long pc; u32 back, len;
            asm volatile("s_getpc_b64 %0\n.Lfk_here:\n\ts_mov_b32 %1, .Lfk_here-.Lfk_begin\n\ts_mov_b32 %2, .Lfk_end-.Lfk_begin" : "=s"(pc), "=s"(back), "=s"(len));
            if (c.w < 2) touch(c.lbase + L_JK, (tid & 63) * 128, mkrs((const void*)(pc - back), len), ((b >> 3) * 2 + c.w) * 8192);
        }
        float s1 = 0.f, s2 = 0.f;
        const u32 piv = xr[0].x & 0xffffu;
        (void)piv;
#pragma unroll
        for (int i = 0; i < 14; ++i)
#pragma unroll
            for (int e = 0; e < 4; ++e) { s1 = dot2(xr[i][e], 0x3f803f80u, s1); s2 = dot2(xr[i][e], xr[i][e], s2); }
        s1 += __shfl_xor(s1, 1); s2 += __shfl_xor(s2, 1); s1 += __shfl_xor(s1, 2); s2 += __shfl_xor(s2, 2);
        const float m = s1 * (1.0f / D);
        const u32 mine = pk(m, ln_sd(fmaxf(__builtin_fmaf(-m, m, s2 * (1.0f / D)), 0.f)));
        const u32 other = __shfl_down(mine, 4);
        if ((tid & 7) == 0) st128(v4u{mine, ep.x + 1u, other, ep.y + 1u}, c.rws, (u32)(b * 32 + (tid >> 3)) * 16, OFF_ST0);
        const int rg = tid >> 3, c8 = min(tid & 7, 6);
        v4u wr[14];
#pragma unroll
        for (int i = 0; i < 14; ++i) wr[i] = *(const v4u*)(w1 + (rg * 14 + i) * F + b * 56 + c8 * 8);
        float cs[8] = {0, 0, 0, 0, 0, 0, 0, 0};
#pragma unroll
        for (int i = 0; i < 14; ++i)
#pragma unroll
            for (int e = 0; e < 4; ++e) { cs[2 * e] += bflo(wr[i][e]); cs[2 * e + 1] += bfhi(wr[i][e]); }
        LAS float* sf = (LAS float*)c.L;
#pragma unroll
        for (int e = 0; e < 8; ++e) sf[rg * 65 + (tid & 7) * 8 + e] = cs[e];
        bar();
        if (tid < 56) {
            float t = 0.f;
#pragma unroll
            for (int i = 0; i < 32; ++i) t += sf[i * 65 + tid];
            const float t2 = __shfl_down(t, 1);
            if ((tid & 1) == 0) st128(v4u{__float_as_uint(t), ep.x + 1u, __float_as_uint(t2), ep.y + 1u}, c.rws, (u32)(b * 28 + (tid >> 1)) * 16, OFF_CSG);
        }
        MEMBAR();
        {
            const __amdgpu_buffer_rsrc_t rw1 = mkrs(w1, D * F * 2), rw2 = mkrs(w2, D * F * 2);
#pragma unroll
            for (int i = 0; i < 6; ++i) touch(c.lbase + L_JK, (tid & 63) * 128, rw1, (u32)(112 + i * 16 + (b >> 3) * 4 + c.w) * 8192);
#pragma unroll
            for (int i = 0; i < 13; ++i) touch(c.lbase + L_JK, (tid & 63) * 128, rw2, (u32)(i * 16 + (b >> 3) * 4 + c.w) * 8192);
        }
        WAIT_VM(0);
        return;
    }
    asm volatile(".Lfk_begin:");
    const int wb = b - 32;
    { const int xcd = wb & 7, idx = wb >> 3; c.grp = xcd * 4 + idx / 7; c.j = idx % 7; }
    c.gi = c.grp & 3;
    const int w = c.w;
    c.rw1 = mkrs(w1, D * F * 2); c.rw2 = mkrs(w2, D * F * 2);
    c.kb0 = (w + c.gi + 2) & 3;
    const int cp = (wb & 7) * 28 + (wb >> 3);
    W1T W1; W2T W2;

    u32 dval;
    {
        const int g = wb % 7, rb = wb / 7, lane = tid & 63;
        const int rg = (lane & 7) * 2 + ((lane >> 4) & 1), cl = ((lane >> 3) & 1) + 2 * (lane >> 5), cq = 4 * w + cl;
        const __amdgpu_buffer_rsrc_t rk = mkrs(kin, TSZ), rv = mkrs(vin, TSZ), rx = mkrs(x0, TSZ), rh = mkrs(hk, TSZ), rs_ = mkrs(sk, D * 8), rd = mkrs(Dv, D * 2);
        STAMP3(1);
        const u32 tb = (u32)((rb * 64) * 896 + g * 128);
        const u32 tv0 = (u32)((lane >> 3) * 896) + (((lane & 7) ^ ((lane >> 5) + 2 * w)) & 7) * 16;
        dma16<false>(c.lbase + T_X + w * 1024, tv0, rx, tb + w * 7168);
        dma16<false>(c.lbase + T_X + (w + 4) * 1024, tv0, rx, tb + (w + 4) * 7168);
        dma16<false>(c.lbase + T_K + w * 1024, tv0 + tb + (w - 1) * 7168, rk, 0u);
        dma16<false>(c.lbase + T_K + (w + 4) * 1024, tv0, rk, tb + (w + 3) * 7168);
        dma16<false>(c.lbase + T_V + w * 1024, tv0, rv, tb + w * 7168);
        dma16<false>(c.lbase + T_V + (w + 4) * 1024, tv0, rv, tb + (w + 4) * 7168);
        if (w == 0) dma16<false>(c.lbase + T_K + 8192, tv0, rk, tb + 7 * 7168);
        else dma16<false>(c.lbase + T_S + (w - 1) * 1024, (u32)lane * 16, rs_, g * 512);
        v4u ep4 = ld128_asm<true>(c.rws, 0u, OFF_EP);
        v4u he[2];
        he[0] = ld128_asm<false>(rh, tid * 16, cp * 8192); he[1] = ld128_asm<false>(rh, tid * 16, cp * 8192 + 4096);
        v4u dv4 = ld128_asm<false>(rd, 0u, cp * 4);
        {
            const WSt wt = w_st<0>(c, lane);
            sfor<0, NW0>([&](auto NC) { w_dma<decltype(NC)::value>(c, wt, w_soff<decltype(NC)::value>(c)); });
        }
        if constexpr (TLBW) {
            if (w == 0) touch(c.lbase + L_JK + 3072, (u32)lane * 524288u, c.rws, 4096u);
            else touch(c.lbase + L_JK + 3072, (u32)lane * 262144u, w == 1 ? c.rw1 : w == 2 ? c.rw2 : mkrs(out, TSZ), 0u);
        }
        if constexpr (NT0) touch(c.lbase + L_JK + 3072, (u32)lane * 128, c.rw1, (u32)((wb >> 3) * 4 + w) * 8192);
        STAMP3(2);
        asm volatile("s_waitcnt vmcnt(%1)" : "+v"(ep4) : "i"(3 + NT0 + TLBW + 2 * NW0) : "memory");
        c.tagA = __builtin_amdgcn_readfirstlane(ep4.x) + 1u; c.tagB = __builtin_amdgcn_readfirstlane(ep4.y) + 1u;
        bar();
        STAMP(1); STAMP3(3);
        const u32 a1 = (u32)(rg * 512 + (((cq >> 1) ^ rg) & 7) * 16 + (cq & 1) * 8);
        const u32 ak1 = (u32)(rg * 512 + 1024 + (((cq >> 1) ^ (rg + 2)) & 7) * 16 + (cq & 1) * 8);
        const u32 ak0 = (u32)(rg * 512 + 512 + (((cq >> 1) ^ (rg + 1)) & 7) * 16 + (cq & 1) * 8);
        v2u kr[7], vr[4], xr[4];
#pragma unroll
        for (int i = 0; i < 4; ++i) xr[i] = *(LAS v2u*)(c.L + T_X + a1 + i * 128);
        const v4u w01 = *(LAS v4u*)(c.L + T_S + cq * 32), w23 = *(LAS v4u*)(c.L + T_S + cq * 32 + 16);
#pragma unroll
        for (int i = 0; i < 3; ++i) kr[i] = *(LAS v2u*)(c.L + T_K + ak0 + (i + 1) * 128);
#pragma unroll
        for (int i = 0; i < 4; ++i) { kr[3 + i] = *(LAS v2u*)(c.L + T_K + ak1 + i * 128); vr[i] = *(LAS v2u*)(c.L + T_V + a1 + i * 128); }
#pragma unroll
        for (int h = 0; h < 2; ++h) {
            auto r0 = __builtin_amdgcn_permlane16_swap(__builtin_amdgcn_perm(xr[1][h], xr[0][h], 0x05040100u), __builtin_amdgcn_perm(xr[1][h], xr[0][h], 0x07060302u), false, false);
            auto r1 = __builtin_amdgcn_permlane16_swap(__builtin_amdgcn_perm(xr[3][h], xr[2][h], 0x05040100u), __builtin_amdgcn_perm(xr[3][h], xr[2][h], 0x07060302u), false, false);
            st128(v4u{r0[0], r1[0], r0[1], r1[1]}, c.rws, (u32)((cl * 4 + ((lane >> 4) & 1)) * 4096 + (lane & 7) * 16), OFF_X0G + (g * 64 + 16 * w + 2 * h) * 4096 + rb * 128);
        }
        STAMP3(27);
        v4u kg[4];
#pragma unroll
        for (int e = 0; e < 4; ++e) {
            const v4u& wq = e < 2 ? w01 : w23;
            const float w0 = bflo(wq[(e & 1) * 2]), w1_ = bfhi(wq[(e & 1) * 2]), w2_ = bflo(wq[(e & 1) * 2 + 1]), w3 = bfhi(wq[(e & 1) * 2 + 1]);
            float kk[7], o[4];
#pragma unroll
            for (int i = 0; i < 7; ++i) kk[i] = (e & 1) ? bfhi(kr[i][e >> 1]) : bflo(kr[i][e >> 1]);
#pragma unroll
            for (int i = 0; i < 4; ++i) o[i] = (kk[i] * w3 + kk[i + 1] * w2_ + kk[i + 2] * w1_ + kk[i + 3] * w0) * ((e & 1) ? bfhi(vr[i][e >> 1]) : bflo(vr[i][e >> 1]));
            kg[e] = v4u{pk(o[3], o[2]), c.tagA, pk(o[1], o[0]), c.tagB};
        }
        STAMP3(28);
        asm volatile("s_waitcnt vmcnt(0)\n\ts_barrier" : "+v"(he[0]), "+v"(he[1]), "+v"(dv4), "+v"(kg[0]), "+v"(kg[1]), "+v"(kg[2]), "+v"(kg[3]) :: "memory");
        STAMP3(29);
#pragma unroll
        for (int e = 0; e < 4; ++e) st128(kg[e], c.rws, (u32)(cl * 4 * 8192 + (15 - rg) * 16), OFF_KVG + ((g * 64 + 16 * w + e) * 512 + 496 - 16 * rb) * 16);
        {
            const WSt wt = w_st<0>(c, lane);
            sfor<NW0, NW1>([&](auto NC) { w_dma<decltype(NC)::value>(c, wt, w_soff<decltype(NC)::value>(c)); });
        }
        STAMP(2); STAMP3(4);
        dval = (w >> 1) ? dv4.x >> 16 : dv4.x & 0xffffu;
#pragma unroll
        for (int cc = 0; cc < 2; ++cc) {
            LAS char* cb = c.L + CV_B + cc * CV_CH;
            *(LAS v4u*)(cb + CV_HE + 32 + tid * 16) = he[cc];
            if (tid < 2) { *(LAS v4u*)(cb + CV_HE + tid * 16) = v4u{0, 0, 0, 0}; *(LAS v4u*)(cb + CV_HO + tid * 16) = v4u{0, 0, 0, 0}; }
            if (tid < 32) *(LAS v4u*)(cb + CV_KV + 4096 + tid * 16) = v4u{0, 0, 0, 0};
        }
        bar();
#pragma unroll
        for (int cc = 0; cc < 2; ++cc) {
            const v4u e = he[cc];
            const u32 pv = *(LAS u32*)(c.L + CV_B + cc * CV_CH + CV_HE + 28 + tid * 16);
            *(LAS v4u*)(c.L + CV_B + cc * CV_CH + CV_HO + 32 + tid * 16) = v4u{__builtin_amdgcn_alignbit(e.x, pv, 16), __builtin_amdgcn_alignbit(e.y, e.x, 16),
                                                                            __builtin_amdgcn_alignbit(e.z, e.y, 16), __builtin_amdgcn_alignbit(e.w, e.z, 16)};
        }
        bar();
        STAMP3(5);
        SB();
    }
    {
        const int lane = lane_id(), i16 = lane & 15, q = lane >> 4, cc = w >> 1, p = w & 1, ch = 2 * cp + cc;
        LAS char* cb = c.L + CV_B + cc * CV_CH;
        const int o0 = i16 + 1 + 32 * p + 8 * q;
        LAS char* ab = cb + ((o0 & 1) ? CV_HO + 4 : CV_HE) + (o0 >> 1) * 4;
        v4u A[32];
        v4u AD;
        {
            const int jj = p ? -1 : 15 - i16 - 8 * q;
            const u32 dv = dval << ((jj & 1) * 16);
#pragma unroll
            for (int d = 0; d < 4; ++d) AD[d] = (jj >= 0 && (jj >> 1) == d) ? dv : 0u;
        }
        const Ctx cs_ = fresh(c);
        const u32 wl = wl1_of(cs_, lane);
        const WSt wt = w_st<0>(cs_, lane);
        v4u sg[4], g[4];
        SB();
        sfor<0, 8>([&](auto NC) {
            constexpr int n = decltype(NC)::value;
            if constexpr (n >= KP && n < KP + 4) { sg[n - KP] = ld128_asm<true>(c.rws, tid * 16, OFF_ST0 + (n - KP) * 4096); g[n - KP] = ld128_asm<true>(c.rws, tid * 16, OFF_KVG + cp * 16384 + (n - KP) * 4096); }
#pragma unroll
            for (int t = n * 4; t < n * 4 + 4; ++t) { LAS u32* pp = (LAS u32*)(ab + t * 128); A[t] = v4u{pp[0], pp[1], pp[2], pp[3]}; }
            SB();
        });
        STAMP(3); STAMP3(6);
        {
            asm volatile("s_waitcnt vmcnt(0)" : "+v"(sg[0]), "+v"(sg[1]), "+v"(sg[2]), "+v"(sg[3]), "+v"(g[0]), "+v"(g[1]), "+v"(g[2]), "+v"(g[3]) :: "memory");
            unsigned spins = 0;
            auto bad4 = [&](const v4u (&x)[4]) { u32 d = 0; for (int t = 0; t < 4; ++t) d = tag_bad(x[t], c.tagA, c.tagB, d); return ANY(d != 0u); };
            if (bad4(g)) {
                NOUNROLL do {
                    MEMBAR();
#pragma unroll
                    for (int i = 0; i < 4; ++i) g[i] = ld128(c.rws, tid * 16, OFF_KVG + cp * 16384 + i * 4096);
                } while (bad4(g) && !spin_fail(c, spins, 5u));
            }
            STAMP3(30);
#pragma unroll
            for (int i = 0; i < 4; ++i) *(LAS v2u*)(c.L + CV_B + (i >> 1) * CV_CH + CV_KV + (tid + 256 * (i & 1)) * 8) = v2u{g[i].x, g[i].z};
            if (bad4(sg)) {
                NOUNROLL do {
                    MEMBAR();
#pragma unroll
                    for (int t = 0; t < 4; ++t) sg[t] = ld128(c.rws, tid * 16, OFF_ST0 + t * 4096);
                } while (bad4(sg) && !spin_fail(c, spins, 6u));
            }
#pragma unroll
            for (int t = 0; t < 4; ++t) *(LAS v2u*)(c.L + CV_ST + (tid + 256 * t) * 8) = v2u{sg[t].x, sg[t].z};
        }
        bar();
        STAMP(4); STAMP3(7);
        LAS char* bb = cb + CV_KV + (2032 - 16 * i16 + 32 * p + 8 * q) * 2;
        f32x4 acc[8];
        float xs[4][4], qs[4][4];
        v4u B[32];
        v2u xg[4];
        B[0] = *(LAS v4u*)(bb + 3 * 128); B[1] = *(LAS v4u*)(bb + 2 * 128); B[2] = *(LAS v4u*)(bb + 128);
        const u32 s4 = (u32)(256 * p + 4 * i16 + q), xo = OFF_X0G + ch * 4096;
        SB();
        sfor<0, 32>([&](auto OC) {
            constexpr int oi = decltype(OC)::value, om = oi - 3;
            constexpr int nm = 8 - (om > 0 ? (om + 3) / 4 : 0) + ((om >= 0 && (om & 3) == 0) ? 1 : 0);
            if constexpr (oi % 4 == 0) STAMP3(8 + oi / 4);
            if constexpr (oi + 3 < 32) B[oi + 3] = *(LAS v4u*)(bb - (om + 3) * 128);
#pragma unroll
            for (int cb_ = 7; cb_ >= 0; --cb_)
                if (4 * cb_ - om >= 0) acc[cb_] = mfma16(A[4 * cb_ - om], B[oi], oi == 0 ? f32x4{0, 0, 0, 0} : acc[cb_]);
            if constexpr (om >= 0 && (om & 3) == 0) acc[om >> 2] = mfma16(AD, B[oi], acc[om >> 2]);
            if constexpr (oi < 2) {
                asm volatile("buffer_load_dwordx2 %0, %2, %3, %4 offen offset:%5 sc1\n\tbuffer_load_dwordx2 %1, %2, %3, %4 offen offset:%6 sc1"
                             : "=&v"(xg[2 * oi]), "=&v"(xg[2 * oi + 1]) : "v"(s4 * 8), "s"(c.rws), "s"(xo), "i"(oi * 1024), "i"(oi * 1024 + 512) : "memory");
            }
            if constexpr (oi >= 5 && oi < 9) sg[oi - 5] = *(LAS v4u*)(c.L + CV_ST + s4 * 16 + (oi - 5) * 1024);
            if constexpr (oi < NW1) w_read1<oi>(cs_, wl, W1, W2);
            if constexpr (oi >= 2 && (oi & 1) == 0 && NW1 + (oi - 2) / 2 < 12) w_dma<NW1 + (oi - 2) / 2>(cs_, wt, w_soff<NW1 + (oi - 2) / 2>(cs_));
            if constexpr (oi == 9) asm volatile("s_waitcnt vmcnt(8)" : "+v"(xg[0]), "+v"(xg[1]), "+v"(xg[2]), "+v"(xg[3]) :: "memory");
            if constexpr (oi >= 9 && oi < 17) {
                constexpr int t = (oi - 9) >> 1, hf = (oi - 9) & 1;
                LV(xg[t]); LV(sg[t]);
                const u32 xw = xg[t][hf];
                const u32 sa = sg[t][2 * hf], sb = sg[t][2 * hf + 1];
                xs[t][2 * hf] = bflo(xw); xs[t][2 * hf + 1] = bfhi(xw);
                qs[t][2 * hf] = (xs[t][2 * hf] - bflo(sa)) * __builtin_amdgcn_rcpf(bfhi(sa));
                qs[t][2 * hf + 1] = (xs[t][2 * hf + 1] - bflo(sb)) * __builtin_amdgcn_rcpf(bfhi(sb));
                asm volatile("" : "+v"(qs[t][2 * hf]), "+v"(qs[t][2 * hf + 1]));
            }
            if constexpr (oi == 21 || oi == 23) {
                const v4u ff = {~0u, ~0u, ~0u, ~0u};
                st128(ff, c.rws, lane * 16, OFF_X3 + (cs_.grp * 56 + (oi - 21) * 14 + 4 * cs_.j + cs_.w) * 1024);
            }
            ilv<nm, 2>();
            SB();
        });
        STAMP(5); STAMP3(16);
        LAS char* ex = c.L;
        f32x4 fin[4];
        if (p == 0) {
#pragma unroll
            for (int t = 0; t < 4; ++t) { *(LAS f32x4*)(ex + w * 4096 + t * 1024 + lane * 16) = acc[4 + t]; fin[t] = acc[t]; }
        } else {
#pragma unroll
            for (int t = 0; t < 4; ++t) { *(LAS f32x4*)(ex + w * 4096 + t * 1024 + lane * 16) = acc[t]; fin[t] = acc[4 + t]; }
        }
        bar();
        STAMP3(17);
        const u32 toff = (u32)((32 * p + 8 * (q & 1) + (i16 >> 1)) * 28672 + ch * 64 + (i16 & 1) * 32 + (q >> 1) * 16);
        v2u y[4], x1[4];
#pragma unroll
        for (int t = 0; t < 4; ++t) {
            fin[t] += *(LAS f32x4*)(ex + (w ^ 1) * 4096 + t * 1024 + lane * 16);
            y[t] = v2u{pk(fin[t][0], fin[t][1]), pk(fin[t][2], fin[t][3])};
            x1[t] = v2u{pk(__builtin_fmaf(qs[t][0], bflo(y[t].x), xs[t][0]), __builtin_fmaf(qs[t][1], bfhi(y[t].x), xs[t][1])),
                        pk(__builtin_fmaf(qs[t][2], bflo(y[t].y), xs[t][2]), __builtin_fmaf(qs[t][3], bfhi(y[t].y), xs[t][3]))};
        }
        auto merge = [&](const v2u& a, const v2u& b_) {
            auto r0 = __builtin_amdgcn_permlane16_swap(a.x, b_.x, false, false);
            auto r1 = __builtin_amdgcn_permlane16_swap(a.y, b_.y, false, false);
            return v4u{r0[0], r1[0], r0[1], r1[1]};
        };
        st128(merge(x1[0], x1[1]), c.rws, toff, OFF_X1);
        st128(merge(x1[2], x1[3]), c.rws, toff, OFF_X1 + 16 * 28672);
        st128(merge(y[0], y[1]), c.rws, toff, OFF_YT);
        st128(merge(y[2], y[3]), c.rws, toff, OFF_YT + 16 * 28672);
        STAMP(6); STAMP3(18);
        sfor<12, 12 + NW1>([&](auto NC) { w_dma<decltype(NC)::value>(cs_, wt, w_soff<decltype(NC)::value>(cs_)); });
        WAIT_VM(6 + 2 * NW1);
        sfor<NW1, 12>([&](auto NC) { w_read1<decltype(NC)::value>(cs_, wl, W1, W2); });
        WAIT_LGKM(0);
        sfor<12 + NW1, 20>([&](auto NC) { w_dma<decltype(NC)::value>(cs_, wt, w_soff<decltype(NC)::value>(cs_)); });
        STAMP3(19);
        WAIT_VM(16);
        bar();
        if (tid < 8) __builtin_amdgcn_raw_buffer_store_b64(v2u{c.tagA, c.tagB}, c.rws, (u32)(tid * 2048 + wb * 8), OFF_FL, AUX_SC1);
        MEMBAR();
        STAMP(7); STAMP3(20);
        sfor<20, 24>([&](auto NC) { w_dma<decltype(NC)::value>(cs_, wt, w_soff<decltype(NC)::value>(cs_)); });
        WAIT_VM(12);
        sfor<12, 18>([&](auto NC) { w_read1<decltype(NC)::value>(cs_, wl, W1, W2); });
        WAIT_LGKM(0);
        sfor<24, 28>([&](auto NC) { w_dma<decltype(NC)::value>(cs_, wt, w_soff<decltype(NC)::value>(cs_)); });
        STAMP3(21);
    }
    Ad ad;
    ad.l16 = lane_id() * 16;
    ad.hst = OFF_H + (c.grp * 224 + 16 * c.j + 4 * w) * 1024;
    ad.x3o = OFF_X3 + (c.grp * 56 + 4 * c.j + w) * 1024;
    ad.sto = OFF_ST + c.grp * 2 * ST_HG;
    asm volatile("" : "+s"(ad.hst), "+s"(ad.x3o), "+s"(ad.sto));
    const u32 swl = wl2_of(c, ad.l16 >> 4);
    const WSt w2t = w_st<28>(c, ad.l16 >> 4);
    u32 wso[7];
#pragma unroll
    for (int m = 0; m < 7; ++m) {
        const int mm = member(c, m);
        wso[m] = (256 * mm + 64 * w) * (D * 2) + 128 * c.j; ad.so[m] = ad.hst + (mm - c.j) * 16384;
        asm volatile("" : "+s"(wso[m]), "+s"(ad.so[m]));
    }
    auto w2_dma = [&](auto NC) { constexpr int n = decltype(NC)::value; w_dma<n>(c, w2t, wso[k_mem(w2_step_of(n))] + k_sub(w2_step_of(n)) * 16 * (D * 2)); };
    {
        sfor<28, 30>(w2_dma);
        WAIT_VM(12);
        w_read<18, 24>(c, W1, W2);
        STAMP(8); STAMP3(22);
        if (w == 1) {
            v4u g[2];
            unsigned spins = 0;
            NOUNROLL for (;;) {
                MEMBAR();
                g[0] = ld128(c.rws, ad.l16, OFF_CSG + c.j * 2048); g[1] = ld128(c.rws, ad.l16 + 1024, OFF_CSG + c.j * 2048);
                if (!ANY(tag_bad(g[0], c.tagA, c.tagB, tag_bad(g[1], c.tagA, c.tagB, 0u)) != 0u)) break;
                if (spin_fail(c, spins, 8u)) break;
            }
            *(LAS v2u*)(c.L + L_CS + (ad.l16 >> 1)) = v2u{g[0].x, g[0].z}; *(LAS v2u*)(c.L + L_CS + 512 + (ad.l16 >> 1)) = v2u{g[1].x, g[1].z};
        }
        if (w == 0) {
            unsigned spins = 0;
            NOUNROLL for (;;) {
                MEMBAR();
                const v4u f0 = ld128(c.rws, ad.l16, OFF_FL + (wb & 7) * 2048);
                const v4u f1 = ld128(c.rws, 1024 + min(ad.l16, 752u), OFF_FL + (wb & 7) * 2048);
                const u32 d = xor_or(f0.x, c.tagA, xor_or(f0.z, c.tagA, xor_or(f1.x, c.tagA, xor_or(f1.z, c.tagA, tag_bad(f0, c.tagB, c.tagB, tag_bad(f1, c.tagB, c.tagB, 0u))))));
                if (!ANY(d != 0u)) break;
                if (spin_fail(c, spins, 7u)) break;
            }
            if (wb == 0 && ad.l16 == 0) __builtin_amdgcn_raw_buffer_store_b64(v2u{c.tagA, c.tagB}, c.rws, 0, OFF_EP, AUX_SC1);
        }
        bar();
        STAMP(9); STAMP3(23);
        {
            const u32 xb = c.lbase + L_X + w * 7168, xs_ = OFF_X1 + c.grp * 57344 + w * 7168;
            sfor<0, 14>([&](auto IC) {
                constexpr int i = decltype(IC)::value, o = (i / 7) * 28672 + (i % 7) * 1024;
                dma16<true>(xb + o, ad.l16, c.rws, xs_ + o);
            });
        }
        STAMP3(24);
        WAIT_VM(14);
        w_read<24, 28>(c, W1, W2);
        w_read1<28>(c, swl, W1, W2); w_read1<29>(c, swl, W1, W2);
        w_neg<28>(W2); w_neg<29>(W2);
        WAIT_VM(7);
        bar();
        STAMP(10); STAMP3(25);
    }
    STAMP3(26);
    pin_a<0, 24>(W1);
    v4u hb[56], xres[2];
    G1Out ga, gb;
    v2u gate;
    f32x4 csq[3];
    St1 st;
    ad.ca = L_CS + (w * 64 + (ad.l16 >> 9) * 4) * 4;
    using M0 = std::integral_constant<int, 0>; using M1 = std::integral_constant<int, 1>;
    {
        st.ad = xtr_of(ad.l16 >> 4) + w * 7168;
        auto stats = [&](auto HC, auto FC, G1Out& o) {
            constexpr int H = decltype(HC)::value, f = decltype(FC)::value;
            if constexpr (f == 0) { st.s1 = 0.f; st.s2 = 0.f; }
            if constexpr (f >= 2 && f < 23) { constexpr int ph = (f - 2) % 3, k = (f - 2) / 3; st1_acc<k, ph * 3, ph == 2 ? 8 : ph * 3 + 3>(st); }
            if constexpr (f % 3 == 0 && f < 21) st1_read<H, f / 3>(c, st);
            if constexpr (f == 23) part_put(c, ad, L_SP + H * 1024, st.s1, st.s2);
            if constexpr (f == 24) { SB(); asm volatile("s_waitcnt lgkmcnt(2)\n\ts_barrier" ::: "memory"); }
            if constexpr (f == 25) { const f32x2 t = part_get(c, ad, L_SP + H * 1024); ln_scalars(t[0], t[1], o.mb, o.r); asm volatile("" : "+v"(o.mb), "+v"(o.r)); }
        };
        auto stage = [&](auto GC_, auto XC) {
            constexpr int g = decltype(GC_)::value, extra = decltype(XC)::value;
            if constexpr (g >= 15 && g < 43 && (g & 1)) w_neg<30 + (g - 15) / 2>(W2);
            if constexpr (g == 41) *(LAS v4u*)(c.L + (ad.l16 + w * 1024) + L_JK) = W2[15][1];
            if constexpr (g >= 13 && g < 41 && (g & 1)) { WAIT_VM(12 + extra); w_read1<30 + (g - 13) / 2>(c, swl, W1, W2); }
            if constexpr (g < 52 && (g & 1) == 0) w2_dma(std::integral_constant<int, 30 + g / 2>{});
        };
        constexpr unsigned E2 = 0xAAAAAAAu;
        g1_main<true, 0, PS0, 0x9AAAAAAu>(c, ad, W1, ga.acc, [&](auto FC) {
            constexpr int f = decltype(FC)::value;
            stage(FC, M0{});
            stats(M0{}, FC, ga);
            if constexpr (f >= 26) csq[f - 26] = epi_cs(c, ad, f - 26);
        });
        STAMP(11);
        auto side_b = [&](auto MC, auto FC) {
            constexpr int f = decltype(FC)::value, M = decltype(MC)::value;
            if constexpr (f < 8) g1_epi<0, M, f>(c, ad, ga, csq[f % 3], hb[f >> 1]);
            if constexpr (f < 8 && (f & 1)) h_store<0, f / 2>(c, ad, hb[f >> 1]);
            if constexpr (f < 6) csq[(f + 2) % 3] = epi_cs(c, ad, f + 2);
            if constexpr (f == 18) gate = gate_ld<true>(c, ad, 0);
            if constexpr (f == 19) xres[0] = xres_ld<0, M>(c, ad);
            if constexpr (f == 23) {
                SB();
                asm volatile("s_waitcnt vmcnt(%1)" : "+v"(gate) : "i"(M == 0 ? 4 : 0) : "memory");
                gate_wait<M>(c, ad, 0, gate);
            }
            if constexpr (f >= 24) hb[f - 20] = h_next<f - 20>(c, ad);
            if constexpr (f >= 26) csq[f - 26] = epi_cs(c, ad, f - 26);
        };
        const u32 ra = L_W + w * 24576 + 1024 + ad.l16;
        v4u rm;
        g1_main<true, 1, PS0 + 7, 0x9AAAAAAu>(c, ad, W1, gb.acc, [&](auto FC) {
            constexpr int f = decltype(FC)::value;
            stage(std::integral_constant<int, 28 + f>{}, std::integral_constant<int, (f < 8 ? f / 2 : 4)>{});
            if constexpr (f >= 15 && f < 21) { LV(rm); *(LAS v4u*)(c.L + ra + ((8 + f - 15) % 12) * 2048) = rm ^ SGN; }
            if constexpr (f >= 14 && f < 20) { WAIT_VM(2 * ((f >> 1) - (f - 14))); rm = *(LAS v4u*)(c.L + ra + ((8 + f - 14) % 12) * 2048); }
            stats(M1{}, FC, gb);
            side_b(M0{}, FC);
        });
        STAMP(12);
        {
            WAIT_VM(4);
            v4u t[6];
#pragma unroll
            for (int i = 0; i < 6; ++i) t[i] = *(LAS v4u*)(c.L + ra + ((14 + i) % 12) * 2048);
#pragma unroll
            for (int i = 0; i < 6; ++i) *(LAS v4u*)(c.L + ra + ((14 + i) % 12) * 2048) = t[i] ^ SGN;
            SB();
        }
        pin_a<0, 24>(W1);
        STAMP2(PS0 + 14);
        STAMP(13);
        g2_pair<0>(c, ad, W2, hb, gb, csq, ga.r, xres, c.rws);
        pin_a<0, 24>(W1);
        STAMP(14);
        v4u t[7];
        Gath g3;
        sfor<0, 7>([&](auto IC) { t[decltype(IC)::value] = x3_ld1<0, decltype(IC)::value>(c, ad); });
        x3_wait<0>(c, ad, t);
        sfor<0, 7>([&](auto IC) { x3_put1<0, decltype(IC)::value>(c, ad, t[decltype(IC)::value]); });
        bar();
        stat_pub<1, 1>(c, ad, L_S2, 3);
        STAMP(17);
        g1_main<false, 0, PS1, 0xEAA8AAAu>(c, ad, W1, ga.acc, [&](auto FC) {
            constexpr int f = decltype(FC)::value;
            if constexpr (f >= 2 && f < 6) gath_ld<0, 1, f - 2>(c, ad, g3);
            if constexpr (f == 11) { SB(); gath_wait<0, 1>(c, ad, g3); }
            if constexpr (f == 12) { gath_ln(g3, ga.mb, ga.r); asm volatile("" : "+v"(ga.mb), "+v"(ga.r)); }
            if constexpr (f >= 18 && f < 25) t[f - 18] = x3_ld1<1, f - 18>(c, ad);
            if constexpr (f == 24 || f == 25) csq[f - 24] = epi_cs(c, ad, f - 24);
            if constexpr (f == 25) { gath_ld<1, 1, 0>(c, ad, g3); gath_ld<1, 1, 1>(c, ad, g3); }
            if constexpr (f == 26) { gath_ld<1, 1, 2>(c, ad, g3); gath_ld<1, 1, 3>(c, ad, g3); SB(); x3_wait<1>(c, ad, t); }
            if constexpr (f == 27) sfor<0, 7>([&](auto IC) { x3_put1<1, decltype(IC)::value>(c, ad, t[decltype(IC)::value]); });
        });
        STAMP(18);
        bar();
        g1_main<false, 1, PS1 + 7, 0x8AA8AAAu>(c, ad, W1, gb.acc, [&](auto FC) {
            constexpr int f = decltype(FC)::value;
            if constexpr (f == 11) { SB(); gath_wait<1, 1>(c, ad, g3); }
            if constexpr (f == 12) { gath_ln(g3, gb.mb, gb.r); asm volatile("" : "+v"(gb.mb), "+v"(gb.r)); }
            if constexpr (f == 21) { SB(); asm volatile("s_waitcnt lgkmcnt(2)\n\ts_barrier" ::: "memory"); }
            side_b(M1{}, FC);
        });
        (void)E2;
        STAMP(19);
        STAMP2(PS1 + 14);
        STAMP(20);
        const __attribute__((address_space(4))) char* ka = (const __attribute__((address_space(4))) char*)__builtin_amdgcn_kernarg_segment_ptr();
        asm volatile("" : "+s"(ka));
        g2_pair<1>(c, ad, W2, hb, gb, csq, ga.r, xres, mkrs(*(void* const __attribute__((address_space(4)))*)(ka + 72), TSZ));
        STAMP(21);
    }
    asm volatile(".Lfk_end:");
}

extern "C" void kernel_launch(void* const* d_in, const int* in_sizes, int n_in,
                              void* d_out, int out_size, void* d_ws, size_t ws_size,
                              hipStream_t stream) {
    (void)hipFuncSetAttribute((const void*)fused_kernel, hipFuncAttributeMaxDynamicSharedMemorySize, L_TOT);
    fused_kernel<<<256, 256, L_TOT, stream>>>((const u16*)d_in[0], (const u16*)d_in[1], (const u16*)d_in[2], (const u16*)d_in[3], (const u16*)d_in[4],
                                              (const u16*)d_in[5], (const u16*)d_in[7], (const u16*)d_in[8], (unsigned char*)d_ws, (u16*)d_out);
}
```

```cpp
#include <hip/hip_runtime.h>
#include <stdint.h>
#include <type_traits>

typedef unsigned short u16;
typedef unsigned u32;
typedef u32 v4u __attribute__((ext_vector_type(4)));
typedef u32 v2u __attribute__((ext_vector_type(2)));
typedef float f32x2 __attribute__((ext_vector_type(2)));
typedef float f32x4 __attribute__((ext_vector_type(4)));
typedef float f32x16 __attribute__((ext_vector_type(16)));
typedef __bf16 bf16x8 __attribute__((ext_vector_type(8)));
typedef __bf16 bf16x2 __attribute__((ext_vector_type(2)));
typedef short s16x4 __attribute__((ext_vector_type(4)));
typedef short s16x2 __attribute__((ext_vector_type(2)));
typedef unsigned short u16x2 __attribute__((ext_vector_type(2)));
#define LAS __attribute__((address_space(3)))
#define GAS __attribute__((address_space(1)))
#define DI __device__ __forceinline__
#define RLX_AGENT __ATOMIC_RELAXED, __HIP_MEMORY_SCOPE_AGENT
#define SB() __builtin_amdgcn_sched_barrier(0)
#define NOUNROLL _Pragma("unroll 1")
#define MEMBAR() asm volatile("" ::: "memory")
template <class T> __device__ __forceinline__ void LV(T& x) { asm volatile("" : "+v"(x)); }
template <class T> __device__ __forceinline__ void LA(T& x) { asm volatile("" : "+a"(x)); }
#define ANY(x) (__builtin_amdgcn_ballot_w64(x) != 0ull)

constexpr int S = 2048, D = 448, F = 1792;
constexpr float EPS = 1e-5f;
constexpr u32 TSZ = S * D * 2;
constexpr u32 OFF_EP  = 0;
constexpr u32 OFF_FL  = 4096;
constexpr u32 OFF_ST0 = OFF_FL + 16384;
constexpr u32 OFF_CSG = OFF_ST0 + 16384;
constexpr u32 OFF_ST  = OFF_CSG + 16384;
constexpr u32 OFF_KVG = 1048576;
constexpr u32 OFF_X0G = OFF_KVG + 2 * TSZ;
constexpr u32 OFF_X1  = OFF_X0G + 2 * TSZ;
constexpr u32 OFF_YT  = OFF_X1 + TSZ;
constexpr u32 OFF_X3  = OFF_YT + TSZ;
constexpr u32 OFF_H   = OFF_X3 + TSZ;
constexpr u32 WS_BYTES = OFF_H + 64 * 112 * 1024;
static_assert(OFF_ST + 64 * 24 * 512 <= OFF_KVG, "ws map");
#define STAMP(i) do {} while (0)
#define STAMP2(i) do {} while (0)
#define STAMP4(i) do {} while (0)
#define STAMP3(i) do {} while (0)
constexpr int RSRC_W3 = 0x00020000;
constexpr int AUX_SC1 = 16;
constexpr unsigned SPIN_CAP = 1u << 14;
constexpr u32 SGN = 0x80008000u;

template <int I, int N, class Fn> DI void sfor(Fn&& fn) { if constexpr (I < N) { fn(std::integral_constant<int, I>{}); sfor<I + 1, N>(fn); } }

DI float bflo(u32 x) { return __uint_as_float(x << 16); }
DI float bfhi(u32 x) { return __uint_as_float(x & 0xffff0000u); }
DI u32 pk(float a, float b) { f32x2 v = {a, b}; return __builtin_bit_cast(u32, __builtin_convertvector(v, bf16x2)); }
DI float rbf(float a) { return bflo(pk(a, a)); }
DI __amdgpu_buffer_rsrc_t mkrs(const void* p, u32 bytes) { return __builtin_amdgcn_make_buffer_rsrc((void*)p, 0, (int)bytes, RSRC_W3); }
DI f32x4 mfma16(v4u a, v4u b, f32x4 c) { return __builtin_amdgcn_mfma_f32_16x16x32_bf16(__builtin_bit_cast(bf16x8, a), __builtin_bit_cast(bf16x8, b), c, 0, 0, 0); }
DI f32x16 mfma32(v4u a, v4u b, f32x16 c) { return __builtin_amdgcn_mfma_f32_32x32x16_bf16(__builtin_bit_cast(bf16x8, a), __builtin_bit_cast(bf16x8, b), c, 0, 0, 0); }
DI f32x16 zero16() { f32x16 z; for (int i = 0; i < 16; ++i) z[i] = 0.f; return z; }
DI v2u trd(LAS char* p) { return __builtin_bit_cast(v2u, __builtin_amdgcn_ds_read_tr16_b64_v4i16((LAS s16x4*)p)); }
DI v4u trd2(LAS char* p, int second) { v2u a = trd(p), b = trd(p + second); return v4u{a.x, a.y, b.x, b.y}; }
DI float dot2(u32 a, u32 b, float c) { return __builtin_amdgcn_fdot2_f32_bf16(__builtin_bit_cast(bf16x2, a), __builtin_bit_cast(bf16x2, b), c, false); }
DI v4u ld128(__amdgpu_buffer_rsrc_t rs, u32 vo, u32 so) { return __builtin_amdgcn_raw_buffer_load_b128(rs, vo, so, AUX_SC1); }
template <bool SC1, int OFF = 0> DI v4u ld128_asm(__amdgpu_buffer_rsrc_t rs, u32 vo, u32 so) {
    v4u r;
    if constexpr (SC1) asm volatile("buffer_load_dwordx4 %0, %1, %2, %3 offen offset:%4 sc1" : "=v"(r) : "v"(vo), "s"(rs), "s"(so), "i"(OFF) : "memory");
    else asm volatile("buffer_load_dwordx4 %0, %1, %2, %3 offen offset:%4" : "=v"(r) : "v"(vo), "s"(rs), "s"(so), "i"(OFF) : "memory");
    return r;
}
template <int OFF = 0> DI void st128(v4u v, __amdgpu_buffer_rsrc_t rs, u32 vo, u32 so) {
    asm volatile("buffer_store_dwordx4 %0, %1, %2, %3 offen offset:%4 sc1\n\ts_nop 1" :: "v"(v), "v"(vo), "s"(rs), "s"(so), "i"(OFF) : "memory");
}
DI int lane_id() { int l; asm volatile("v_mbcnt_lo_u32_b32 %0, -1, 0\n\tv_mbcnt_hi_u32_b32 %0, -1, %0" : "=v"(l)); return l; }
DI float hsum(float a) {
    const u32 v = __float_as_uint(a);
    auto t = __builtin_amdgcn_permlane32_swap(v, v, false, false);
    return __uint_as_float(t[0]) + __uint_as_float(t[1]);
}
DI float ln_sd(float var) { return rbf(__builtin_amdgcn_sqrtf(rbf(rbf(var) + EPS))); }
DI void ln_scalars(float s1, float s2, float& m, float& r) {
    m = s1 * (1.0f / D);
    r = __builtin_amdgcn_rsqf(fmaxf(__builtin_fmaf(-m, m, s2 * (1.0f / D)), 0.f) + EPS);
}
DI u32 xor_or(u32 a, u32 b, u32 c) { return __builtin_amdgcn_bitop3_b32(a, b, c, 0xBE); }
DI u32 tag_bad(const v4u& g, u32 tA, u32 tB, u32 acc) { return xor_or(g.w, tB, xor_or(g.y, tA, acc)); }
template <bool SC1> DI void dma16(u32 ldsaddr, u32 voff, __amdgpu_buffer_rsrc_t rs, u32 soff) {
    if constexpr (SC1) asm volatile("s_mov_b32 m0, %0\n\ts_nop 0\n\tbuffer_load_dwordx4 %1, %2, %3 offen sc1 lds" :: "s"(ldsaddr), "v"(voff), "s"(rs), "s"(soff) : "memory", "m0");
    else asm volatile("s_mov_b32 m0, %0\n\ts_nop 0\n\tbuffer_load_dwordx4 %1, %2, %3 offen lds" :: "s"(ldsaddr), "v"(voff), "s"(rs), "s"(soff) : "memory", "m0");
}
template <int LI> DI void dma_pair(u32 ldsbase, u32 vo0, u32 vo1, __amdgpu_buffer_rsrc_t rs, u32 soff) {
    asm volatile("s_add_u32 m0, %0, %5\n\ts_nop 0\n\tbuffer_load_dwordx4 %1, %3, %4 offen lds\n\tbuffer_load_dwordx4 %2, %3, %4 offen offset:1024 lds"
                 :: "s"(ldsbase), "v"(vo0), "v"(vo1), "s"(rs), "s"(soff), "i"(LI) : "memory", "m0", "scc");
}
DI void touch(u32 junk, u32 voff, __amdgpu_buffer_rsrc_t rs, u32 soff) {
    asm volatile("s_mov_b32 m0, %0\n\ts_nop 0\n\tbuffer_load_dword %1, %2, %3 offen lds" :: "s"(junk), "v"(voff), "s"(rs), "s"(soff) : "memory", "m0");
}
#define WAIT_VM(n) asm volatile("s_waitcnt vmcnt(%0)" :: "i"(n) : "memory")
#define WAIT_LGKM(n) asm volatile("s_waitcnt lgkmcnt(%0)" :: "i"(n) : "memory")
DI void bar() { asm volatile("s_waitcnt lgkmcnt(0)\n\ts_barrier" ::: "memory"); }
DI int wrap7(int x) { return x >= 7 ? x - 7 : x; }
template <int N, int K> DI void ilv() { if constexpr (N > 0) { __builtin_amdgcn_sched_group_barrier(0x8, 1, 0); __builtin_amdgcn_sched_group_barrier(0x366, K, 0); ilv<N - 1, K>(); } }

constexpr int L_X = 0;
constexpr int L_W = 57344;
constexpr int L_M = L_W + 98304;
constexpr int L_SP = L_M;
constexpr int L_CS = L_M + 2048;
constexpr int L_S2 = L_M + 3072;
constexpr int L_JK = L_M + 4096;
constexpr int L_TOT = L_M + 8192;
constexpr int T_K = 0, T_V = 9216, T_X = 17408, T_S = L_JK;
constexpr int CV_B = 25600;
constexpr int CV_HE = 0, CV_HO = 4160, CV_KV = CV_HO + 4160, CV_CH = CV_KV + 4608;
constexpr int CV_ST = 16384;
static_assert(CV_B + 2 * CV_CH <= L_W, "lds map");

struct Ctx {
    LAS char* L;
    __amdgpu_buffer_rsrc_t rws, rw1, rw2;
    GAS u32* tmo;
    int w, j, grp, kb0, gi;
    u32 lbase, tagA, tagB;
};
DI Ctx fresh(const Ctx& c0) { Ctx c = c0; asm volatile("" : "+s"(c.w), "+s"(c.j), "+s"(c.grp), "+s"(c.kb0), "+s"(c.gi)); return c; }
DI bool spin_fail(const Ctx& c, unsigned& spins, u32 code) {
    if (++spins > SPIN_CAP) { __hip_atomic_store(c.tmo, code, RLX_AGENT); return true; }
    __builtin_amdgcn_s_sleep(1);
    return false;
}
DI u32 xtr_of(int lane) { return (u32)(((lane >> 5) * 4 + ((lane >> 2) & 3)) * 64 + ((lane >> 4) & 1) * 32 + (lane & 3) * 8); }
DI u32 wl1_of(const Ctx& c, int lane) {
    const int rr = (lane >> 5) * 4 + ((lane >> 2) & 3);
    return (u32)(L_W + c.w * 24576 + rr * 128 + ((rr >> 1) & 1) * 64 + ((lane >> 4) & 1) * 32 + (lane & 3) * 8);
}
DI u32 wl2_of(const Ctx& c, int lane) { return wl1_of(c, lane) ^ (u32)((c.w >> 1) * 64 + (c.w & 1) * 32); }
DI int member(const Ctx& c, int m) {
    if (m == 0) return c.j;
    int t = m - 1 + 2 * c.gi; t = t >= 6 ? t - 6 : t; t = t >= 6 ? t - 6 : t;
    return wrap7(c.j + 1 + t);
}
typedef v4u W1T[28][2];
typedef v4u W2T[16][2];
typedef f32x16 AccT[2];

DI constexpr int k_mem(int f) { return f < 4 ? 0 : 1 + (f - 4) % 6; }
DI constexpr int k_sub(int f) { return f < 4 ? f : (f - 4) / 6; }
DI constexpr int w2_step_of(int n) { return n < 40 ? 2 * (n - 28) : (n < 44 ? n - 16 : 2 * (n - 44) + 1); }
template <int N> DI u32 w_soff(const Ctx& c) {
    if constexpr (N < 28) return (N % 7 + 7 * ((N / 7 + c.kb0) & 3)) * 16 * (F * 2) + (256 * c.j + 64 * c.w) * 2;
    else return (256 * member(c, k_mem(w2_step_of(N))) + 64 * c.w + 16 * k_sub(w2_step_of(N))) * (D * 2) + 128 * c.j;
}
template <int N> DI u32 w_voff(int lane) { return (lane >> 3) * ((N < 28 ? F : D) * 2) + ((lane & 7) ^ (((lane >> 4) & 1) << 2)) * 16; }
struct WSt { u32 ring, vo0, vo1; };
template <int N> DI WSt w_st(const Ctx& c, int lane) { WSt t; t.ring = c.lbase + L_W + c.w * 24576; t.vo0 = w_voff<N>(lane); t.vo1 = t.vo0 + 8 * ((N < 28 ? F : D) * 2) - 1024; return t; }
template <int N> DI void w_dma(const Ctx& c, const WSt& t, u32 so) { dma_pair<(N % 12) * 2048>(t.ring, t.vo0, t.vo1, N < 28 ? c.rw1 : c.rw2, so); }
DI v4u w_frag(const Ctx& c, u32 wl, int slot, int t) { return trd2(c.L + (wl ^ (u32)(t * 64)) + slot * 2048, 1024); }
template <int N> DI void w_read1(const Ctx& c, u32 wl, W1T& W1, W2T& W2) {
#pragma unroll
    for (int t = 0; t < 2; ++t) {
        if constexpr (N < 28) W1[N][t] = w_frag(c, wl, N % 12, t);
        else W2[N - 28][t] = w_frag(c, wl, N % 12, t);
    }
}
template <int N> DI void w_neg(W2T& W2) {
#pragma unroll
    for (int t = 0; t < 2; ++t) { LV(W2[N - 28][t]); W2[N - 28][t].z ^= SGN; W2[N - 28][t].w ^= SGN; asm volatile("" : "+v"(W2[N - 28][t])); }
}
template <int N0, int N1> DI void w_read(const Ctx& c0, W1T& W1, W2T& W2) {
    const Ctx c = fresh(c0);
    const u32 wl = wl1_of(c, lane_id());
    sfor<N0, N1>([&](auto NC) { w_read1<decltype(NC)::value>(c, wl, W1, W2); });
}

constexpr int PS0 = 0, PS1 = 33;
template <int N0, int N1> DI void pin_a(W1T& W1) {
    if constexpr (N0 < N1) { asm volatile("" : "+a"(W1[N0][0]), "+a"(W1[N0][1])); pin_a<N0 + 1, N1>(W1); }
}
struct G1Out { AccT acc; float mb, r; };
DI constexpr bool reg_start(unsigned long long end, int f) { return f == 0 || ((end >> (f - 1)) & 1ull); }
DI constexpr int reg_len(unsigned long long end, int f) { int n = 1; while (!reg_start(end, f - n + 1)) ++n; return n; }
struct Ad {
    u32 l16;
    u32 ca;
    u32 hst;
    u32 x3o;
    u32 sto;
    u32 so[7];
};
constexpr u32 ST_HG = 3 * 4096;
DI u32 tok32(const Ad& a) { return a.l16 & 496u; }

template <bool TR, int HALF, int S0, unsigned END, class Side>
DI void g1_main(const Ctx& c, Ad& a, const W1T& W1, AccT& acc, Side&& side) {
    const u32 lp = TR ? xtr_of(a.l16 >> 4) : a.l16;
    int kb = c.kb0; asm volatile("" : "+s"(kb));
    u32 la = 0;
    auto ldb = [&](int f) {
        if (f % 7 == 0) la = lp + ((f / 7 + kb) & 3) * 7168;
        LAS char* p = c.L + la + (L_X + HALF * 28672 + (f % 7) * 1024);
        if constexpr (TR) return trd2(p, 512);
        else return *(LAS v4u*)p;
    };
    v4u B[28];
    B[0] = ldb(0); B[1] = ldb(1); B[2] = ldb(2);
    SB();
    sfor<0, 28>([&](auto FC) {
        constexpr int f = decltype(FC)::value;
        if constexpr (f % 4 == 0) STAMP2(S0 + f / 4);
        if constexpr (reg_start(END, f)) asm volatile("" : "+v"(a.l16), "+v"(a.ca));
        acc[0] = mfma32(W1[f][0], B[f], f == 0 ? zero16() : acc[0]);
        acc[1] = mfma32(W1[f][1], B[f], f == 0 ? zero16() : acc[1]);
        if constexpr (f + 3 < 28) B[f + 3] = ldb(f + 3);
        side(FC);
        if constexpr ((END >> f) & 1u) { ilv<2 * reg_len(END, f), 5>(); SB(); }
    });
}
struct St1 { u32 ad; v4u fr[2]; float s1, s2; };
template <bool TR, int HALF, int K> DI void st1_read(const Ctx& c, St1& s) {
    if constexpr (TR) s.fr[K & 1] = trd2(c.L + s.ad + (L_X + HALF * 28672 + K * 1024), 512);
    else s.fr[K & 1] = *(LAS v4u*)(c.L + s.ad + (L_X + HALF * 28672 + K * 1024));
}
template <int K, int E0, int E1> DI void st1_acc(St1& s) {
#pragma unroll
    for (int e = E0; e < E1; ++e) { if (e == E0) LV(s.fr[K & 1]); const u32 x = s.fr[K & 1][e >> 1]; if (e & 1) s.s2 = dot2(x, x, s.s2); else s.s1 = dot2(x, 0x3f803f80u, s.s1); }
}
DI void part_put(const Ctx& c, const Ad& a, int off, float s1, float s2) { *(LAS f32x2*)(c.L + off + tok32(a) * 2 + c.w * 8) = f32x2{hsum(s1), hsum(s2)}; }
DI f32x2 part_get(const Ctx& c, const Ad& a, int off) {
    const f32x4 u = *(LAS f32x4*)(c.L + off + tok32(a) * 2), v = *(LAS f32x4*)(c.L + off + tok32(a) * 2 + 16);
    return f32x2{(u[0] + u[2]) + (v[0] + v[2]), (u[1] + u[3]) + (v[1] + v[3])};
}
DI void frag_sums(const v4u& p, float& s1, float& s2) {
    s1 = 0.f; s2 = 0.f;
#pragma unroll
    for (int e = 0; e < 4; ++e) { s1 = dot2(p[e], 0x3f803f80u, s1); s2 = dot2(p[e], p[e], s2); }
}
template <int HALF, int KIND> DI void stat_pub(const Ctx& c, const Ad& a, int off, int who) {
    if (c.w == who) {
        f32x2 t = part_get(c, a, off);
        const bool z = c.j == 6 && a.l16 >= 512u;
        st128(v4u{z ? 0u : __float_as_uint(t[0]), c.tagA, z ? 0u : __float_as_uint(t[1]), c.tagB}, c.rws, z ? a.l16 : tok32(a), a.sto + HALF * ST_HG + KIND * 4096 + c.j * 512);
    }
}
template <bool DO> DI void stat_pub_if(const Ctx& c, const Ad& a) { if constexpr (DO) stat_pub<0, 0>(c, a, L_S2, 0); }
struct Gath { v4u g[4]; };
template <int HALF, int KIND, int I> DI void gath_ld(const Ctx& c, const Ad& a, Gath& g) {
    g.g[I] = ld128(c.rws, a.l16 + I * 1024, a.sto + HALF * ST_HG + KIND * 4096);
}
DI bool gath_bad(const Ctx& c, const Gath& g) {
    u32 d = 0;
#pragma unroll
    for (int i = 0; i < 4; ++i) d = tag_bad(g.g[i], c.tagA, c.tagB, d);
    return ANY(d != 0u);
}
template <int HALF, int KIND> DI void gath_wait(const Ctx& c, const Ad& a, Gath& g) {
    if (gath_bad(c, g)) {
        unsigned spins = 0;
        NOUNROLL do { MEMBAR(); sfor<0, 4>([&](auto IC) { gath_ld<HALF, KIND, decltype(IC)::value>(c, a, g); }); } while (gath_bad(c, g) && !spin_fail(c, spins, 2u));
    }
}
DI void gath_ln(const Gath& g, float& m, float& r) {
    const float t1 = (__uint_as_float(g.g[0].x) + __uint_as_float(g.g[1].x)) + (__uint_as_float(g.g[2].x) + __uint_as_float(g.g[3].x));
    const float t2 = (__uint_as_float(g.g[0].z) + __uint_as_float(g.g[1].z)) + (__uint_as_float(g.g[2].z) + __uint_as_float(g.g[3].z));
    ln_scalars(hsum(t1), hsum(t2), m, r);
}
DI f32x4 epi_cs(const Ctx& c, const Ad& a, int E) { return *(LAS f32x4*)(c.L + a.ca + E * 32); }
template <int HALF, int MLP, int E>
DI void g1_epi(const Ctx& c, const Ad& a, G1Out& o, f32x4& cs, v4u& fr) {
    constexpr int t = E >> 2, r0 = 4 * (E & 3), g = E & 1;
    constexpr bool neg = (g ^ MLP) != 0;
    float h[4];
    LV(cs); LA(o.acc[t]);
#pragma unroll
    for (int e = 0; e < 4; ++e) h[e] = __builtin_fmaf(-o.mb, cs[e], o.acc[t][r0 + e]);
#pragma unroll
    for (int e = 0; e < 2; ++e) {
        const u32 p = __builtin_bit_cast(u32, __builtin_elementwise_max(__builtin_bit_cast(s16x2, pk(h[2 * e], h[2 * e + 1])), s16x2{0, 0}));
        fr[2 * g + e] = neg ? (p | SGN) : p;
    }
    if (g == 0) LV(fr);
}
template <int HALF, int K> DI void h_store(const Ctx& c, const Ad& a, const v4u& fr) { st128<K * 1024>(fr, c.rws, a.l16, a.hst + HALF * 114688); }

#define NW0_ 0
#define NT0_ 0
constexpr int NT0 = NT0_;
#define NW1_ 2
constexpr int NW1 = NW1_;
#define KP_ 4
constexpr int KP = KP_;
#define TLBW_ 0
constexpr int TLBW = TLBW_;
#define SLP0 0
#define SLP1 16
constexpr int NW0 = NW0_;
#define EPE 8
constexpr int HR = 10, GL = 14, GC = 20;
template <int MLP> DI bool h_bad(const v4u& a, const v4u& b) {
    const u32 lo = MLP == 0 ? (a.x | a.y | b.x | b.y) : ~(a.x & a.y & b.x & b.y);
    const u32 hi = MLP == 0 ? ~(a.z & a.w & b.z & b.w) : (a.z | a.w | b.z | b.w);
    return ((lo | hi) & SGN) != 0u;
}
DI u32 h_base(const Ctx& c, int half) { return OFF_H + ((c.grp * 2 + half) * 112 + 4 * c.w) * 1024; }
template <bool ASM> DI v2u gate_ld(const Ctx& c, const Ad& a, int half) {
    const u32 vo = min(a.l16, 96u) * 1024, so = a.hst - c.j * 16384 + half * 114688 + 4;
    if constexpr (ASM) { v2u r; asm volatile("buffer_load_dwordx2 %0, %1, %2, %3 offen sc1" : "=v"(r) : "v"(vo), "s"(c.rws), "s"(so) : "memory"); return r; }
    else return __builtin_amdgcn_raw_buffer_load_b64(c.rws, vo, so, AUX_SC1);
}
template <int MLP> DI bool gate_bad(v2u x) { return ((MLP == 0 ? (x.x | ~x.y) : (~x.x | x.y)) & SGN) != 0u; }
template <int MLP> DI void gate_wait(const Ctx& c, const Ad& a, int half, v2u x) {
    if (ANY(gate_bad<MLP>(x))) {
        unsigned spins = 0;
        NOUNROLL do { MEMBAR(); x = gate_ld<false>(c, a, half); } while (ANY(gate_bad<MLP>(x)) && !spin_fail(c, spins, 1u));
    }
}
template <int N> DI v4u h_next(const Ctx& c, Ad& a) {
    constexpr int f = N >= 28 ? N - 28 : N;
    if constexpr (N >= 32 && N < 38) a.so[k_mem(f)] += 114688u;
    v4u r = __builtin_amdgcn_raw_buffer_load_b128(c.rws, a.l16 + k_sub(f) * 1024, a.so[k_mem(f)], AUX_SC1);
    if constexpr (N >= 50) a.so[k_mem(f)] -= 114688u;
    return r;
}
template <int MLP, int F_> DI void h_check(const Ctx& c0, v4u& a, v4u& b) {
    if (__builtin_expect(ANY(h_bad<MLP>(a, b)), 0)) {
        const Ctx c = fresh(c0);
        constexpr int half = F_ >= 28, f = F_ - 28 * half;
        const u32 vo = lane_id() * 16 + k_sub(f) * 1024, s0 = h_base(c, half) + member(c, k_mem(f)) * 16384, s1 = h_base(c, half) + member(c, k_mem(f + 1)) * 16384;
        unsigned spins = 0;
        NOUNROLL do {
            MEMBAR();
            a = ld128(c.rws, vo, s0); b = ld128(c.rws, vo, s1);
        } while (ANY(h_bad<MLP>(a, b)) && !spin_fail(c, spins, 3u));
    }
}
template <int HALF, int P> DI void red_write(const Ctx& c, const Ad& a, const AccT& acc) {
    LAS char* dst = c.L + (a.l16 + (P ^ c.w) * 6144) + (L_X + (1 - HALF) * 28672 + (P - 1) * 2048);
    const f32x16& x = acc[P >> 1];
    constexpr int r0 = (P & 1) * 8;
    *(LAS f32x4*)dst = f32x4{x[r0], x[r0 + 1], x[r0 + 2], x[r0 + 3]};
    *(LAS f32x4*)(dst + 1024) = f32x4{x[r0 + 4], x[r0 + 5], x[r0 + 6], x[r0 + 7]};
}
template <int HALF> DI void red_read(const Ctx& c, const Ad& a, f32x4 (&t)[6]) {
    LAS char* src = c.L + (a.l16 + c.w * 6144) + (L_X + (1 - HALF) * 28672);
#pragma unroll
    for (int i = 0; i < 6; ++i) t[i] = *(LAS f32x4*)(src + i * 1024);
}
template <int K> DI void post_half(const f32x4& o, float r, v4u& xres) {
#pragma unroll
    for (int e = 0; e < 2; ++e) xres[2 * K + e] = pk(__builtin_fmaf(o[2 * e], r, bflo(xres[2 * K + e])), __builtin_fmaf(o[2 * e + 1], r, bfhi(xres[2 * K + e])));
}
template <int HALF> DI void out_store(const Ctx& c, const Ad& a, const v4u& x, __amdgpu_buffer_rsrc_t rout) {
    auto r0 = __builtin_amdgcn_permlane32_swap(x.x, x.z, false, false);
    auto r1 = __builtin_amdgcn_permlane32_swap(x.y, x.w, false, false);
    const v4u v = {r0[0], r1[0], r0[1], r1[1]};
    const u32 vo = tok32(a) * (D / 8) + (a.l16 >> 9) * 16, so = (u32)((c.grp * 64 + HALF * 32) * D + 64 * c.j + 16 * c.w) * 2;
    asm volatile("buffer_store_dwordx4 %0, %1, %2, %3 offen\n\ts_nop 1" :: "v"(v), "v"(vo), "s"(rout), "s"(so) : "memory");
}
template <int HALF, int MLP> DI v4u xres_ld(const Ctx& c, const Ad& a) {
    if constexpr (MLP == 0) return trd2(c.L + (xtr_of(a.l16 >> 4) + (4 * c.j + c.w) * 1024) + (L_X + HALF * 28672), 512);
    else return *(LAS v4u*)(c.L + (a.l16 + (4 * c.j + c.w) * 1024) + (L_X + HALF * 28672));
}
template <int HALF> DI v4u y_ld(const Ctx& c, const Ad& a) { return trd2(c.L + (xtr_of(a.l16 >> 4) + c.w * 1024) + (L_X + HALF * 28672 + 24576), 512); }
DI void x3_make(v4u& x, const v4u& yf, float m, float r) {
    const float n = -m * r;
#pragma unroll
    for (int e = 0; e < 4; ++e) {
        const float p = bflo(x[e]), q = bfhi(x[e]);
        x[e] = pk(__builtin_fmaf(__builtin_fmaf(p, r, n), bflo(yf[e]), p), __builtin_fmaf(__builtin_fmaf(q, r, n), bfhi(yf[e]), q));
    }
}
template <int HALF> DI void x3_store(const Ctx& c, const Ad& a, const v4u& x) { st128(x, c.rws, a.l16, a.x3o + HALF * 28672); }
template <int HALF, int I> DI v4u x3_ld1(const Ctx& c, const Ad& a) {
    return ld128(c.rws, a.l16 + (I & 3) * 1024, a.x3o + (6 * c.w - 4 * c.j + (I & 4)) * 1024 + HALF * 28672);
}
DI u32 max3u(u32 a, u32 b, u32 c) { u32 r; asm("v_max3_u32 %0, %1, %2, %3" : "=v"(r) : "v"(a), "v"(b), "v"(c)); return r; }
DI bool x3_bad(const v4u (&t)[7]) {
    u32 mx = 0;
#pragma unroll
    for (int i = 0; i < 7; ++i) mx = max3u(max3u(mx, t[i].x, t[i].y), t[i].z, t[i].w);
    return ANY(mx == ~0u);
}
template <bool EQ> DI void gate_spin(const Ctx& c, u32 vo, u32 so, u32 word, u32 code) {
    unsigned spins = 0;
    NOUNROLL for (;;) {
        MEMBAR();
        const u32 x = __builtin_amdgcn_raw_buffer_load_b32(c.rws, vo, so, AUX_SC1);
        if (!ANY(EQ ? x == word : x != word)) break;
        if (spin_fail(c, spins, code)) break;
    }
}
template <int HALF> DI void x3_wait(const Ctx& c, const Ad& a, v4u (&t)[7]) {
    if (x3_bad(t)) {
        unsigned spins = 0;
        NOUNROLL do { MEMBAR(); sfor<0, 7>([&](auto IC) { t[decltype(IC)::value] = x3_ld1<HALF, decltype(IC)::value>(c, a); }); } while (x3_bad(t) && !spin_fail(c, spins, 4u));
    }
}
template <int HALF, int I> DI void x3_put1(const Ctx& c, const Ad& a, const v4u& t) { *(LAS v4u*)(c.L + (a.l16 + c.w * 7168) + (L_X + HALF * 28672 + I * 1024)) = t; }

template <int MLP>
DI void g2_pair(const Ctx& c, Ad& a, const W2T& W2, v4u (&hb)[56], G1Out& gb, f32x4 (&csq)[3], float r0, v4u (&xres)[2], __amdgpu_buffer_rsrc_t out) {
    const u32 wl0 = wl2_of(c, a.l16 >> 4), wl1 = wl0 ^ 64u;
    AccT acc[2];
    v2u gate = {0, 0};
    v4u wl[2], fr, yf;
    f32x4 o[2], u[2];
    float s1, s2;
    Gath gs;
    const float ra_ = MLP ? -r0 : r0, rb_ = MLP ? -gb.r : gb.r;
    auto wres = [&](int slot, int k) { return trd2(c.L + (k ? wl1 : wl0) + slot * 2048, 1024); };
    auto fetch = [&](auto NC) {
        constexpr int N = decltype(NC)::value;
        if constexpr (N < 56) {
            if constexpr (N >= 28 && N < 32) hb[N] = *(LAS v4u*)(c.L + (a.l16 + c.w * 4096) + (L_X + (N - 28) * 1024));
            else hb[N] = h_next<N>(c, a);
        }
    };
    auto rd = [&](int i) { return *(LAS f32x4*)(c.L + (a.l16 + c.w * 6144) + (L_X + 28672 + i * 1024)); };
    auto own = [&](int k) { return f32x4{acc[0][0][4 * k], acc[0][0][4 * k + 1], acc[0][0][4 * k + 2], acc[0][0][4 * k + 3]}; };
    wl[0] = wres(8, 0); wl[1] = wres(8, 1);
    SB();
    sfor<0, 28>([&](auto PC) {
        constexpr int F_ = 2 * decltype(PC)::value, H = F_ >= 28, f = F_ - 28 * H;
        constexpr bool res = f < 24;
        constexpr int r0_ = f < 24 ? f >> 1 : f - 12;
        if constexpr (F_ == 24 || F_ == 30 || (MLP == 0 && F_ == 36)) { bar(); SB(); stat_pub_if<MLP == 0 && F_ == 36>(c, a); }
        if constexpr (F_ % 4 == 0) STAMP2((MLP ? PS1 : PS0) + 15 + F_ / 4);
        asm volatile("" : "+v"(a.l16), "+v"(a.ca));
        acc[H][0] = mfma32(W2[r0_][0], hb[F_], f == 0 ? zero16() : acc[H][0]);
        acc[H][1] = mfma32(W2[r0_][1], hb[F_], f == 0 ? zero16() : acc[H][1]);
        fetch(std::integral_constant<int, F_ + (F_ < EPE ? HR - 2 : HR)>{});
        fetch(std::integral_constant<int, F_ + 1 + (F_ < EPE ? HR - 2 : HR)>{});
        if constexpr (F_ == EPE - 2) { fetch(std::integral_constant<int, F_ + HR>{}); fetch(std::integral_constant<int, F_ + HR + 1>{}); }
        if constexpr (EPE == 16 && F_ < 16) {
            constexpr int E = F_ / 2;
            g1_epi<1, MLP, E>(c, a, gb, csq[E % 3], fr);
            if constexpr (E & 1) { *(LAS v4u*)(c.L + (a.l16 + c.w * 4096) + (L_X + (E / 2) * 1024)) = fr; h_store<1, E / 2>(c, a, fr); }
            if constexpr (E < 6) csq[(E + 2) % 3] = epi_cs(c, a, E + 2);
        }
        if constexpr (EPE == 8 && F_ < 8) {
            g1_epi<1, MLP, F_>(c, a, gb, csq[F_ % 3], fr); g1_epi<1, MLP, F_ + 1>(c, a, gb, csq[(F_ + 1) % 3], fr);
            *(LAS v4u*)(c.L + (a.l16 + c.w * 4096) + (L_X + (F_ / 2) * 1024)) = fr; h_store<1, F_ / 2>(c, a, fr);
            if constexpr (F_ < 6) { csq[(F_ + 2) % 3] = epi_cs(c, a, F_ + 2); csq[F_ % 3] = epi_cs(c, a, F_ + 3); }
        }
        acc[H][0] = mfma32(res ? wl[0] : W2[r0_ + 1][0], hb[F_ + 1], acc[H][0]);
        acc[H][1] = mfma32((res || f == 26) ? wl[1] : W2[r0_ + 1][1], hb[F_ + 1], acc[H][1]);
        if constexpr (f < 22) { wl[0] = wres((9 + (f >> 1)) % 12, 0); wl[1] = wres((9 + (f >> 1)) % 12, 1); }
        if constexpr (f == 24) wl[1] = *(LAS v4u*)(c.L + (a.l16 + c.w * 1024) + L_JK);
        if constexpr (F_ == 26) { wl[0] = wres(8, 0); wl[1] = wres(8, 1); }
        if constexpr (MLP == 0 && (F_ == 24 || F_ == 26))
            dma16<true>(c.lbase + L_X + (F_ - 24) / 2 * 28672 + 24576 + c.w * 1024, a.l16, c.rws, OFF_YT + (c.grp * 2 + (F_ - 24) / 2) * 28672 + c.j * 4096 + c.w * 1024);
        if constexpr (F_ == GL) gate = gate_ld<false>(c, a, 1);
        if constexpr (F_ == 22) xres[1] = xres_ld<1, MLP>(c, a);
        if constexpr (F_ == 28) { red_write<0, 1>(c, a, acc[0]); red_write<0, 2>(c, a, acc[0]); red_write<0, 3>(c, a, acc[0]); }
        if constexpr (F_ == 30) { u[0] = rd(0); u[1] = rd(2); o[0] = rd(4); }
        if constexpr (F_ == 32) { LA(acc[0][0]); LV(o[0]); LV(u[0]); LV(u[1]); o[0] = (o[0] + own(0)) + (u[0] + u[1]); post_half<0>(o[0], ra_, xres[0]); LV(xres[0]); u[0] = rd(1); u[1] = rd(3); o[0] = rd(5); }
        if constexpr (F_ == 34) { LA(acc[0][0]); LV(o[0]); LV(u[0]); LV(u[1]); o[0] = (o[0] + own(1)) + (u[0] + u[1]); post_half<1>(o[0], ra_, xres[0]); LV(xres[0]); }
        if constexpr (MLP == 0 && F_ == 34) { frag_sums(xres[0], s1, s2); part_put(c, a, L_S2, s1, s2); }
        if constexpr (MLP == 1 && F_ == 36) out_store<0>(c, a, xres[0], out);
        if constexpr (MLP == 0 && F_ == 54) yf = y_ld<0>(c, a);
        ilv<4, 5>();
        if constexpr (F_ + 2 < 56 && f + 2 != 28 && f + 2 >= 4) h_check<MLP, F_ + 2>(c, hb[F_ + 2], hb[F_ + 3]);
        if constexpr (F_ == GC) gate_wait<MLP>(c, a, 1, gate);
        SB();
    });
    static_assert(GC + 2 + HR <= 32 && (GC & 1) == 0 && (HR & 1) == 0, "half B's first foreign fragment is requested behind the gate");
    STAMP2((MLP ? PS1 : PS0) + 29);
    if constexpr (MLP == 0) STAMP4(0);
    red_write<1, 1>(c, a, acc[1]); red_write<1, 2>(c, a, acc[1]); red_write<1, 3>(c, a, acc[1]);
    if constexpr (MLP == 0) sfor<0, 4>([&](auto IC) { gath_ld<0, 0, decltype(IC)::value>(c, a, gs); });
    bar();
    if constexpr (MLP == 0) STAMP4(1);
    f32x4 t[6];
    red_read<1>(c, a, t);
    o[0] = (f32x4{acc[1][0][0], acc[1][0][1], acc[1][0][2], acc[1][0][3]} + t[0]) + (t[2] + t[4]);
    o[1] = (f32x4{acc[1][0][4], acc[1][0][5], acc[1][0][6], acc[1][0][7]} + t[1]) + (t[3] + t[5]);
    post_half<0>(o[0], rb_, xres[1]); post_half<1>(o[1], rb_, xres[1]);
    if constexpr (MLP == 0) {
        frag_sums(xres[1], s1, s2); part_put(c, a, L_S2, s1, s2);
        STAMP4(2);
        bar();
        stat_pub<1, 0>(c, a, L_S2, 1);
        STAMP4(3);
        gath_wait<0, 0>(c, a, gs);
        gath_ln(gs, s1, s2);
        x3_make(xres[0], yf, s1, s2);
        x3_store<0>(c, a, xres[0]);
        STAMP4(4);
        yf = y_ld<1>(c, a);
        __builtin_amdgcn_s_sleep(SLP1);
        v4u x[7];
        unsigned spins = 0;
        NOUNROLL do {
            MEMBAR();
            sfor<0, 4>([&](auto IC) { gath_ld<1, 0, decltype(IC)::value>(c, a, gs); });
            sfor<0, 7>([&](auto IC) { x[decltype(IC)::value] = x3_ld1<0, decltype(IC)::value>(c, a); });
        } while ((gath_bad(c, gs) || x3_bad(x)) && !spin_fail(c, spins, 4u));
        STAMP4(5);
        gath_ln(gs, s1, s2);
        x3_make(xres[1], yf, s1, s2);
        x3_store<1>(c, a, xres[1]);
        sfor<0, 7>([&](auto IC) { x3_put1<0, decltype(IC)::value>(c, a, x[decltype(IC)::value]); });
    } else out_store<1>(c, a, xres[1], out);
}

__global__ void __launch_bounds__(256, 1) fused_kernel(const u16* __restrict__ x0, const u16* __restrict__ kin, const u16* __restrict__ vin,
                                                       const u16* __restrict__ sk, const u16* __restrict__ hk, const u16* __restrict__ Dv,
                                                       const u16* __restrict__ w1, const u16* __restrict__ w2,
                                                       unsigned char* __restrict__ ws, u16* __restrict__ out) {
    extern __shared__ __attribute__((aligned(16))) char smem[];
    Ctx c;
    c.L = (LAS char*)smem;
    c.lbase = (u32)(uintptr_t)c.L;
    const int b = blockIdx.x, tid = threadIdx.x;
    c.w = __builtin_amdgcn_readfirstlane(tid >> 6);
    c.rws = mkrs(ws, WS_BYTES);
    c.tmo = (GAS u32*)(ws + 256);
    STAMP(0);
    STAMP3(0);

    if (b < 32) {
        const v2u ep = __builtin_amdgcn_raw_buffer_load_b64(c.rws, 0, OFF_EP, AUX_SC1);
        const int row = b * 64 + (tid >> 2), part = tid & 3;
        v4u xr[14];
#pragma unroll
        for (int i = 0; i < 14; ++i) xr[i] = *(const v4u*)(x0 + row * D + (i * 4 + part) * 8);
        MEMBAR();
        {
            unsigned long long pc; u32 back, len;
            asm volatile("s_getpc_b64 %0\n.Lfk_here:\n\ts_mov_b32 %1, .Lfk_here-.Lfk_begin\n\ts_mov_b32 %2, .Lfk_end-.Lfk_begin" : "=s"(pc), "=s"(back), "=s"(len));
            if (c.w < 2) touch(c.lbase + L_JK, (tid & 63) * 128, mkrs((const void*)(pc - back), len), ((b >> 3) * 2 + c.w) * 8192);
        }
        float s1 = 0.f, s2 = 0.f;
        const u32 piv = xr[0].x & 0xffffu;
        (void)piv;
#pragma unroll
        for (int i = 0; i < 14; ++i)
#pragma unroll
            for (int e = 0; e < 4; ++e) { s1 = dot2(xr[i][e], 0x3f803f80u, s1); s2 = dot2(xr[i][e], xr[i][e], s2); }
        s1 += __shfl_xor(s1, 1); s2 += __shfl_xor(s2, 1); s1 += __shfl_xor(s1, 2); s2 += __shfl_xor(s2, 2);
        const float m = s1 * (1.0f / D);
        const u32 mine = pk(m, ln_sd(fmaxf(__builtin_fmaf(-m, m, s2 * (1.0f / D)), 0.f)));
        const u32 other = __shfl_down(mine, 4);
        if ((tid & 7) == 0) st128(v4u{mine, ep.x + 1u, other, ep.y + 1u}, c.rws, (u32)(b * 32 + (tid >> 3)) * 16, OFF_ST0);
        const int rg = tid >> 3, c8 = min(tid & 7, 6);
        v4u wr[14];
#pragma unroll
        for (int i = 0; i < 14; ++i) wr[i] = *(const v4u*)(w1 + (rg * 14 + i) * F + b * 56 + c8 * 8);
        float cs[8] = {0, 0, 0, 0, 0, 0, 0, 0};
#pragma unroll
        for (int i = 0; i < 14; ++i)
#pragma unroll
            for (int e = 0; e < 4; ++e) { cs[2 * e] += bflo(wr[i][e]); cs[2 * e + 1] += bfhi(wr[i][e]); }
        LAS float* sf = (LAS float*)c.L;
#pragma unroll
        for (int e = 0; e < 8; ++e) sf[rg * 65 + (tid & 7) * 8 + e] = cs[e];
        bar();
        if (tid < 56) {
            float t = 0.f;
#pragma unroll
            for (int i = 0; i < 32; ++i) t += sf[i * 65 + tid];
            const float t2 = __shfl_down(t, 1);
            if ((tid & 1) == 0) st128(v4u{__float_as_uint(t), ep.x + 1u, __float_as_uint(t2), ep.y + 1u}, c.rws, (u32)(b * 28 + (tid >> 1)) * 16, OFF_CSG);
        }
        MEMBAR();
        {
            const __amdgpu_buffer_rsrc_t rw1 = mkrs(w1, D * F * 2), rw2 = mkrs(w2, D * F * 2);
#pragma unroll
            for (int i = 0; i < 6; ++i) touch(c.lbase + L_JK, (tid & 63) * 128, rw1, (u32)(112 + i * 16 + (b >> 3) * 4 + c.w) * 8192);
#pragma unroll
            for (int i = 0; i < 13; ++i) touch(c.lbase + L_JK, (tid & 63) * 128, rw2, (u32)(i * 16 + (b >> 3) * 4 + c.w) * 8192);
        }
        WAIT_VM(0);
        return;
    }
    asm volatile(".Lfk_begin:");
    const int wb = b - 32;
    { const int xcd = wb & 7, idx = wb >> 3; c.grp = xcd * 4 + idx / 7; c.j = idx % 7; }
    c.gi = c.grp & 3;
    const int w = c.w;
    c.rw1 = mkrs(w1, D * F * 2); c.rw2 = mkrs(w2, D * F * 2);
    c.kb0 = (w + c.gi + 2) & 3;
    const int cp = (wb & 7) * 28 + (wb >> 3);
    W1T W1; W2T W2;

    u32 dval;
    {
        const int g = wb % 7, rb = wb / 7, lane = tid & 63;
        const int rg = (lane & 7) * 2 + ((lane >> 4) & 1), cl = ((lane >> 3) & 1) + 2 * (lane >> 5), cq = 4 * w + cl;
        const __amdgpu_buffer_rsrc_t rk = mkrs(kin, TSZ), rv = mkrs(vin, TSZ), rx = mkrs(x0, TSZ), rh = mkrs(hk, TSZ), rs_ = mkrs(sk, D * 8), rd = mkrs(Dv, D * 2);
        STAMP3(1);
        const u32 tb = (u32)((rb * 64) * 896 + g * 128);
        const u32 tv0 = (u32)((lane >> 3) * 896) + (((lane & 7) ^ ((lane >> 5) + 2 * w)) & 7) * 16;
        dma16<false>(c.lbase + T_X + w * 1024, tv0, rx, tb + w * 7168);
        dma16<false>(c.lbase + T_X + (w + 4) * 1024, tv0, rx, tb + (w + 4) * 7168);
        dma16<false>(c.lbase + T_K + w * 1024, tv0 + tb + (w - 1) * 7168, rk, 0u);
        dma16<false>(c.lbase + T_K + (w + 4) * 1024, tv0, rk, tb + (w + 3) * 7168);
        dma16<false>(c.lbase + T_V + w * 1024, tv0, rv, tb + w * 7168);
        dma16<false>(c.lbase + T_V + (w + 4) * 1024, tv0, rv, tb + (w + 4) * 7168);
        if (w == 0) dma16<false>(c.lbase + T_K + 8192, tv0, rk, tb + 7 * 7168);
        else dma16<false>(c.lbase + T_S + (w - 1) * 1024, (u32)lane * 16, rs_, g * 512);
        v4u ep4 = ld128_asm<true>(c.rws, 0u, OFF_EP);
        v4u he[2];
        he[0] = ld128_asm<false>(rh, tid * 16, cp * 8192); he[1] = ld128_asm<false>(rh, tid * 16, cp * 8192 + 4096);
        v4u dv4 = ld128_asm<false>(rd, 0u, cp * 4);
        {
            const WSt wt = w_st<0>(c, lane);
            sfor<0, NW0>([&](auto NC) { w_dma<decltype(NC)::value>(c, wt, w_soff<decltype(NC)::value>(c)); });
        }
        if constexpr (TLBW) {
            if (w == 0) touch(c.lbase + L_JK + 3072, (u32)lane * 524288u, c.rws, 4096u);
            else touch(c.lbase + L_JK + 3072, (u32)lane * 262144u, w == 1 ? c.rw1 : w == 2 ? c.rw2 : mkrs(out, TSZ), 0u);
        }
        if constexpr (NT0) touch(c.lbase + L_JK + 3072, (u32)lane * 128, c.rw1, (u32)((wb >> 3) * 4 + w) * 8192);
        STAMP3(2);
        asm volatile("s_waitcnt vmcnt(%1)" : "+v"(ep4) : "i"(3 + NT0 + TLBW + 2 * NW0) : "memory");
        c.tagA = __builtin_amdgcn_readfirstlane(ep4.x) + 1u; c.tagB = __builtin_amdgcn_readfirstlane(ep4.y) + 1u;
        bar();
        STAMP(1); STAMP3(3);
        const u32 a1 = (u32)(rg * 512 + (((cq >> 1) ^ rg) & 7) * 16 + (cq & 1) * 8);
        const u32 ak1 = (u32)(rg * 512 + 1024 + (((cq >> 1) ^ (rg + 2)) & 7) * 16 + (cq & 1) * 8);
        const u32 ak0 = (u32)(rg * 512 + 512 + (((cq >> 1) ^ (rg + 1)) & 7) * 16 + (cq & 1) * 8);
        v2u kr[7], vr[4], xr[4];
#pragma unroll
        for (int i = 0; i < 4; ++i) xr[i] = *(LAS v2u*)(c.L + T_X + a1 + i * 128);
        const v4u w01 = *(LAS v4u*)(c.L + T_S + cq * 32), w23 = *(LAS v4u*)(c.L + T_S + cq * 32 + 16);
#pragma unroll
        for (int i = 0; i < 3; ++i) kr[i] = *(LAS v2u*)(c.L + T_K + ak0 + (i + 1) * 128);
#pragma unroll
        for (int i = 0; i < 4; ++i) { kr[3 + i] = *(LAS v2u*)(c.L + T_K + ak1 + i * 128); vr[i] = *(LAS v2u*)(c.L + T_V + a1 + i * 128); }
#pragma unroll
        for (int h = 0; h < 2; ++h) {
            auto r0 = __builtin_amdgcn_permlane16_swap(__builtin_amdgcn_perm(xr[1][h], xr[0][h], 0x05040100u), __builtin_amdgcn_perm(xr[1][h], xr[0][h], 0x07060302u), false, false);
            auto r1 = __builtin_amdgcn_permlane16_swap(__builtin_amdgcn_perm(xr[3][h], xr[2][h], 0x05040100u), __builtin_amdgcn_perm(xr[3][h], xr[2][h], 0x07060302u), false, false);
            st128(v4u{r0[0], r1[0], r0[1], r1[1]}, c.rws, (u32)((cl * 4 + ((lane >> 4) & 1)) * 4096 + (lane & 7) * 16), OFF_X0G + (g * 64 + 16 * w + 2 * h) * 4096 + rb * 128);
        }
        STAMP3(27);
        v4u kg[4];
#pragma unroll
        for (int e = 0; e < 4; ++e) {
            const v4u& wq = e < 2 ? w01 : w23;
            const float w0 = bflo(wq[(e & 1) * 2]), w1_ = bfhi(wq[(e & 1) * 2]), w2_ = bflo(wq[(e & 1) * 2 + 1]), w3 = bfhi(wq[(e & 1) * 2 + 1]);
            float kk[7], o[4];
#pragma unroll
            for (int i = 0; i < 7; ++i) kk[i] = (e & 1) ? bfhi(kr[i][e >> 1]) : bflo(kr[i][e >> 1]);
#pragma unroll
            for (int i = 0; i < 4; ++i) o[i] = (kk[i] * w3 + kk[i + 1] * w2_ + kk[i + 2] * w1_ + kk[i + 3] * w0) * ((e & 1) ? bfhi(vr[i][e >> 1]) : bflo(vr[i][e >> 1]));
            kg[e] = v4u{pk(o[3], o[2]), c.tagA, pk(o[1], o[0]), c.tagB};
        }
        STAMP3(28);
        asm volatile("s_waitcnt vmcnt(0)\n\ts_barrier" : "+v"(he[0]), "+v"(he[1]), "+v"(dv4), "+v"(kg[0]), "+v"(kg[1]), "+v"(kg[2]), "+v"(kg[3]) :: "memory");
        STAMP3(29);
#pragma unroll
        for (int e = 0; e < 4; ++e) st128(kg[e], c.rws, (u32)(cl * 4 * 8192 + (15 - rg) * 16), OFF_KVG + ((g * 64 + 16 * w + e) * 512 + 496 - 16 * rb) * 16);
        {
            const WSt wt = w_st<0>(c, lane);
            sfor<NW0, NW1>([&](auto NC) { w_dma<decltype(NC)::value>(c, wt, w_soff<decltype(NC)::value>(c)); });
        }
        STAMP(2); STAMP3(4);
        dval = (w >> 1) ? dv4.x >> 16 : dv4.x & 0xffffu;
#pragma unroll
        for (int cc = 0; cc < 2; ++cc) {
            LAS char* cb = c.L + CV_B + cc * CV_CH;
            *(LAS v4u*)(cb + CV_HE + 32 + tid * 16) = he[cc];
            if (tid < 2) { *(LAS v4u*)(cb + CV_HE + tid * 16) = v4u{0, 0, 0, 0}; *(LAS v4u*)(cb + CV_HO + tid * 16) = v4u{0, 0, 0, 0}; }
            if (tid < 32) *(LAS v4u*)(cb + CV_KV + 4096 + tid * 16) = v4u{0, 0, 0, 0};
        }
        bar();
#pragma unroll
        for (int cc = 0; cc < 2; ++cc) {
            const v4u e = he[cc];
            const u32 pv = *(LAS u32*)(c.L + CV_B + cc * CV_CH + CV_HE + 28 + tid * 16);
            *(LAS v4u*)(c.L + CV_B + cc * CV_CH + CV_HO + 32 + tid * 16) = v4u{__builtin_amdgcn_alignbit(e.x, pv, 16), __builtin_amdgcn_alignbit(e.y, e.x, 16),
                                                                            __builtin_amdgcn_alignbit(e.z, e.y, 16), __builtin_amdgcn_alignbit(e.w, e.z, 16)};
        }
        bar();
        STAMP3(5);
        SB();
    }
    {
        const int lane = lane_id(), i16 = lane & 15, q = lane >> 4, cc = w >> 1, p = w & 1, ch = 2 * cp + cc;
        LAS char* cb = c.L + CV_B + cc * CV_CH;
        const int o0 = i16 + 1 + 32 * p + 8 * q;
        LAS char* ab = cb + ((o0 & 1) ? CV_HO + 4 : CV_HE) + (o0 >> 1) * 4;
        v4u A[32];
        v4u AD;
        {
            const int jj = p ? -1 : 15 - i16 - 8 * q;
            const u32 dv = dval << ((jj & 1) * 16);
#pragma unroll
            for (int d = 0; d < 4; ++d) AD[d] = (jj >= 0 && (jj >> 1) == d) ? dv : 0u;
        }
        const Ctx cs_ = fresh(c);
        const u32 wl = wl1_of(cs_, lane);
        const WSt wt = w_st<0>(cs_, lane);
        v4u sg[4], g[4];
        SB();
        sfor<0, 8>([&](auto NC) {
            constexpr int n = decltype(NC)::value;
            if constexpr (n >= 4) sg[n - 4] = ld128_asm<true>(c.rws, tid * 16, OFF_ST0 + (n - 4) * 4096);
#pragma unroll
            for (int t = n * 4; t < n * 4 + 4; ++t) { LAS u32* pp = (LAS u32*)(ab + t * 128); A[t] = v4u{pp[0], pp[1], pp[2], pp[3]}; }
            SB();
        });
        STAMP(3); STAMP3(6);
        {
            __builtin_amdgcn_s_sleep(SLP0);
            unsigned spins = 0;
            auto bad4 = [&](const v4u (&x)[4]) { u32 d = 0; for (int t = 0; t < 4; ++t) d = tag_bad(x[t], c.tagA, c.tagB, d); return ANY(d != 0u); };
            NOUNROLL do {
                MEMBAR();
#pragma unroll
                for (int i = 0; i < 4; ++i) g[i] = ld128(c.rws, tid * 16, OFF_KVG + cp * 16384 + i * 4096);
            } while (bad4(g) && !spin_fail(c, spins, 5u));
            asm volatile("" : "+v"(sg[0]), "+v"(sg[1]), "+v"(sg[2]), "+v"(sg[3]));
            STAMP3(30);
#pragma unroll
            for (int i = 0; i < 4; ++i) *(LAS v2u*)(c.L + CV_B + (i >> 1) * CV_CH + CV_KV + (tid + 256 * (i & 1)) * 8) = v2u{g[i].x, g[i].z};
            if (bad4(sg)) {
                NOUNROLL do {
                    MEMBAR();
#pragma unroll
                    for (int t = 0; t < 4; ++t) sg[t] = ld128(c.rws, tid * 16, OFF_ST0 + t * 4096);
                } while (bad4(sg) && !spin_fail(c, spins, 6u));
            }
#pragma unroll
            for (int t = 0; t < 4; ++t) *(LAS v2u*)(c.L + CV_ST + (tid + 256 * t) * 8) = v2u{sg[t].x, sg[t].z};
        }
        bar();
        STAMP(4); STAMP3(7);
        LAS char* bb = cb + CV_KV + (2032 - 16 * i16 + 32 * p + 8 * q) * 2;
        f32x4 acc[8];
        float xs[4][4], qs[4][4];
        v4u B[32];
        v2u xg[4];
        B[0] = *(LAS v4u*)(bb + 3 * 128); B[1] = *(LAS v4u*)(bb + 2 * 128); B[2] = *(LAS v4u*)(bb + 128);
        const u32 s4 = (u32)(256 * p + 4 * i16 + q), xo = OFF_X0G + ch * 4096;
        SB();
        sfor<0, 32>([&](auto OC) {
            constexpr int oi = decltype(OC)::value, om = oi - 3;
            constexpr int nm = 8 - (om > 0 ? (om + 3) / 4 : 0) + ((om >= 0 && (om & 3) == 0) ? 1 : 0);
            if constexpr (oi % 4 == 0) STAMP3(8 + oi / 4);
            if constexpr (oi + 3 < 32) B[oi + 3] = *(LAS v4u*)(bb - (om + 3) * 128);
#pragma unroll
            for (int cb_ = 7; cb_ >= 0; --cb_)
                if (4 * cb_ - om >= 0) acc[cb_] = mfma16(A[4 * cb_ - om], B[oi], oi == 0 ? f32x4{0, 0, 0, 0} : acc[cb_]);
            if constexpr (om >= 0 && (om & 3) == 0) acc[om >> 2] = mfma16(AD, B[oi], acc[om >> 2]);
            if constexpr (oi < 2) {
                asm volatile("buffer_load_dwordx2 %0, %2, %3, %4 offen offset:%5 sc1\n\tbuffer_load_dwordx2 %1, %2, %3, %4 offen offset:%6 sc1"
                             : "=&v"(xg[2 * oi]), "=&v"(xg[2 * oi + 1]) : "v"(s4 * 8), "s"(c.rws), "s"(xo), "i"(oi * 1024), "i"(oi * 1024 + 512) : "memory");
            }
            if constexpr (oi >= 5 && oi < 9) sg[oi - 5] = *(LAS v4u*)(c.L + CV_ST + s4 * 16 + (oi - 5) * 1024);
            if constexpr (oi < NW1) w_read1<oi>(cs_, wl, W1, W2);
            if constexpr (oi >= 2 && (oi & 1) == 0 && NW1 + (oi - 2) / 2 < 12) w_dma<NW1 + (oi - 2) / 2>(cs_, wt, w_soff<NW1 + (oi - 2) / 2>(cs_));
            if constexpr (oi == 9) asm volatile("s_waitcnt vmcnt(8)" : "+v"(xg[0]), "+v"(xg[1]), "+v"(xg[2]), "+v"(xg[3]) :: "memory");
            if constexpr (oi >= 9 && oi < 17) {
                constexpr int t = (oi - 9) >> 1, hf = (oi - 9) & 1;
                LV(xg[t]); LV(sg[t]);
                const u32 xw = xg[t][hf];
                const u32 sa = sg[t][2 * hf], sb = sg[t][2 * hf + 1];
                xs[t][2 * hf] = bflo(xw); xs[t][2 * hf + 1] = bfhi(xw);
                qs[t][2 * hf] = (xs[t][2 * hf] - bflo(sa)) * __builtin_amdgcn_rcpf(bfhi(sa));
                qs[t][2 * hf + 1] = (xs[t][2 * hf + 1] - bflo(sb)) * __builtin_amdgcn_rcpf(bfhi(sb));
                asm volatile("" : "+v"(qs[t][2 * hf]), "+v"(qs[t][2 * hf + 1]));
            }
            if constexpr (oi == 21 || oi == 23) {
                const v4u ff = {~0u, ~0u, ~0u, ~0u};
                st128(ff, c.rws, lane * 16, OFF_X3 + (cs_.grp * 56 + (oi - 21) * 14 + 4 * cs_.j + cs_.w) * 1024);
            }
            ilv<nm, 2>();
            SB();
        });
        STAMP(5); STAMP3(16);
        LAS char* ex = c.L;
        f32x4 fin[4];
        if (p == 0) {
#pragma unroll
            for (int t = 0; t < 4; ++t) { *(LAS f32x4*)(ex + w * 4096 + t * 1024 + lane * 16) = acc[4 + t]; fin[t] = acc[t]; }
        } else {
#pragma unroll
            for (int t = 0; t < 4; ++t) { *(LAS f32x4*)(ex + w * 4096 + t * 1024 + lane * 16) = acc[t]; fin[t] = acc[4 + t]; }
        }
        bar();
        STAMP3(17);
        const u32 toff = (u32)((32 * p + 8 * (q & 1) + (i16 >> 1)) * 28672 + ch * 64 + (i16 & 1) * 32 + (q >> 1) * 16);
        v2u y[4], x1[4];
#pragma unroll
        for (int t = 0; t < 4; ++t) {
            fin[t] += *(LAS f32x4*)(ex + (w ^ 1) * 4096 + t * 1024 + lane * 16);
            y[t] = v2u{pk(fin[t][0], fin[t][1]), pk(fin[t][2], fin[t][3])};
            x1[t] = v2u{pk(__builtin_fmaf(qs[t][0], bflo(y[t].x), xs[t][0]), __builtin_fmaf(qs[t][1], bfhi(y[t].x), xs[t][1])),
                        pk(__builtin_fmaf(qs[t][2], bflo(y[t].y), xs[t][2]), __builtin_fmaf(qs[t][3], bfhi(y[t].y), xs[t][3]))};
        }
        auto merge = [&](const v2u& a, const v2u& b_) {
            auto r0 = __builtin_amdgcn_permlane16_swap(a.x, b_.x, false, false);
            auto r1 = __builtin_amdgcn_permlane16_swap(a.y, b_.y, false, false);
            return v4u{r0[0], r1[0], r0[1], r1[1]};
        };
        st128(merge(x1[0], x1[1]), c.rws, toff, OFF_X1);
        st128(merge(x1[2], x1[3]), c.rws, toff, OFF_X1 + 16 * 28672);
        st128(merge(y[0], y[1]), c.rws, toff, OFF_YT);
        st128(merge(y[2], y[3]), c.rws, toff, OFF_YT + 16 * 28672);
        STAMP(6); STAMP3(18);
        sfor<12, 12 + NW1>([&](auto NC) { w_dma<decltype(NC)::value>(cs_, wt, w_soff<decltype(NC)::value>(cs_)); });
        WAIT_VM(6 + 2 * NW1);
        sfor<NW1, 12>([&](auto NC) { w_read1<decltype(NC)::value>(cs_, wl, W1, W2); });
        WAIT_LGKM(0);
        sfor<12 + NW1, 20>([&](auto NC) { w_dma<decltype(NC)::value>(cs_, wt, w_soff<decltype(NC)::value>(cs_)); });
        STAMP3(19);
        WAIT_VM(16);
        bar();
        if (tid < 8) __builtin_amdgcn_raw_buffer_store_b64(v2u{c.tagA, c.tagB}, c.rws, (u32)(tid * 2048 + wb * 8), OFF_FL, AUX_SC1);
        MEMBAR();
        STAMP(7);
        sfor<20, 24>([&](auto NC) { w_dma<decltype(NC)::value>(cs_, wt, w_soff<decltype(NC)::value>(cs_)); });
        WAIT_VM(12);
        sfor<12, 18>([&](auto NC) { w_read1<decltype(NC)::value>(cs_, wl, W1, W2); });
        WAIT_LGKM(0);
        sfor<24, 28>([&](auto NC) { w_dma<decltype(NC)::value>(cs_, wt, w_soff<decltype(NC)::value>(cs_)); });
        STAMP3(21);
    }
    Ad ad;
    ad.l16 = lane_id() * 16;
    ad.hst = OFF_H + (c.grp * 224 + 16 * c.j + 4 * w) * 1024;
    ad.x3o = OFF_X3 + (c.grp * 56 + 4 * c.j + w) * 1024;
    ad.sto = OFF_ST + c.grp * 2 * ST_HG;
    asm volatile("" : "+s"(ad.hst), "+s"(ad.x3o), "+s"(ad.sto));
    const u32 swl = wl2_of(c, ad.l16 >> 4);
    const WSt w2t = w_st<28>(c, ad.l16 >> 4);
    u32 wso[7];
#pragma unroll
    for (int m = 0; m < 7; ++m) {
        const int mm = member(c, m);
        wso[m] = (256 * mm + 64 * w) * (D * 2) + 128 * c.j; ad.so[m] = ad.hst + (mm - c.j) * 16384;
        asm volatile("" : "+s"(wso[m]), "+s"(ad.so[m]));
    }
    auto w2_dma = [&](auto NC) { constexpr int n = decltype(NC)::value; w_dma<n>(c, w2t, wso[k_mem(w2_step_of(n))] + k_sub(w2_step_of(n)) * 16 * (D * 2)); };
    {
        sfor<28, 30>(w2_dma);
        WAIT_VM(12);
        w_read<18, 24>(c, W1, W2);
        STAMP(8); STAMP3(22);
        if (w == 1) {
            v4u g[2];
            unsigned spins = 0;
            NOUNROLL for (;;) {
                MEMBAR();
                g[0] = ld128(c.rws, ad.l16, OFF_CSG + c.j * 2048); g[1] = ld128(c.rws, ad.l16 + 1024, OFF_CSG + c.j * 2048);
                if (!ANY(tag_bad(g[0], c.tagA, c.tagB, tag_bad(g[1], c.tagA, c.tagB, 0u)) != 0u)) break;
                if (spin_fail(c, spins, 8u)) break;
            }
            *(LAS v2u*)(c.L + L_CS + (ad.l16 >> 1)) = v2u{g[0].x, g[0].z}; *(LAS v2u*)(c.L + L_CS + 512 + (ad.l16 >> 1)) = v2u{g[1].x, g[1].z};
        }
        if (w == 0) {
            unsigned spins = 0;
            NOUNROLL for (;;) {
                MEMBAR();
                const v4u f0 = ld128(c.rws, ad.l16, OFF_FL + (wb & 7) * 2048);
                const v4u f1 = ld128(c.rws, 1024 + min(ad.l16, 752u), OFF_FL + (wb & 7) * 2048);
                const u32 d = xor_or(f0.x, c.tagA, xor_or(f0.z, c.tagA, xor_or(f1.x, c.tagA, xor_or(f1.z, c.tagA, tag_bad(f0, c.tagB, c.tagB, tag_bad(f1, c.tagB, c.tagB, 0u))))));
                if (!ANY(d != 0u)) break;
                if (spin_fail(c, spins, 7u)) break;
            }
            if (wb == 0 && ad.l16 == 0) __builtin_amdgcn_raw_buffer_store_b64(v2u{c.tagA, c.tagB}, c.rws, 0, OFF_EP, AUX_SC1);
        }
        bar();
        STAMP(9); STAMP3(23);
        {
            const u32 xb = c.lbase + L_X + w * 7168, xs_ = OFF_X1 + c.grp * 57344 + w * 7168;
            sfor<0, 14>([&](auto IC) {
                constexpr int i = decltype(IC)::value, o = (i / 7) * 28672 + (i % 7) * 1024;
                dma16<true>(xb + o, ad.l16, c.rws, xs_ + o);
            });
        }
        STAMP3(24);
        WAIT_VM(14);
        w_read<24, 28>(c, W1, W2);
        w_read1<28>(c, swl, W1, W2); w_read1<29>(c, swl, W1, W2);
        w_neg<28>(W2); w_neg<29>(W2);
        WAIT_VM(7);
        bar();
        STAMP(10); STAMP3(25);
    }
    STAMP3(26);
    pin_a<0, 24>(W1);
    v4u hb[56], xres[2];
    G1Out ga, gb;
    v2u gate;
    f32x4 csq[3];
    St1 st;
    ad.ca = L_CS + (w * 64 + (ad.l16 >> 9) * 4) * 4;
    using M0 = std::integral_constant<int, 0>; using M1 = std::integral_constant<int, 1>;
    {
        st.ad = xtr_of(ad.l16 >> 4) + w * 7168;
        auto stats = [&](auto MC, auto HC, auto FC, G1Out& o) {
            constexpr int H = decltype(HC)::value, f = decltype(FC)::value;
            constexpr bool TR = decltype(MC)::value == 0;
            if constexpr (f == 0) { st.s1 = 0.f; st.s2 = 0.f; }
            if constexpr (f >= 2 && f < 23) { constexpr int ph = (f - 2) % 3, k = (f - 2) / 3; st1_acc<k, ph * 3, ph == 2 ? 8 : ph * 3 + 3>(st); }
            if constexpr (f % 3 == 0 && f < 21) st1_read<TR, H, f / 3>(c, st);
            if constexpr (f == 23) part_put(c, ad, L_SP + H * 1024, st.s1, st.s2);
            if constexpr (f == 24) { SB(); asm volatile("s_waitcnt lgkmcnt(%0)\n\ts_barrier" :: "i"(TR ? 2 : 1) : "memory"); }
            if constexpr (f == 25) { const f32x2 t = part_get(c, ad, L_SP + H * 1024); ln_scalars(t[0], t[1], o.mb, o.r); asm volatile("" : "+v"(o.mb), "+v"(o.r)); }
        };
        auto stage = [&](auto GC_, auto XC) {
            constexpr int g = decltype(GC_)::value, extra = decltype(XC)::value;
            if constexpr (g >= 15 && g < 43 && (g & 1)) w_neg<30 + (g - 15) / 2>(W2);
            if constexpr (g == 41) *(LAS v4u*)(c.L + (ad.l16 + w * 1024) + L_JK) = W2[15][1];
            if constexpr (g >= 13 && g < 41 && (g & 1)) { WAIT_VM(12 + extra); w_read1<30 + (g - 13) / 2>(c, swl, W1, W2); }
            if constexpr (g < 52 && (g & 1) == 0) w2_dma(std::integral_constant<int, 30 + g / 2>{});
        };
        constexpr unsigned E2 = 0xAAAAAAAu;
        g1_main<true, 0, PS0, 0x9AAAAAAu>(c, ad, W1, ga.acc, [&](auto FC) {
            constexpr int f = decltype(FC)::value;
            stage(FC, M0{});
            stats(M0{}, M0{}, FC, ga);
            if constexpr (f >= 26) csq[f - 26] = epi_cs(c, ad, f - 26);
        });
        STAMP(11);
        auto side_b = [&](auto MC, auto FC) {
            constexpr int f = decltype(FC)::value, M = decltype(MC)::value;
            if constexpr (f < 8) g1_epi<0, M, f>(c, ad, ga, csq[f % 3], hb[f >> 1]);
            if constexpr (f < 8 && (f & 1)) h_store<0, f / 2>(c, ad, hb[f >> 1]);
            if constexpr (f < 6) csq[(f + 2) % 3] = epi_cs(c, ad, f + 2);
            if constexpr (f == 18) gate = gate_ld<true>(c, ad, 0);
            if constexpr (f == 19) xres[0] = xres_ld<0, M>(c, ad);
            if constexpr (f == 23) {
                SB();
                asm volatile("s_waitcnt vmcnt(%1)" : "+v"(gate) : "i"(M == 0 ? 4 : 0) : "memory");
                gate_wait<M>(c, ad, 0, gate);
            }
            if constexpr (f >= 24) hb[f - 20] = h_next<f - 20>(c, ad);
            if constexpr (f >= 26) csq[f - 26] = epi_cs(c, ad, f - 26);
        };
        const u32 ra = L_W + w * 24576 + 1024 + ad.l16;
        v4u rm;
        g1_main<true, 1, PS0 + 7, 0x9AAAAAAu>(c, ad, W1, gb.acc, [&](auto FC) {
            constexpr int f = decltype(FC)::value;
            stage(std::integral_constant<int, 28 + f>{}, std::integral_constant<int, (f < 8 ? f / 2 : 4)>{});
            if constexpr (f >= 16 && f < 28 && (f & 1) == 0) { LV(rm); *(LAS v4u*)(c.L + ra + ((8 + (f - 16) / 2) % 12) * 2048) = rm ^ SGN; }
            if constexpr (f >= 14 && f < 26 && (f & 1) == 0) { WAIT_VM(f < 24 ? 14 : 12); rm = *(LAS v4u*)(c.L + ra + ((8 + (f - 14) / 2) % 12) * 2048); }
            stats(M0{}, M1{}, FC, gb);
            side_b(M0{}, FC);
        });
        STAMP(12);
        {
            WAIT_VM(4);
            v4u t[6];
#pragma unroll
            for (int i = 0; i < 6; ++i) t[i] = *(LAS v4u*)(c.L + ra + ((14 + i) % 12) * 2048);
#pragma unroll
            for (int i = 0; i < 6; ++i) *(LAS v4u*)(c.L + ra + ((14 + i) % 12) * 2048) = t[i] ^ SGN;
            SB();
        }
        pin_a<0, 24>(W1);
        STAMP2(PS0 + 14);
        STAMP(13);
        g2_pair<0>(c, ad, W2, hb, gb, csq, ga.r, xres, c.rws);
        pin_a<0, 24>(W1);
        STAMP(14);
        v4u t[7];
        st.ad = ad.l16 + w * 7168;
        STAMP4(6);
        bar();
        STAMP4(7);
        STAMP(17);
        g1_main<false, 0, PS1, 0x9AAAAAAu>(c, ad, W1, ga.acc, [&](auto FC) {
            constexpr int f = decltype(FC)::value;
            stats(M1{}, M0{}, FC, ga);
            if constexpr (f >= 12 && f < 19) t[f - 12] = x3_ld1<1, f - 12>(c, ad);
            if constexpr (f == 24) x3_wait<1>(c, ad, t);
            if constexpr (f >= 25) { x3_put1<1, 2 * (f - 25)>(c, ad, t[2 * (f - 25)]); x3_put1<1, 2 * (f - 25) + 1>(c, ad, t[2 * (f - 25) + 1]); }
            if constexpr (f == 27) x3_put1<1, 6>(c, ad, t[6]);
            if constexpr (f >= 26) csq[f - 26] = epi_cs(c, ad, f - 26);
        });
        STAMP4(8);
        STAMP4(9);
        STAMP4(10);
        STAMP(18);
        bar();
        STAMP4(11);
        g1_main<false, 1, PS1 + 7, 0x9AAAAAAu>(c, ad, W1, gb.acc, [&](auto FC) {
            stats(M1{}, M1{}, FC, gb);
            side_b(M1{}, FC);
        });
        (void)E2;
        STAMP(19);
        STAMP2(PS1 + 14);
        STAMP(20);
        const __attribute__((address_space(4))) char* ka = (const __attribute__((address_space(4))) char*)__builtin_amdgcn_kernarg_segment_ptr();
        asm volatile("" : "+s"(ka));
        g2_pair<1>(c, ad, W2, hb, gb, csq, ga.r, xres, mkrs(*(void* const __attribute__((address_space(4)))*)(ka + 72), TSZ));
        STAMP(21);
    }
    asm volatile(".Lfk_end:");
}

extern "C" void kernel_launch(void* const* d_in, const int* in_sizes, int n_in,
                              void* d_out, int out_size, void* d_ws, size_t ws_size,
                              hipStream_t stream) {
    (void)hipFuncSetAttribute((const void*)fused_kernel, hipFuncAttributeMaxDynamicSharedMemorySize, L_TOT);
    fused_kernel<<<256, 256, L_TOT, stream>>>((const u16*)d_in[0], (const u16*)d_in[1], (const u16*)d_in[2], (const u16*)d_in[3], (const u16*)d_in[4],
                                              (const u16*)d_in[5], (const u16*)d_in[7], (const u16*)d_in[8], (unsigned char*)d_ws, (u16*)d_out);
}
```

```cpp
#include <hip/hip_runtime.h>
#include <stdint.h>
#include <type_traits>

typedef unsigned short u16;
typedef unsigned u32;
typedef u32 v4u __attribute__((ext_vector_type(4)));
typedef u32 v2u __attribute__((ext_vector_type(2)));
typedef float f32x2 __attribute__((ext_vector_type(2)));
typedef float f32x4 __attribute__((ext_vector_type(4)));
typedef float f32x16 __attribute__((ext_vector_type(16)));
typedef __bf16 bf16x8 __attribute__((ext_vector_type(8)));
typedef __bf16 bf16x2 __attribute__((ext_vector_type(2)));
typedef short s16x4 __attribute__((ext_vector_type(4)));
typedef short s16x2 __attribute__((ext_vector_type(2)));
typedef unsigned short u16x2 __attribute__((ext_vector_type(2)));
#define LAS __attribute__((address_space(3)))
#define GAS __attribute__((address_space(1)))
#define DI __device__ __forceinline__
#define RLX_AGENT __ATOMIC_RELAXED, __HIP_MEMORY_SCOPE_AGENT
#define SB() __builtin_amdgcn_sched_barrier(0)
#define NOUNROLL _Pragma("unroll 1")
#define MEMBAR() asm volatile("" ::: "memory")
template <class T> __device__ __forceinline__ void LV(T& x) { asm volatile("" : "+v"(x)); }
template <class T> __device__ __forceinline__ void LA(T& x) { asm volatile("" : "+a"(x)); }
#define ANY(x) (__builtin_amdgcn_ballot_w64(x) != 0ull)

constexpr int S = 2048, D = 448, F = 1792;
constexpr float EPS = 1e-5f;
constexpr u32 TSZ = S * D * 2;
constexpr u32 OFF_EP  = 0;
constexpr u32 OFF_FL  = 4096;
constexpr u32 OFF_ST0 = OFF_FL + 16384;
constexpr u32 OFF_CSG = OFF_ST0 + 16384;
constexpr u32 OFF_ST  = OFF_CSG + 16384;
constexpr u32 OFF_KVG = 1048576;
constexpr u32 OFF_X0G = OFF_KVG + 2 * TSZ;
constexpr u32 OFF_X1  = OFF_X0G + 2 * TSZ;
constexpr u32 OFF_YT  = OFF_X1 + TSZ;
constexpr u32 OFF_X3  = OFF_YT + TSZ;
constexpr u32 OFF_H   = OFF_X3 + TSZ;
constexpr u32 WS_BYTES = OFF_H + 64 * 112 * 1024;
static_assert(OFF_ST + 64 * 24 * 512 <= OFF_KVG, "ws map");
#define STAMP(i) do {} while (0)
#define STAMP2(i) do {} while (0)
#define STAMP4(i) do {} while (0)
#define STAMP3(i) do {} while (0)
constexpr int RSRC_W3 = 0x00020000;
constexpr int AUX_SC1 = 16;
constexpr unsigned SPIN_CAP = 1u << 14;
constexpr u32 SGN = 0x80008000u;

template <int I, int N, class Fn> DI void sfor(Fn&& fn) { if constexpr (I < N) { fn(std::integral_constant<int, I>{}); sfor<I + 1, N>(fn); } }

DI float bflo(u32 x) { return __uint_as_float(x << 16); }
DI float bfhi(u32 x) { return __uint_as_float(x & 0xffff0000u); }
DI u32 pk(float a, float b) { f32x2 v = {a, b}; return __builtin_bit_cast(u32, __builtin_convertvector(v, bf16x2)); }
DI float rbf(float a) { return bflo(pk(a, a)); }
DI __amdgpu_buffer_rsrc_t mkrs(const void* p, u32 bytes) { return __builtin_amdgcn_make_buffer_rsrc((void*)p, 0, (int)bytes, RSRC_W3); }
DI f32x4 mfma16(v4u a, v4u b, f32x4 c) { return __builtin_amdgcn_mfma_f32_16x16x32_bf16(__builtin_bit_cast(bf16x8, a), __builtin_bit_cast(bf16x8, b), c, 0, 0, 0); }
DI f32x16 mfma32(v4u a, v4u b, f32x16 c) { return __builtin_amdgcn_mfma_f32_32x32x16_bf16(__builtin_bit_cast(bf16x8, a), __builtin_bit_cast(bf16x8, b), c, 0, 0, 0); }
DI f32x16 zero16() { f32x16 z; for (int i = 0; i < 16; ++i) z[i] = 0.f; return z; }
DI v2u trd(LAS char* p) { return __builtin_bit_cast(v2u, __builtin_amdgcn_ds_read_tr16_b64_v4i16((LAS s16x4*)p)); }
DI v4u trd2(LAS char* p, int second) { v2u a = trd(p), b = trd(p + second); return v4u{a.x, a.y, b.x, b.y}; }
DI float dot2(u32 a, u32 b, float c) { return __builtin_amdgcn_fdot2_f32_bf16(__builtin_bit_cast(bf16x2, a), __builtin_bit_cast(bf16x2, b), c, false); }
DI v4u ld128(__amdgpu_buffer_rsrc_t rs, u32 vo, u32 so) { return __builtin_amdgcn_raw_buffer_load_b128(rs, vo, so, AUX_SC1); }
template <bool SC1, int OFF = 0> DI v4u ld128_asm(__amdgpu_buffer_rsrc_t rs, u32 vo, u32 so) {
    v4u r;
    if constexpr (SC1) asm volatile("buffer_load_dwordx4 %0, %1, %2, %3 offen offset:%4 sc1" : "=v"(r) : "v"(vo), "s"(rs), "s"(so), "i"(OFF) : "memory");
    else asm volatile("buffer_load_dwordx4 %0, %1, %2, %3 offen offset:%4" : "=v"(r) : "v"(vo), "s"(rs), "s"(so), "i"(OFF) : "memory");
    return r;
}
template <int OFF = 0> DI void st128(v4u v, __amdgpu_buffer_rsrc_t rs, u32 vo, u32 so) {
    asm volatile("buffer_store_dwordx4 %0, %1, %2, %3 offen offset:%4 sc1\n\ts_nop 1" :: "v"(v), "v"(vo), "s"(rs), "s"(so), "i"(OFF) : "memory");
}
DI int lane_id() { int l; asm volatile("v_mbcnt_lo_u32_b32 %0, -1, 0\n\tv_mbcnt_hi_u32_b32 %0, -1, %0" : "=v"(l)); return l; }
DI float hsum(float a) {
    const u32 v = __float_as_uint(a);
    auto t = __builtin_amdgcn_permlane32_swap(v, v, false, false);
    return __uint_as_float(t[0]) + __uint_as_float(t[1]);
}
DI float ln_sd(float var) { return rbf(__builtin_amdgcn_sqrtf(rbf(rbf(var) + EPS))); }
DI void ln_scalars(float s1, float s2, float& m, float& r) {
    m = s1 * (1.0f / D);
    r = __builtin_amdgcn_rsqf(fmaxf(__builtin_fmaf(-m, m, s2 * (1.0f / D)), 0.f) + EPS);
}
DI u32 xor_or(u32 a, u32 b, u32 c) { return __builtin_amdgcn_bitop3_b32(a, b, c, 0xBE); }
DI u32 tag_bad(const v4u& g, u32 tA, u32 tB, u32 acc) { return xor_or(g.w, tB, xor_or(g.y, tA, acc)); }
template <bool SC1> DI void dma16(u32 ldsaddr, u32 voff, __amdgpu_buffer_rsrc_t rs, u32 soff) {
    if constexpr (SC1) asm volatile("s_mov_b32 m0, %0\n\ts_nop 0\n\tbuffer_load_dwordx4 %1, %2, %3 offen sc1 lds" :: "s"(ldsaddr), "v"(voff), "s"(rs), "s"(soff) : "memory", "m0");
    else asm volatile("s_mov_b32 m0, %0\n\ts_nop 0\n\tbuffer_load_dwordx4 %1, %2, %3 offen lds" :: "s"(ldsaddr), "v"(voff), "s"(rs), "s"(soff) : "memory", "m0");
}
template <int LI> DI void dma_pair(u32 ldsbase, u32 vo0, u32 vo1, __amdgpu_buffer_rsrc_t rs, u32 soff) {
    asm volatile("s_add_u32 m0, %0, %5\n\ts_nop 0\n\tbuffer_load_dwordx4 %1, %3, %4 offen lds\n\tbuffer_load_dwordx4 %2, %3, %4 offen offset:1024 lds"
                 :: "s"(ldsbase), "v"(vo0), "v"(vo1), "s"(rs), "s"(soff), "i"(LI) : "memory", "m0", "scc");
}
DI void touch(u32 junk, u32 voff, __amdgpu_buffer_rsrc_t rs, u32 soff) {
    asm volatile("s_mov_b32 m0, %0\n\ts_nop 0\n\tbuffer_load_dword %1, %2, %3 offen lds" :: "s"(junk), "v"(voff), "s"(rs), "s"(soff) : "memory", "m0");
}
#define WAIT_VM(n) asm volatile("s_waitcnt vmcnt(%0)" :: "i"(n) : "memory")
#define WAIT_LGKM(n) asm volatile("s_waitcnt lgkmcnt(%0)" :: "i"(n) : "memory")
DI void bar() { asm volatile("s_waitcnt lgkmcnt(0)\n\ts_barrier" ::: "memory"); }
DI int wrap7(int x) { return x >= 7 ? x - 7 : x; }
template <int N, int K> DI void ilv() { if constexpr (N > 0) { __builtin_amdgcn_sched_group_barrier(0x8, 1, 0); __builtin_amdgcn_sched_group_barrier(0x366, K, 0); ilv<N - 1, K>(); } }

constexpr int L_X = 0;
constexpr int L_W = 57344;
constexpr int L_M = L_W + 98304;
constexpr int L_SP = L_M;
constexpr int L_CS = L_M + 2048;
constexpr int L_S2 = L_M + 3072;
constexpr int L_JK = L_M + 4096;
constexpr int L_TOT = L_M + 8192;
constexpr int T_K = 0, T_V = 9216, T_X = 17408, T_S = L_JK;
constexpr int CV_B = 25600;
constexpr int CV_HE = 0, CV_HO = 4160, CV_KV = CV_HO + 4160, CV_CH = CV_KV + 4608;
constexpr int CV_ST = 16384;
static_assert(CV_B + 2 * CV_CH <= L_W, "lds map");

struct Ctx {
    LAS char* L;
    __amdgpu_buffer_rsrc_t rws, rw1, rw2;
    GAS u32* tmo;
    int w, j, grp, kb0, gi;
    u32 lbase, tagA, tagB;
};
DI Ctx fresh(const Ctx& c0) { Ctx c = c0; asm volatile("" : "+s"(c.w), "+s"(c.j), "+s"(c.grp), "+s"(c.kb0), "+s"(c.gi)); return c; }
DI bool spin_fail(const Ctx& c, unsigned& spins, u32 code) {
    if (++spins > SPIN_CAP) { __hip_atomic_store(c.tmo, code, RLX_AGENT); return true; }
    __builtin_amdgcn_s_sleep(1);
    return false;
}
DI u32 xtr_of(int lane) { return (u32)(((lane >> 5) * 4 + ((lane >> 2) & 3)) * 64 + ((lane >> 4) & 1) * 32 + (lane & 3) * 8); }
DI u32 wl1_of(const Ctx& c, int lane) {
    const int rr = (lane >> 5) * 4 + ((lane >> 2) & 3);
    return (u32)(L_W + c.w * 24576 + rr * 128 + ((rr >> 1) & 1) * 64 + ((lane >> 4) & 1) * 32 + (lane & 3) * 8);
}
DI u32 wl2_of(const Ctx& c, int lane) { return wl1_of(c, lane) ^ (u32)((c.w >> 1) * 64 + (c.w & 1) * 32); }
DI int member(const Ctx& c, int m) {
    if (m == 0) return c.j;
    int t = m - 1 + 2 * c.gi; t = t >= 6 ? t - 6 : t; t = t >= 6 ? t - 6 : t;
    return wrap7(c.j + 1 + t);
}
typedef v4u W1T[28][2];
typedef v4u W2T[16][2];
typedef f32x16 AccT[2];

DI constexpr int k_mem(int f) { return f < 4 ? 0 : 1 + (f - 4) % 6; }
DI constexpr int k_sub(int f) { return f < 4 ? f : (f - 4) / 6; }
DI constexpr int w2_step_of(int n) { return n < 40 ? 2 * (n - 28) : (n < 44 ? n - 16 : 2 * (n - 44) + 1); }
template <int N> DI u32 w_soff(const Ctx& c) {
    if constexpr (N < 28) return (N % 7 + 7 * ((N / 7 + c.kb0) & 3)) * 16 * (F * 2) + (256 * c.j + 64 * c.w) * 2;
    else return (256 * member(c, k_mem(w2_step_of(N))) + 64 * c.w + 16 * k_sub(w2_step_of(N))) * (D * 2) + 128 * c.j;
}
template <int N> DI u32 w_voff(int lane) { return (lane >> 3) * ((N < 28 ? F : D) * 2) + ((lane & 7) ^ (((lane >> 4) & 1) << 2)) * 16; }
struct WSt { u32 ring, vo0, vo1; };
template <int N> DI WSt w_st(const Ctx& c, int lane) { WSt t; t.ring = c.lbase + L_W + c.w * 24576; t.vo0 = w_voff<N>(lane); t.vo1 = t.vo0 + 8 * ((N < 28 ? F : D) * 2) - 1024; return t; }
template <int N> DI void w_dma(const Ctx& c, const WSt& t, u32 so) { dma_pair<(N % 12) * 2048>(t.ring, t.vo0, t.vo1, N < 28 ? c.rw1 : c.rw2, so); }
DI v4u w_frag(const Ctx& c, u32 wl, int slot, int t) { return trd2(c.L + (wl ^ (u32)(t * 64)) + slot * 2048, 1024); }
template <int N> DI void w_read1(const Ctx& c, u32 wl, W1T& W1, W2T& W2) {
#pragma unroll
    for (int t = 0; t < 2; ++t) {
        if constexpr (N < 28) W1[N][t] = w_frag(c, wl, N % 12, t);
        else W2[N - 28][t] = w_frag(c, wl, N % 12, t);
    }
}
template <int N> DI void w_neg(W2T& W2) {
#pragma unroll
    for (int t = 0; t < 2; ++t) { LV(W2[N - 28][t]); W2[N - 28][t].z ^= SGN; W2[N - 28][t].w ^= SGN; asm volatile("" : "+v"(W2[N - 28][t])); }
}
template <int N0, int N1> DI void w_read(const Ctx& c0, W1T& W1, W2T& W2) {
    const Ctx c = fresh(c0);
    const u32 wl = wl1_of(c, lane_id());
    sfor<N0, N1>([&](auto NC) { w_read1<decltype(NC)::value>(c, wl, W1, W2); });
}

constexpr int PS0 = 0, PS1 = 33;
template <int N0, int N1> DI void pin_a(W1T& W1) {
    if constexpr (N0 < N1) { asm volatile("" : "+a"(W1[N0][0]), "+a"(W1[N0][1])); pin_a<N0 + 1, N1>(W1); }
}
struct G1Out { AccT acc; float mb, r; };
DI constexpr bool reg_start(unsigned long long end, int f) { return f == 0 || ((end >> (f - 1)) & 1ull); }
DI constexpr int reg_len(unsigned long long end, int f) { int n = 1; while (!reg_start(end, f - n + 1)) ++n; return n; }
struct Ad {
    u32 l16;
    u32 ca;
    u32 hst;
    u32 x3o;
    u32 sto;
    u32 so[7];
};
constexpr u32 ST_HG = 3 * 4096;
DI u32 tok32(const Ad& a) { return a.l16 & 496u; }

template <bool TR, int HALF, int S0, unsigned END, class Side>
DI void g1_main(const Ctx& c, Ad& a, const W1T& W1, AccT& acc, Side&& side) {
    const u32 lp = TR ? xtr_of(a.l16 >> 4) : a.l16;
    int kb = c.kb0; asm volatile("" : "+s"(kb));
    u32 la = 0;
    auto ldb = [&](int f) {
        if (f % 7 == 0) la = lp + ((f / 7 + kb) & 3) * 7168;
        LAS char* p = c.L + la + (L_X + HALF * 28672 + (f % 7) * 1024);
        if constexpr (TR) return trd2(p, 512);
        else return *(LAS v4u*)p;
    };
    v4u B[28];
    B[0] = ldb(0); B[1] = ldb(1); B[2] = ldb(2);
    SB();
    sfor<0, 28>([&](auto FC) {
        constexpr int f = decltype(FC)::value;
        if constexpr (f % 4 == 0) STAMP2(S0 + f / 4);
        if constexpr (reg_start(END, f)) asm volatile("" : "+v"(a.l16), "+v"(a.ca));
        acc[0] = mfma32(W1[f][0], B[f], f == 0 ? zero16() : acc[0]);
        acc[1] = mfma32(W1[f][1], B[f], f == 0 ? zero16() : acc[1]);
        if constexpr (f + 3 < 28) B[f + 3] = ldb(f + 3);
        side(FC);
        if constexpr ((END >> f) & 1u) { ilv<2 * reg_len(END, f), 5>(); SB(); }
    });
}
struct St1 { u32 ad; v4u fr[2]; float s1, s2; };
template <bool TR, int HALF, int K> DI void st1_read(const Ctx& c, St1& s) {
    if constexpr (TR) s.fr[K & 1] = trd2(c.L + s.ad + (L_X + HALF * 28672 + K * 1024), 512);
    else s.fr[K & 1] = *(LAS v4u*)(c.L + s.ad + (L_X + HALF * 28672 + K * 1024));
}
template <int K, int E0, int E1> DI void st1_acc(St1& s) {
#pragma unroll
    for (int e = E0; e < E1; ++e) { if (e == E0) LV(s.fr[K & 1]); const u32 x = s.fr[K & 1][e >> 1]; if (e & 1) s.s2 = dot2(x, x, s.s2); else s.s1 = dot2(x, 0x3f803f80u, s.s1); }
}
DI void part_put(const Ctx& c, const Ad& a, int off, float s1, float s2) { *(LAS f32x2*)(c.L + off + tok32(a) * 2 + c.w * 8) = f32x2{hsum(s1), hsum(s2)}; }
DI f32x2 part_get(const Ctx& c, const Ad& a, int off) {
    const f32x4 u = *(LAS f32x4*)(c.L + off + tok32(a) * 2), v = *(LAS f32x4*)(c.L + off + tok32(a) * 2 + 16);
    return f32x2{(u[0] + u[2]) + (v[0] + v[2]), (u[1] + u[3]) + (v[1] + v[3])};
}
DI void frag_sums(const v4u& p, float& s1, float& s2) {
    s1 = 0.f; s2 = 0.f;
#pragma unroll
    for (int e = 0; e < 4; ++e) { s1 = dot2(p[e], 0x3f803f80u, s1); s2 = dot2(p[e], p[e], s2); }
}
template <int HALF, int KIND> DI void stat_pub(const Ctx& c, const Ad& a, int off, int who) {
    if (c.w == who) {
        f32x2 t = part_get(c, a, off);
        const bool z = c.j == 6 && a.l16 >= 512u;
        st128(v4u{z ? 0u : __float_as_uint(t[0]), c.tagA, z ? 0u : __float_as_uint(t[1]), c.tagB}, c.rws, z ? a.l16 : tok32(a), a.sto + HALF * ST_HG + KIND * 4096 + c.j * 512);
    }
}
template <bool DO> DI void stat_pub_if(const Ctx& c, const Ad& a) { if constexpr (DO) stat_pub<0, 0>(c, a, L_S2, 0); }
struct Gath { v4u g[4]; };
template <int HALF, int KIND, int I> DI void gath_ld(const Ctx& c, const Ad& a, Gath& g) {
    g.g[I] = ld128(c.rws, a.l16 + I * 1024, a.sto + HALF * ST_HG + KIND * 4096);
}
DI bool gath_bad(const Ctx& c, const Gath& g) {
    u32 d = 0;
#pragma unroll
    for (int i = 0; i < 4; ++i) d = tag_bad(g.g[i], c.tagA, c.tagB, d);
    return ANY(d != 0u);
}
template <int HALF, int KIND> DI void gath_wait(const Ctx& c, const Ad& a, Gath& g) {
    if (gath_bad(c, g)) {
        unsigned spins = 0;
        NOUNROLL do { MEMBAR(); sfor<0, 4>([&](auto IC) { gath_ld<HALF, KIND, decltype(IC)::value>(c, a, g); }); } while (gath_bad(c, g) && !spin_fail(c, spins, 2u));
    }
}
DI void gath_ln(const Gath& g, float& m, float& r) {
    const float t1 = (__uint_as_float(g.g[0].x) + __uint_as_float(g.g[1].x)) + (__uint_as_float(g.g[2].x) + __uint_as_float(g.g[3].x));
    const float t2 = (__uint_as_float(g.g[0].z) + __uint_as_float(g.g[1].z)) + (__uint_as_float(g.g[2].z) + __uint_as_float(g.g[3].z));
    ln_scalars(hsum(t1), hsum(t2), m, r);
}
DI f32x4 epi_cs(const Ctx& c, const Ad& a, int E) { return *(LAS f32x4*)(c.L + a.ca + E * 32); }
template <int HALF, int MLP, int E>
DI void g1_epi(const Ctx& c, const Ad& a, G1Out& o, f32x4& cs, v4u& fr) {
    constexpr int t = E >> 2, r0 = 4 * (E & 3), g = E & 1;
    constexpr bool neg = (g ^ MLP) != 0;
    float h[4];
    LV(cs); LA(o.acc[t]);
#pragma unroll
    for (int e = 0; e < 4; ++e) h[e] = __builtin_fmaf(-o.mb, cs[e], o.acc[t][r0 + e]);
#pragma unroll
    for (int e = 0; e < 2; ++e) {
        const u32 p = __builtin_bit_cast(u32, __builtin_elementwise_max(__builtin_bit_cast(s16x2, pk(h[2 * e], h[2 * e + 1])), s16x2{0, 0}));
        fr[2 * g + e] = neg ? (p | SGN) : p;
    }
    if (g == 0) LV(fr);
}
template <int HALF, int K> DI void h_store(const Ctx& c, const Ad& a, const v4u& fr) { st128<K * 1024>(fr, c.rws, a.l16, a.hst + HALF * 114688); }

#define NW0_ 0
#define NT0_ 0
constexpr int NT0 = NT0_;
#define NW1_ 2
constexpr int NW1 = NW1_;
#define KP_ 4
constexpr int KP = KP_;
#define TLBW_ 0
constexpr int TLBW = TLBW_;
#define SLP0 0
#define HT1 0
#define HT2 13
#define SLP1 16
constexpr int NW0 = NW0_;
#define EPE 8
constexpr int HR = 10, GL = 14, GC = 20;
template <int MLP> DI bool h_bad(const v4u& a, const v4u& b) {
    const u32 lo = MLP == 0 ? (a.x | a.y | b.x | b.y) : ~(a.x & a.y & b.x & b.y);
    const u32 hi = MLP == 0 ? ~(a.z & a.w & b.z & b.w) : (a.z | a.w | b.z | b.w);
    return ((lo | hi) & SGN) != 0u;
}
DI u32 h_base(const Ctx& c, int half) { return OFF_H + ((c.grp * 2 + half) * 112 + 4 * c.w) * 1024; }
template <bool ASM> DI v2u gate_ld(const Ctx& c, const Ad& a, int half) {
    const u32 vo = min(a.l16, 96u) * 1024, so = a.hst - c.j * 16384 + half * 114688 + 4;
    if constexpr (ASM) { v2u r; asm volatile("buffer_load_dwordx2 %0, %1, %2, %3 offen sc1" : "=v"(r) : "v"(vo), "s"(c.rws), "s"(so) : "memory"); return r; }
    else return __builtin_amdgcn_raw_buffer_load_b64(c.rws, vo, so, AUX_SC1);
}
template <int MLP> DI bool gate_bad(v2u x) { return ((MLP == 0 ? (x.x | ~x.y) : (~x.x | x.y)) & SGN) != 0u; }
template <int MLP> DI void gate_wait(const Ctx& c, const Ad& a, int half, v2u x) {
    if (ANY(gate_bad<MLP>(x))) {
        unsigned spins = 0;
        NOUNROLL do { MEMBAR(); x = gate_ld<false>(c, a, half); } while (ANY(gate_bad<MLP>(x)) && !spin_fail(c, spins, 1u));
    }
}
template <int N> DI v4u h_next(const Ctx& c, Ad& a) {
    constexpr int f = N >= 28 ? N - 28 : N;
    if constexpr (N >= 32 && N < 38) a.so[k_mem(f)] += 114688u;
    v4u r = __builtin_amdgcn_raw_buffer_load_b128(c.rws, a.l16 + k_sub(f) * 1024, a.so[k_mem(f)], AUX_SC1);
    if constexpr (N >= 50) a.so[k_mem(f)] -= 114688u;
    return r;
}
template <int MLP, int F_> DI void h_check(const Ctx& c0, v4u& a, v4u& b) {
    if (__builtin_expect(ANY(h_bad<MLP>(a, b)), 0)) {
        const Ctx c = fresh(c0);
        constexpr int half = F_ >= 28, f = F_ - 28 * half;
        const u32 vo = lane_id() * 16 + k_sub(f) * 1024, s0 = h_base(c, half) + member(c, k_mem(f)) * 16384, s1 = h_base(c, half) + member(c, k_mem(f + 1)) * 16384;
        unsigned spins = 0;
        NOUNROLL do {
            MEMBAR();
            a = ld128(c.rws, vo, s0); b = ld128(c.rws, vo, s1);
        } while (ANY(h_bad<MLP>(a, b)) && !spin_fail(c, spins, 3u));
    }
}
template <int HALF, int P> DI void red_write(const Ctx& c, const Ad& a, const AccT& acc) {
    LAS char* dst = c.L + (a.l16 + (P ^ c.w) * 6144) + (L_X + (1 - HALF) * 28672 + (P - 1) * 2048);
    const f32x16& x = acc[P >> 1];
    constexpr int r0 = (P & 1) * 8;
    *(LAS f32x4*)dst = f32x4{x[r0], x[r0 + 1], x[r0 + 2], x[r0 + 3]};
    *(LAS f32x4*)(dst + 1024) = f32x4{x[r0 + 4], x[r0 + 5], x[r0 + 6], x[r0 + 7]};
}
template <int HALF> DI void red_read(const Ctx& c, const Ad& a, f32x4 (&t)[6]) {
    LAS char* src = c.L + (a.l16 + c.w * 6144) + (L_X + (1 - HALF) * 28672);
#pragma unroll
    for (int i = 0; i < 6; ++i) t[i] = *(LAS f32x4*)(src + i * 1024);
}
template <int K> DI void post_half(const f32x4& o, float r, v4u& xres) {
#pragma unroll
    for (int e = 0; e < 2; ++e) xres[2 * K + e] = pk(__builtin_fmaf(o[2 * e], r, bflo(xres[2 * K + e])), __builtin_fmaf(o[2 * e + 1], r, bfhi(xres[2 * K + e])));
}
template <int HALF> DI void out_store(const Ctx& c, const Ad& a, const v4u& x, __amdgpu_buffer_rsrc_t rout) {
    auto r0 = __builtin_amdgcn_permlane32_swap(x.x, x.z, false, false);
    auto r1 = __builtin_amdgcn_permlane32_swap(x.y, x.w, false, false);
    const v4u v = {r0[0], r1[0], r0[1], r1[1]};
    const u32 vo = tok32(a) * (D / 8) + (a.l16 >> 9) * 16, so = (u32)((c.grp * 64 + HALF * 32) * D + 64 * c.j + 16 * c.w) * 2;
    asm volatile("buffer_store_dwordx4 %0, %1, %2, %3 offen\n\ts_nop 1" :: "v"(v), "v"(vo), "s"(rout), "s"(so) : "memory");
}
template <int HALF, int MLP> DI v4u xres_ld(const Ctx& c, const Ad& a) {
    if constexpr (MLP == 0) return trd2(c.L + (xtr_of(a.l16 >> 4) + (4 * c.j + c.w) * 1024) + (L_X + HALF * 28672), 512);
    else return *(LAS v4u*)(c.L + (a.l16 + (4 * c.j + c.w) * 1024) + (L_X + HALF * 28672));
}
template <int HALF> DI v4u y_ld(const Ctx& c, const Ad& a) { return trd2(c.L + (xtr_of(a.l16 >> 4) + c.w * 1024) + (L_X + HALF * 28672 + 24576), 512); }
DI void x3_make(v4u& x, const v4u& yf, float m, float r) {
    const float n = -m * r;
#pragma unroll
    for (int e = 0; e < 4; ++e) {
        const float p = bflo(x[e]), q = bfhi(x[e]);
        x[e] = pk(__builtin_fmaf(__builtin_fmaf(p, r, n), bflo(yf[e]), p), __builtin_fmaf(__builtin_fmaf(q, r, n), bfhi(yf[e]), q));
    }
}
template <int HALF> DI void x3_store(const Ctx& c, const Ad& a, const v4u& x) { st128(x, c.rws, a.l16, a.x3o + HALF * 28672); }
template <int HALF, int I> DI v4u x3_ld1(const Ctx& c, const Ad& a) {
    return ld128(c.rws, a.l16 + (I & 3) * 1024, a.x3o + (6 * c.w - 4 * c.j + (I & 4)) * 1024 + HALF * 28672);
}
DI u32 max3u(u32 a, u32 b, u32 c) { u32 r; asm("v_max3_u32 %0, %1, %2, %3" : "=v"(r) : "v"(a), "v"(b), "v"(c)); return r; }
DI bool x3_bad(const v4u (&t)[7]) {
    u32 mx = 0;
#pragma unroll
    for (int i = 0; i < 7; ++i) mx = max3u(max3u(mx, t[i].x, t[i].y), t[i].z, t[i].w);
    return ANY(mx == ~0u);
}
template <bool EQ> DI void gate_spin(const Ctx& c, u32 vo, u32 so, u32 word, u32 code) {
    unsigned spins = 0;
    NOUNROLL for (;;) {
        MEMBAR();
        const u32 x = __builtin_amdgcn_raw_buffer_load_b32(c.rws, vo, so, AUX_SC1);
        if (!ANY(EQ ? x == word : x != word)) break;
        if (spin_fail(c, spins, code)) break;
    }
}
template <int HALF> DI void x3_wait(const Ctx& c, const Ad& a, v4u (&t)[7]) {
    if (x3_bad(t)) {
        unsigned spins = 0;
        NOUNROLL do { MEMBAR(); sfor<0, 7>([&](auto IC) { t[decltype(IC)::value] = x3_ld1<HALF, decltype(IC)::value>(c, a); }); } while (x3_bad(t) && !spin_fail(c, spins, 4u));
    }
}
template <int HALF, int I> DI void x3_put1(const Ctx& c, const Ad& a, const v4u& t) { *(LAS v4u*)(c.L + (a.l16 + c.w * 7168) + (L_X + HALF * 28672 + I * 1024)) = t; }

template <int MLP>
DI void g2_pair(const Ctx& c, Ad& a, const W2T& W2, v4u (&hb)[56], G1Out& gb, f32x4 (&csq)[3], float r0, v4u (&xres)[2], __amdgpu_buffer_rsrc_t out) {
    const u32 wl0 = wl2_of(c, a.l16 >> 4), wl1 = wl0 ^ 64u;
    AccT acc[2];
    v2u gate = {0, 0};
    v4u wl[2], fr, yf;
    f32x4 o[2], u[2];
    float s1, s2;
    Gath gs;
    const float ra_ = MLP ? -r0 : r0, rb_ = MLP ? -gb.r : gb.r;
    auto wres = [&](int slot, int k) { return trd2(c.L + (k ? wl1 : wl0) + slot * 2048, 1024); };
    auto fetch = [&](auto NC) {
        constexpr int N = decltype(NC)::value;
        if constexpr (N < 56) {
            if constexpr (N >= 28 && N < 32) hb[N] = *(LAS v4u*)(c.L + (a.l16 + c.w * 4096) + (L_X + (N - 28) * 1024));
            else hb[N] = h_next<N>(c, a);
        }
    };
    auto rd = [&](int i) { return *(LAS f32x4*)(c.L + (a.l16 + c.w * 6144) + (L_X + 28672 + i * 1024)); };
    auto own = [&](int k) { return f32x4{acc[0][0][4 * k], acc[0][0][4 * k + 1], acc[0][0][4 * k + 2], acc[0][0][4 * k + 3]}; };
    wl[0] = wres(8, 0); wl[1] = wres(8, 1);
    SB();
    sfor<0, 28>([&](auto PC) {
        constexpr int F_ = 2 * decltype(PC)::value, H = F_ >= 28, f = F_ - 28 * H;
        constexpr bool res = f < 24;
        constexpr int r0_ = f < 24 ? f >> 1 : f - 12;
        if constexpr (F_ == 24 || F_ == 30 || (MLP == 0 && F_ == 36)) { bar(); SB(); stat_pub_if<MLP == 0 && F_ == 36>(c, a); }
        if constexpr (F_ % 4 == 0) STAMP2((MLP ? PS1 : PS0) + 15 + F_ / 4);
        asm volatile("" : "+v"(a.l16), "+v"(a.ca));
        acc[H][0] = mfma32(W2[r0_][0], hb[F_], f == 0 ? zero16() : acc[H][0]);
        acc[H][1] = mfma32(W2[r0_][1], hb[F_], f == 0 ? zero16() : acc[H][1]);
        fetch(std::integral_constant<int, F_ + (F_ < 8 ? HR - 2 : HR)>{});
        fetch(std::integral_constant<int, F_ + 1 + (F_ < 8 ? HR - 2 : HR)>{});
        if constexpr (F_ == 6) { fetch(std::integral_constant<int, F_ + HR>{}); fetch(std::integral_constant<int, F_ + HR + 1>{}); }
        if constexpr (F_ < 6) csq[(F_ + 2) % 3] = epi_cs(c, a, F_ + 2);
        if constexpr (F_ < 8) g1_epi<1, MLP, F_>(c, a, gb, csq[F_ % 3], fr);
        if constexpr (F_ < 6) csq[F_ % 3] = epi_cs(c, a, F_ + 3);
        if constexpr (F_ < 8) g1_epi<1, MLP, F_ + 1>(c, a, gb, csq[(F_ + 1) % 3], fr);
        if constexpr (F_ == 32) { LA(acc[0][0]); LV(o[0]); LV(u[0]); LV(u[1]); o[0] = (o[0] + own(0)) + (u[0] + u[1]); post_half<0>(o[0], ra_, xres[0]); LV(xres[0]); }
        if constexpr (F_ == 34) { LA(acc[0][0]); LV(o[0]); LV(u[0]); LV(u[1]); o[0] = (o[0] + own(1)) + (u[0] + u[1]); post_half<1>(o[0], ra_, xres[0]); LV(xres[0]); }
        acc[H][0] = mfma32(res ? wl[0] : W2[r0_ + 1][0], hb[F_ + 1], acc[H][0]);
        acc[H][1] = mfma32((res || f == 26) ? wl[1] : W2[r0_ + 1][1], hb[F_ + 1], acc[H][1]);
        if constexpr (f < 22) { wl[0] = wres((9 + (f >> 1)) % 12, 0); wl[1] = wres((9 + (f >> 1)) % 12, 1); }
        if constexpr (f == 24) wl[1] = *(LAS v4u*)(c.L + (a.l16 + c.w * 1024) + L_JK);
        if constexpr (F_ == 26) { wl[0] = wres(8, 0); wl[1] = wres(8, 1); }
        if constexpr (F_ == 22) xres[1] = xres_ld<1, MLP>(c, a);
        if constexpr (F_ == 30) { u[0] = rd(0); u[1] = rd(2); o[0] = rd(4); }
        if constexpr (F_ == 32) { u[0] = rd(1); u[1] = rd(3); o[0] = rd(5); }
        if constexpr (MLP == 0 && F_ == 54) yf = y_ld<0>(c, a);
        if constexpr (F_ == GL) gate = gate_ld<false>(c, a, 1);
        if constexpr (F_ < 8) { *(LAS v4u*)(c.L + (a.l16 + c.w * 4096) + (L_X + (F_ / 2) * 1024)) = fr; h_store<1, F_ / 2>(c, a, fr); }
        if constexpr (MLP == 0 && (F_ == 24 || F_ == 26))
            dma16<true>(c.lbase + L_X + (F_ - 24) / 2 * 28672 + 24576 + c.w * 1024, a.l16, c.rws, OFF_YT + (c.grp * 2 + (F_ - 24) / 2) * 28672 + c.j * 4096 + c.w * 1024);
        if constexpr (F_ == 28) { red_write<0, 1>(c, a, acc[0]); red_write<0, 2>(c, a, acc[0]); red_write<0, 3>(c, a, acc[0]); }
        if constexpr (MLP == 0 && F_ == 34) { frag_sums(xres[0], s1, s2); part_put(c, a, L_S2, s1, s2); }
        if constexpr (MLP == 1 && F_ == 36) out_store<0>(c, a, xres[0], out);
        ilv<4, 5>();
        if constexpr (F_ + 2 < 56 && f + 2 != 28 && f + 2 >= 4) h_check<MLP, F_ + 2>(c, hb[F_ + 2], hb[F_ + 3]);
        if constexpr (F_ == GC) gate_wait<MLP>(c, a, 1, gate);
        SB();
    });
    static_assert(GC + 2 + HR <= 32 && (GC & 1) == 0 && (HR & 1) == 0, "half B's first foreign fragment is requested behind the gate");
    STAMP2((MLP ? PS1 : PS0) + 29);
    if constexpr (MLP == 0) STAMP4(0);
    red_write<1, 1>(c, a, acc[1]); red_write<1, 2>(c, a, acc[1]); red_write<1, 3>(c, a, acc[1]);
    if constexpr (MLP == 0) sfor<0, 4>([&](auto IC) { gath_ld<0, 0, decltype(IC)::value>(c, a, gs); });
    bar();
    if constexpr (MLP == 0) STAMP4(1);
    f32x4 t[6];
    red_read<1>(c, a, t);
    o[0] = (f32x4{acc[1][0][0], acc[1][0][1], acc[1][0][2], acc[1][0][3]} + t[0]) + (t[2] + t[4]);
    o[1] = (f32x4{acc[1][0][4], acc[1][0][5], acc[1][0][6], acc[1][0][7]} + t[1]) + (t[3] + t[5]);
    post_half<0>(o[0], rb_, xres[1]); post_half<1>(o[1], rb_, xres[1]);
    if constexpr (MLP == 0) {
        frag_sums(xres[1], s1, s2); part_put(c, a, L_S2, s1, s2);
        STAMP4(2);
        bar();
        stat_pub<1, 0>(c, a, L_S2, 1);
        STAMP4(3);
        gath_wait<0, 0>(c, a, gs);
        gath_ln(gs, s1, s2);
        x3_make(xres[0], yf, s1, s2);
        x3_store<0>(c, a, xres[0]);
        STAMP4(4);
        yf = y_ld<1>(c, a);
        __builtin_amdgcn_s_sleep(SLP1);
        v4u x[7];
        unsigned spins = 0;
        NOUNROLL do {
            MEMBAR();
            sfor<0, 4>([&](auto IC) { gath_ld<1, 0, decltype(IC)::value>(c, a, gs); });
            sfor<0, 7>([&](auto IC) { x[decltype(IC)::value] = x3_ld1<0, decltype(IC)::value>(c, a); });
        } while ((gath_bad(c, gs) || x3_bad(x)) && !spin_fail(c, spins, 4u));
        STAMP4(5);
        gath_ln(gs, s1, s2);
        x3_make(xres[1], yf, s1, s2);
        x3_store<1>(c, a, xres[1]);
        sfor<0, 7>([&](auto IC) { x3_put1<0, decltype(IC)::value>(c, a, x[decltype(IC)::value]); });
    } else out_store<1>(c, a, xres[1], out);
}

__global__ void __launch_bounds__(256, 1) fused_kernel(const u16* __restrict__ x0, const u16* __restrict__ kin, const u16* __restrict__ vin,
                                                       const u16* __restrict__ sk, const u16* __restrict__ hk, const u16* __restrict__ Dv,
                                                       const u16* __restrict__ w1, const u16* __restrict__ w2,
                                                       unsigned char* __restrict__ ws, u16* __restrict__ out) {
    extern __shared__ __attribute__((aligned(16))) char smem[];
    Ctx c;
    c.L = (LAS char*)smem;
    c.lbase = (u32)(uintptr_t)c.L;
    const int b = blockIdx.x, tid = threadIdx.x;
    c.w = __builtin_amdgcn_readfirstlane(tid >> 6);
    c.rws = mkrs(ws, WS_BYTES);
    c.tmo = (GAS u32*)(ws + 256);
    STAMP(0);
    STAMP3(0);

    if (b < 32) {
        const v2u ep = __builtin_amdgcn_raw_buffer_load_b64(c.rws, 0, OFF_EP, AUX_SC1);
        v4u xr[2][7];
#pragma unroll
        for (int h = 0; h < 2; ++h)
#pragma unroll
            for (int i = 0; i < 7; ++i) xr[h][i] = *(const v4u*)(x0 + (b * 64 + 2 * (tid >> 3) + h) * D + (i * 8 + (tid & 7)) * 8);
        MEMBAR();
        {
            unsigned long long pc; u32 back, len;
            asm volatile("s_getpc_b64 %0\n.Lfk_here:\n\ts_mov_b32 %1, .Lfk_here-.Lfk_begin\n\ts_mov_b32 %2, .Lfk_end-.Lfk_begin" : "=s"(pc), "=s"(back), "=s"(len));
            if (c.w < 2) touch(c.lbase + L_JK, (tid & 63) * 128, mkrs((const void*)(pc - back), len), ((b >> 3) * 2 + c.w) * 8192);
        }
        u32 res[2];
#pragma unroll
        for (int h = 0; h < 2; ++h) {
            float s1 = 0.f, s2 = 0.f;
#pragma unroll
            for (int i = 0; i < 7; ++i)
#pragma unroll
                for (int e = 0; e < 4; ++e) { s1 = dot2(xr[h][i][e], 0x3f803f80u, s1); s2 = dot2(xr[h][i][e], xr[h][i][e], s2); }
#pragma unroll
            for (int o = 1; o < 8; o <<= 1) { s1 += __shfl_xor(s1, o); s2 += __shfl_xor(s2, o); }
            const float m = s1 * (1.0f / D);
            res[h] = pk(m, ln_sd(fmaxf(__builtin_fmaf(-m, m, s2 * (1.0f / D)), 0.f)));
        }
        if ((tid & 7) == 0) st128(v4u{res[0], ep.x + 1u, res[1], ep.y + 1u}, c.rws, (u32)(b * 32 + (tid >> 3)) * 16, OFF_ST0);
        const int rg = tid >> 3, c8 = min(tid & 7, 6);
        v4u wr[14];
#pragma unroll
        for (int i = 0; i < 14; ++i) wr[i] = *(const v4u*)(w1 + (rg * 14 + i) * F + b * 56 + c8 * 8);
        float cs[8] = {0, 0, 0, 0, 0, 0, 0, 0};
#pragma unroll
        for (int i = 0; i < 14; ++i)
#pragma unroll
            for (int e = 0; e < 4; ++e) { cs[2 * e] += bflo(wr[i][e]); cs[2 * e + 1] += bfhi(wr[i][e]); }
        LAS float* sf = (LAS float*)c.L;
#pragma unroll
        for (int e = 0; e < 8; ++e) sf[rg * 65 + (tid & 7) * 8 + e] = cs[e];
        bar();
        if (tid < 56) {
            float t = 0.f;
#pragma unroll
            for (int i = 0; i < 32; ++i) t += sf[i * 65 + tid];
            const float t2 = __shfl_down(t, 1);
            if ((tid & 1) == 0) st128(v4u{__float_as_uint(t), ep.x + 1u, __float_as_uint(t2), ep.y + 1u}, c.rws, (u32)(b * 28 + (tid >> 1)) * 16, OFF_CSG);
        }
        WAIT_VM(0);
        bar();
        {
            const __amdgpu_buffer_rsrc_t rw1 = mkrs(w1, D * F * 2), rw2 = mkrs(w2, D * F * 2);
#pragma unroll
            for (int i = 0; i < HT1; ++i) touch(c.lbase + L_JK, (tid & 63) * 128, rw1, (u32)(112 + i * 16 + (b >> 3) * 4 + c.w) * 8192);
#pragma unroll
            for (int i = 0; i < HT2; ++i) touch(c.lbase + L_JK, (tid & 63) * 128, rw2, (u32)(i * 16 + (b >> 3) * 4 + c.w) * 8192);
        }
        WAIT_VM(0);
        return;
    }
    asm volatile(".Lfk_begin:");
    const int wb = b - 32;
    { const int xcd = wb & 7, idx = wb >> 3; c.grp = xcd * 4 + idx / 7; c.j = idx % 7; }
    c.gi = c.grp & 3;
    const int w = c.w;
    c.rw1 = mkrs(w1, D * F * 2); c.rw2 = mkrs(w2, D * F * 2);
    c.kb0 = (w + c.gi + 2) & 3;
    const int cp = (wb & 7) * 28 + (wb >> 3);
    W1T W1; W2T W2;

    u32 dval;
    {
        const int g = wb % 7, rb = wb / 7, lane = tid & 63;
        const int rg = (lane & 7) * 2 + ((lane >> 4) & 1), cl = ((lane >> 3) & 1) + 2 * (lane >> 5), cq = 4 * w + cl;
        const __amdgpu_buffer_rsrc_t rk = mkrs(kin, TSZ), rv = mkrs(vin, TSZ), rx = mkrs(x0, TSZ), rh = mkrs(hk, TSZ), rs_ = mkrs(sk, D * 8), rd = mkrs(Dv, D * 2);
        STAMP3(1);
        const u32 tb = (u32)((rb * 64) * 896 + g * 128);
        const u32 tv0 = (u32)((lane >> 3) * 896) + (((lane & 7) ^ ((lane >> 5) + 2 * w)) & 7) * 16;
        dma16<false>(c.lbase + T_X + w * 1024, tv0, rx, tb + w * 7168);
        dma16<false>(c.lbase + T_X + (w + 4) * 1024, tv0, rx, tb + (w + 4) * 7168);
        dma16<false>(c.lbase + T_K + w * 1024, tv0 + tb + (w - 1) * 7168, rk, 0u);
        dma16<false>(c.lbase + T_K + (w + 4) * 1024, tv0, rk, tb + (w + 3) * 7168);
        dma16<false>(c.lbase + T_V + w * 1024, tv0, rv, tb + w * 7168);
        dma16<false>(c.lbase + T_V + (w + 4) * 1024, tv0, rv, tb + (w + 4) * 7168);
        if (w == 0) dma16<false>(c.lbase + T_K + 8192, tv0, rk, tb + 7 * 7168);
        else dma16<false>(c.lbase + T_S + (w - 1) * 1024, (u32)lane * 16, rs_, g * 512);
        v4u ep4 = ld128_asm<true>(c.rws, 0u, OFF_EP);
        v4u he[2];
        he[0] = ld128_asm<false>(rh, tid * 16, cp * 8192); he[1] = ld128_asm<false>(rh, tid * 16, cp * 8192 + 4096);
        v4u dv4 = ld128_asm<false>(rd, 0u, cp * 4);
        {
            const WSt wt = w_st<0>(c, lane);
            sfor<0, NW0>([&](auto NC) { w_dma<decltype(NC)::value>(c, wt, w_soff<decltype(NC)::value>(c)); });
        }
        if constexpr (TLBW) {
            if (w == 0) touch(c.lbase + L_JK + 3072, (u32)lane * 524288u, c.rws, 4096u);
            else touch(c.lbase + L_JK + 3072, (u32)lane * 262144u, w == 1 ? c.rw1 : w == 2 ? c.rw2 : mkrs(out, TSZ), 0u);
        }
        if constexpr (NT0) touch(c.lbase + L_JK + 3072, (u32)lane * 128, c.rw1, (u32)((wb >> 3) * 4 + w) * 8192);
        STAMP3(2);
        asm volatile("s_waitcnt vmcnt(%1)" : "+v"(ep4) : "i"(3 + NT0 + TLBW + 2 * NW0) : "memory");
        c.tagA = __builtin_amdgcn_readfirstlane(ep4.x) + 1u; c.tagB = __builtin_amdgcn_readfirstlane(ep4.y) + 1u;
        bar();
        STAMP(1); STAMP3(3);
        const u32 a1 = (u32)(rg * 512 + (((cq >> 1) ^ rg) & 7) * 16 + (cq & 1) * 8);
        const u32 ak1 = (u32)(rg * 512 + 1024 + (((cq >> 1) ^ (rg + 2)) & 7) * 16 + (cq & 1) * 8);
        const u32 ak0 = (u32)(rg * 512 + 512 + (((cq >> 1) ^ (rg + 1)) & 7) * 16 + (cq & 1) * 8);
        v2u kr[7], vr[4], xr[4];
#pragma unroll
        for (int i = 0; i < 4; ++i) xr[i] = *(LAS v2u*)(c.L + T_X + a1 + i * 128);
        const v4u w01 = *(LAS v4u*)(c.L + T_S + cq * 32), w23 = *(LAS v4u*)(c.L + T_S + cq * 32 + 16);
#pragma unroll
        for (int i = 0; i < 3; ++i) kr[i] = *(LAS v2u*)(c.L + T_K + ak0 + (i + 1) * 128);
#pragma unroll
        for (int i = 0; i < 4; ++i) { kr[3 + i] = *(LAS v2u*)(c.L + T_K + ak1 + i * 128); vr[i] = *(LAS v2u*)(c.L + T_V + a1 + i * 128); }
#pragma unroll
        for (int h = 0; h < 2; ++h) {
            auto r0 = __builtin_amdgcn_permlane16_swap(__builtin_amdgcn_perm(xr[1][h], xr[0][h], 0x05040100u), __builtin_amdgcn_perm(xr[1][h], xr[0][h], 0x07060302u), false, false);
            auto r1 = __builtin_amdgcn_permlane16_swap(__builtin_amdgcn_perm(xr[3][h], xr[2][h], 0x05040100u), __builtin_amdgcn_perm(xr[3][h], xr[2][h], 0x07060302u), false, false);
            st128(v4u{r0[0], r1[0], r0[1], r1[1]}, c.rws, (u32)((cl * 4 + ((lane >> 4) & 1)) * 4096 + (lane & 7) * 16), OFF_X0G + (g * 64 + 16 * w + 2 * h) * 4096 + rb * 128);
        }
        STAMP3(27);
        v4u kg[4];
#pragma unroll
        for (int e = 0; e < 4; ++e) {
            const v4u& wq = e < 2 ? w01 : w23;
            const float w0 = bflo(wq[(e & 1) * 2]), w1_ = bfhi(wq[(e & 1) * 2]), w2_ = bflo(wq[(e & 1) * 2 + 1]), w3 = bfhi(wq[(e & 1) * 2 + 1]);
            float kk[7], o[4];
#pragma unroll
            for (int i = 0; i < 7; ++i) kk[i] = (e & 1) ? bfhi(kr[i][e >> 1]) : bflo(kr[i][e >> 1]);
#pragma unroll
            for (int i = 0; i < 4; ++i) o[i] = (kk[i] * w3 + kk[i + 1] * w2_ + kk[i + 2] * w1_ + kk[i + 3] * w0) * ((e & 1) ? bfhi(vr[i][e >> 1]) : bflo(vr[i][e >> 1]));
            kg[e] = v4u{pk(o[3], o[2]), c.tagA, pk(o[1], o[0]), c.tagB};
        }
        STAMP3(28);
        asm volatile("s_waitcnt vmcnt(0)\n\ts_barrier" : "+v"(he[0]), "+v"(he[1]), "+v"(dv4), "+v"(kg[0]), "+v"(kg[1]), "+v"(kg[2]), "+v"(kg[3]) :: "memory");
        STAMP3(29);
#pragma unroll
        for (int e = 0; e < 4; ++e) st128(kg[e], c.rws, (u32)(cl * 4 * 8192 + (15 - rg) * 16), OFF_KVG + ((g * 64 + 16 * w + e) * 512 + 496 - 16 * rb) * 16);
        {
            const WSt wt = w_st<0>(c, lane);
            sfor<NW0, NW1>([&](auto NC) { w_dma<decltype(NC)::value>(c, wt, w_soff<decltype(NC)::value>(c)); });
        }
        STAMP(2); STAMP3(4);
        dval = (w >> 1) ? dv4.x >> 16 : dv4.x & 0xffffu;
#pragma unroll
        for (int cc = 0; cc < 2; ++cc) {
            LAS char* cb = c.L + CV_B + cc * CV_CH;
            *(LAS v4u*)(cb + CV_HE + 32 + tid * 16) = he[cc];
            if (tid < 2) { *(LAS v4u*)(cb + CV_HE + tid * 16) = v4u{0, 0, 0, 0}; *(LAS v4u*)(cb + CV_HO + tid * 16) = v4u{0, 0, 0, 0}; }
            if (tid < 32) *(LAS v4u*)(cb + CV_KV + 4096 + tid * 16) = v4u{0, 0, 0, 0};
        }
        bar();
#pragma unroll
        for (int cc = 0; cc < 2; ++cc) {
            const v4u e = he[cc];
            const u32 pv = *(LAS u32*)(c.L + CV_B + cc * CV_CH + CV_HE + 28 + tid * 16);
            *(LAS v4u*)(c.L + CV_B + cc * CV_CH + CV_HO + 32 + tid * 16) = v4u{__builtin_amdgcn_alignbit(e.x, pv, 16), __builtin_amdgcn_alignbit(e.y, e.x, 16),
                                                                            __builtin_amdgcn_alignbit(e.z, e.y, 16), __builtin_amdgcn_alignbit(e.w, e.z, 16)};
        }
        bar();
        STAMP3(5);
        SB();
    }
    {
        const int lane = lane_id(), i16 = lane & 15, q = lane >> 4, cc = w >> 1, p = w & 1, ch = 2 * cp + cc;
        LAS char* cb = c.L + CV_B + cc * CV_CH;
        const int o0 = i16 + 1 + 32 * p + 8 * q;
        LAS char* ab = cb + ((o0 & 1) ? CV_HO + 4 : CV_HE) + (o0 >> 1) * 4;
        v4u A[32];
        v4u AD;
        {
            const int jj = p ? -1 : 15 - i16 - 8 * q;
            const u32 dv = dval << ((jj & 1) * 16);
#pragma unroll
            for (int d = 0; d < 4; ++d) AD[d] = (jj >= 0 && (jj >> 1) == d) ? dv : 0u;
        }
        const Ctx cs_ = fresh(c);
        const u32 wl = wl1_of(cs_, lane);
        const WSt wt = w_st<0>(cs_, lane);
        v4u sg[4], g[4];
        SB();
        sfor<0, 8>([&](auto NC) {
            constexpr int n = decltype(NC)::value;
#pragma unroll
            for (int t = n * 4; t < n * 4 + 4; ++t) { LAS u32* pp = (LAS u32*)(ab + t * 128); A[t] = v4u{pp[0], pp[1], pp[2], pp[3]}; }
            SB();
        });
        STAMP(3); STAMP3(6);
        {
            __builtin_amdgcn_s_sleep(SLP0);
            unsigned spins = 0;
            auto bad4 = [&](const v4u (&x)[4]) { u32 d = 0; for (int t = 0; t < 4; ++t) d = tag_bad(x[t], c.tagA, c.tagB, d); return ANY(d != 0u); };
            NOUNROLL do {
                MEMBAR();
#pragma unroll
                for (int i = 0; i < 4; ++i) g[i] = ld128(c.rws, tid * 16, OFF_KVG + cp * 16384 + i * 4096);
            } while (bad4(g) && !spin_fail(c, spins, 5u));
            STAMP3(30);
#pragma unroll
            for (int i = 0; i < 4; ++i) *(LAS v2u*)(c.L + CV_B + (i >> 1) * CV_CH + CV_KV + (tid + 256 * (i & 1)) * 8) = v2u{g[i].x, g[i].z};
        }
        bar();
        STAMP(4); STAMP3(7);
        LAS char* bb = cb + CV_KV + (2032 - 16 * i16 + 32 * p + 8 * q) * 2;
        f32x4 acc[8];
        float xs[4][4], qs[4][4];
        v4u B[32];
        v2u xg[4];
        B[0] = *(LAS v4u*)(bb + 3 * 128); B[1] = *(LAS v4u*)(bb + 2 * 128); B[2] = *(LAS v4u*)(bb + 128);
        const u32 s4 = (u32)(256 * p + 4 * i16 + q), xo = OFF_X0G + ch * 4096;
        SB();
        sfor<0, 32>([&](auto OC) {
            constexpr int oi = decltype(OC)::value, om = oi - 3;
            constexpr int nm = 8 - (om > 0 ? (om + 3) / 4 : 0) + ((om >= 0 && (om & 3) == 0) ? 1 : 0);
            if constexpr (oi % 4 == 0) STAMP3(8 + oi / 4);
            if constexpr (oi == 9) { bar(); SB(); }
            if constexpr (oi + 3 < 32) B[oi + 3] = *(LAS v4u*)(bb - (om + 3) * 128);
#pragma unroll
            for (int cb_ = 7; cb_ >= 0; --cb_)
                if (4 * cb_ - om >= 0) acc[cb_] = mfma16(A[4 * cb_ - om], B[oi], oi == 0 ? f32x4{0, 0, 0, 0} : acc[cb_]);
            if constexpr (om >= 0 && (om & 3) == 0) acc[om >> 2] = mfma16(AD, B[oi], acc[om >> 2]);
            if constexpr (oi < 2) { sg[2 * oi] = ld128_asm<true>(c.rws, tid * 16, OFF_ST0 + oi * 8192); sg[2 * oi + 1] = ld128_asm<true>(c.rws, tid * 16, OFF_ST0 + oi * 8192 + 4096); }
            if constexpr (oi == 2 || oi == 3) {
                asm volatile("buffer_load_dwordx2 %0, %2, %3, %4 offen offset:%5 sc1\n\tbuffer_load_dwordx2 %1, %2, %3, %4 offen offset:%6 sc1"
                             : "=&v"(xg[2 * oi - 4]), "=&v"(xg[2 * oi - 3]) : "v"(s4 * 8), "s"(c.rws), "s"(xo), "i"((oi - 2) * 1024), "i"((oi - 2) * 1024 + 512) : "memory");
            }
            if constexpr (oi < NW1) w_read1<oi>(cs_, wl, W1, W2);
            if constexpr (oi >= 4 && (oi & 1) == 0 && NW1 + (oi - 4) / 2 < 12) w_dma<NW1 + (oi - 4) / 2>(cs_, wt, w_soff<NW1 + (oi - 4) / 2>(cs_));
            if constexpr (oi == 7) {
                asm volatile("s_waitcnt vmcnt(4)" : "+v"(sg[0]), "+v"(sg[1]), "+v"(sg[2]), "+v"(sg[3]), "+v"(xg[0]), "+v"(xg[1]), "+v"(xg[2]), "+v"(xg[3]) :: "memory");
                auto bad = [&]() { u32 d = 0; for (int t = 0; t < 4; ++t) d = tag_bad(sg[t], c.tagA, c.tagB, d); return ANY(d != 0u); };
                if (bad()) {
                    unsigned spins = 0;
                    NOUNROLL do {
                        MEMBAR();
#pragma unroll
                        for (int t = 0; t < 4; ++t) sg[t] = ld128(c.rws, tid * 16, OFF_ST0 + t * 4096);
                    } while (bad() && !spin_fail(c, spins, 6u));
                }
#pragma unroll
                for (int t = 0; t < 4; ++t) *(LAS v2u*)(c.L + CV_ST + (tid + 256 * t) * 8) = v2u{sg[t].x, sg[t].z};
            }
            if constexpr (oi >= 9 && oi < 13) sg[oi - 9] = *(LAS v4u*)(c.L + CV_ST + s4 * 16 + (oi - 9) * 1024);
            if constexpr (oi >= 11 && oi < 19) {
                constexpr int t = (oi - 11) >> 1, hf = (oi - 11) & 1;
                LV(xg[t]); LV(sg[t]);
                const u32 xw = xg[t][hf];
                const u32 sa = sg[t][2 * hf], sb = sg[t][2 * hf + 1];
                xs[t][2 * hf] = bflo(xw); xs[t][2 * hf + 1] = bfhi(xw);
                qs[t][2 * hf] = (xs[t][2 * hf] - bflo(sa)) * __builtin_amdgcn_rcpf(bfhi(sa));
                qs[t][2 * hf + 1] = (xs[t][2 * hf + 1] - bflo(sb)) * __builtin_amdgcn_rcpf(bfhi(sb));
                asm volatile("" : "+v"(qs[t][2 * hf]), "+v"(qs[t][2 * hf + 1]));
            }
            if constexpr (oi == 23 || oi == 25) {
                const v4u ff = {~0u, ~0u, ~0u, ~0u};
                st128(ff, c.rws, lane * 16, OFF_X3 + (cs_.grp * 56 + (oi - 23) * 14 + 4 * cs_.j + cs_.w) * 1024);
            }
            ilv<nm, 2>();
            SB();
        });
        STAMP(5); STAMP3(16);
        LAS char* ex = c.L;
        f32x4 fin[4];
        if (p == 0) {
#pragma unroll
            for (int t = 0; t < 4; ++t) { *(LAS f32x4*)(ex + w * 4096 + t * 1024 + lane * 16) = acc[4 + t]; fin[t] = acc[t]; }
        } else {
#pragma unroll
            for (int t = 0; t < 4; ++t) { *(LAS f32x4*)(ex + w * 4096 + t * 1024 + lane * 16) = acc[t]; fin[t] = acc[4 + t]; }
        }
        bar();
        STAMP3(17);
        const u32 toff = (u32)((32 * p + 8 * (q & 1) + (i16 >> 1)) * 28672 + ch * 64 + (i16 & 1) * 32 + (q >> 1) * 16);
        v2u y[4], x1[4];
#pragma unroll
        for (int t = 0; t < 4; ++t) {
            fin[t] += *(LAS f32x4*)(ex + (w ^ 1) * 4096 + t * 1024 + lane * 16);
            y[t] = v2u{pk(fin[t][0], fin[t][1]), pk(fin[t][2], fin[t][3])};
            x1[t] = v2u{pk(__builtin_fmaf(qs[t][0], bflo(y[t].x), xs[t][0]), __builtin_fmaf(qs[t][1], bfhi(y[t].x), xs[t][1])),
                        pk(__builtin_fmaf(qs[t][2], bflo(y[t].y), xs[t][2]), __builtin_fmaf(qs[t][3], bfhi(y[t].y), xs[t][3]))};
        }
        auto merge = [&](const v2u& a, const v2u& b_) {
            auto r0 = __builtin_amdgcn_permlane16_swap(a.x, b_.x, false, false);
            auto r1 = __builtin_amdgcn_permlane16_swap(a.y, b_.y, false, false);
            return v4u{r0[0], r1[0], r0[1], r1[1]};
        };
        st128(merge(x1[0], x1[1]), c.rws, toff, OFF_X1);
        st128(merge(x1[2], x1[3]), c.rws, toff, OFF_X1 + 16 * 28672);
        st128(merge(y[0], y[1]), c.rws, toff, OFF_YT);
        st128(merge(y[2], y[3]), c.rws, toff, OFF_YT + 16 * 28672);
        STAMP(6); STAMP3(18);
        sfor<12, 12 + NW1>([&](auto NC) { w_dma<decltype(NC)::value>(cs_, wt, w_soff<decltype(NC)::value>(cs_)); });
        WAIT_VM(6 + 2 * NW1);
        sfor<NW1, 12>([&](auto NC) { w_read1<decltype(NC)::value>(cs_, wl, W1, W2); });
        WAIT_LGKM(0);
        sfor<12 + NW1, 20>([&](auto NC) { w_dma<decltype(NC)::value>(cs_, wt, w_soff<decltype(NC)::value>(cs_)); });
        STAMP3(19);
        WAIT_VM(16);
        bar();
        if (tid < 8) __builtin_amdgcn_raw_buffer_store_b64(v2u{c.tagA, c.tagB}, c.rws, (u32)(tid * 2048 + wb * 8), OFF_FL, AUX_SC1);
        MEMBAR();
        STAMP(7);
        sfor<20, 24>([&](auto NC) { w_dma<decltype(NC)::value>(cs_, wt, w_soff<decltype(NC)::value>(cs_)); });
        WAIT_VM(12);
        sfor<12, 18>([&](auto NC) { w_read1<decltype(NC)::value>(cs_, wl, W1, W2); });
        WAIT_LGKM(0);
        sfor<24, 28>([&](auto NC) { w_dma<decltype(NC)::value>(cs_, wt, w_soff<decltype(NC)::value>(cs_)); });
        STAMP3(21);
    }
    Ad ad;
    ad.l16 = lane_id() * 16;
    ad.hst = OFF_H + (c.grp * 224 + 16 * c.j + 4 * w) * 1024;
    ad.x3o = OFF_X3 + (c.grp * 56 + 4 * c.j + w) * 1024;
    ad.sto = OFF_ST + c.grp * 2 * ST_HG;
    asm volatile("" : "+s"(ad.hst), "+s"(ad.x3o), "+s"(ad.sto));
    const u32 swl = wl2_of(c, ad.l16 >> 4);
    const WSt w2t = w_st<28>(c, ad.l16 >> 4);
    u32 wso[7];
#pragma unroll
    for (int m = 0; m < 7; ++m) {
        const int mm = member(c, m);
        wso[m] = (256 * mm + 64 * w) * (D * 2) + 128 * c.j; ad.so[m] = ad.hst + (mm - c.j) * 16384;
        asm volatile("" : "+s"(wso[m]), "+s"(ad.so[m]));
    }
    auto w2_dma = [&](auto NC) { constexpr int n = decltype(NC)::value; w_dma<n>(c, w2t, wso[k_mem(w2_step_of(n))] + k_sub(w2_step_of(n)) * 16 * (D * 2)); };
    {
        v4u cg = ld128_asm<true>(c.rws, tok32(ad), OFF_CSG + c.j * 2048 + w * 512);
        sfor<28, 30>(w2_dma);
        WAIT_VM(12);
        w_read<18, 24>(c, W1, W2);
        STAMP(8); STAMP3(22);
        if (w == 0) {
            unsigned spins = 0;
            NOUNROLL for (;;) {
                MEMBAR();
                const v4u f0 = ld128(c.rws, ad.l16, OFF_FL + (wb & 7) * 2048);
                const v4u f1 = ld128(c.rws, 1024 + min(ad.l16, 752u), OFF_FL + (wb & 7) * 2048);
                const u32 d = xor_or(f0.x, c.tagA, xor_or(f0.z, c.tagA, xor_or(f1.x, c.tagA, xor_or(f1.z, c.tagA, tag_bad(f0, c.tagB, c.tagB, tag_bad(f1, c.tagB, c.tagB, 0u))))));
                if (!ANY(d != 0u)) break;
                if (spin_fail(c, spins, 7u)) break;
            }
            if (wb == 0 && ad.l16 == 0) __builtin_amdgcn_raw_buffer_store_b64(v2u{c.tagA, c.tagB}, c.rws, 0, OFF_EP, AUX_SC1);
        }
        bar();
        STAMP(9); STAMP3(23);
        {
            const u32 xb = c.lbase + L_X + w * 7168, xs_ = OFF_X1 + c.grp * 57344 + w * 7168;
            sfor<0, 14>([&](auto IC) {
                constexpr int i = decltype(IC)::value, o = (i / 7) * 28672 + (i % 7) * 1024;
                dma16<true>(xb + o, ad.l16, c.rws, xs_ + o);
            });
        }
        STAMP3(24);
        WAIT_VM(14);
        w_read<24, 28>(c, W1, W2);
        w_read1<28>(c, swl, W1, W2); w_read1<29>(c, swl, W1, W2);
        w_neg<28>(W2); w_neg<29>(W2);
        LV(cg);
        if (ANY(tag_bad(cg, c.tagA, c.tagB, 0u) != 0u)) {
            unsigned spins = 0;
            NOUNROLL do { MEMBAR(); cg = ld128(c.rws, tok32(ad), OFF_CSG + c.j * 2048 + w * 512); } while (ANY(tag_bad(cg, c.tagA, c.tagB, 0u) != 0u) && !spin_fail(c, spins, 8u));
        }
        *(LAS v2u*)(c.L + L_CS + w * 256 + (tok32(ad) >> 1)) = v2u{cg.x, cg.z};
        WAIT_VM(7);
        bar();
        STAMP(10); STAMP3(25);
    }
    STAMP3(26);
    pin_a<0, 25>(W1);
    v4u hb[56], xres[2];
    G1Out ga, gb;
    v2u gate;
    f32x4 csq[3];
    St1 st;
    ad.ca = L_CS + (w * 64 + (ad.l16 >> 9) * 4) * 4;
    using M0 = std::integral_constant<int, 0>; using M1 = std::integral_constant<int, 1>;
    {
        st.ad = xtr_of(ad.l16 >> 4) + w * 7168;
        auto stats = [&](auto MC, auto HC, auto FC, G1Out& o) {
            constexpr int H = decltype(HC)::value, f = decltype(FC)::value;
            constexpr bool TR = decltype(MC)::value == 0;
            if constexpr (f == 0) { st.s1 = 0.f; st.s2 = 0.f; }
            if constexpr (f >= 2 && f < 23) { constexpr int ph = (f - 2) % 3, k = (f - 2) / 3; st1_acc<k, ph * 3, ph == 2 ? 8 : ph * 3 + 3>(st); }
            if constexpr (f % 3 == 0 && f < 21) st1_read<TR, H, f / 3>(c, st);
            if constexpr (f == 23) part_put(c, ad, L_SP + H * 1024, st.s1, st.s2);
            if constexpr (f == 24) { SB(); asm volatile("s_waitcnt lgkmcnt(%0)\n\ts_barrier" :: "i"(TR ? 2 : 1) : "memory"); }
            if constexpr (f == 25) { const f32x2 t = part_get(c, ad, L_SP + H * 1024); ln_scalars(t[0], t[1], o.mb, o.r); asm volatile("" : "+v"(o.mb), "+v"(o.r)); }
        };
        auto stage = [&](auto GC_, auto XC) {
            constexpr int g = decltype(GC_)::value, extra = decltype(XC)::value;
            if constexpr (g >= 15 && g < 43 && (g & 1)) w_neg<30 + (g - 15) / 2>(W2);
            if constexpr (g == 41) *(LAS v4u*)(c.L + (ad.l16 + w * 1024) + L_JK) = W2[15][1];
            if constexpr (g >= 13 && g < 41 && (g & 1)) { WAIT_VM(12 + extra); w_read1<30 + (g - 13) / 2>(c, swl, W1, W2); }
            if constexpr (g < 52 && (g & 1) == 0) w2_dma(std::integral_constant<int, 30 + g / 2>{});
        };
        constexpr unsigned E2 = 0xAAAAAAAu;
        g1_main<true, 0, PS0, 0x9AAAAAAu>(c, ad, W1, ga.acc, [&](auto FC) {
            constexpr int f = decltype(FC)::value;
            stage(FC, M0{});
            stats(M0{}, M0{}, FC, ga);
            if constexpr (f >= 26) csq[f - 26] = epi_cs(c, ad, f - 26);
        });
        STAMP(11);
        auto side_b = [&](auto MC, auto FC) {
            constexpr int f = decltype(FC)::value, M = decltype(MC)::value;
            if constexpr (f < 6) csq[(f + 2) % 3] = epi_cs(c, ad, f + 2);
            if constexpr (f < 8) g1_epi<0, M, f>(c, ad, ga, csq[f % 3], hb[f >> 1]);
            if constexpr (f < 8 && (f & 1)) h_store<0, f / 2>(c, ad, hb[f >> 1]);
            if constexpr (f == 18) gate = gate_ld<true>(c, ad, 0);
            if constexpr (f == 19) xres[0] = xres_ld<0, M>(c, ad);
            if constexpr (f == 23) {
                SB();
                asm volatile("s_waitcnt vmcnt(%1)" : "+v"(gate) : "i"(M == 0 ? 4 : 0) : "memory");
                gate_wait<M>(c, ad, 0, gate);
            }
            if constexpr (f >= 24) hb[f - 20] = h_next<f - 20>(c, ad);
            if constexpr (f >= 26) csq[f - 26] = epi_cs(c, ad, f - 26);
        };
        const u32 ra = L_W + w * 24576 + 1024 + ad.l16;
        v4u rm;
        g1_main<true, 1, PS0 + 7, 0x9AAAAAAu>(c, ad, W1, gb.acc, [&](auto FC) {
            constexpr int f = decltype(FC)::value;
            stage(std::integral_constant<int, 28 + f>{}, std::integral_constant<int, (f < 8 ? f / 2 : 4)>{});
            if constexpr (f >= 16 && f < 28 && (f & 1) == 0) { LV(rm); *(LAS v4u*)(c.L + ra + ((8 + (f - 16) / 2) % 12) * 2048) = rm ^ SGN; }
            if constexpr (f >= 14 && f < 26 && (f & 1) == 0) { WAIT_VM(f < 24 ? 14 : 12); rm = *(LAS v4u*)(c.L + ra + ((8 + (f - 14) / 2) % 12) * 2048); }
            stats(M0{}, M1{}, FC, gb);
            side_b(M0{}, FC);
        });
        STAMP(12);
        {
            WAIT_VM(4);
            v4u t[6];
#pragma unroll
            for (int i = 0; i < 6; ++i) t[i] = *(LAS v4u*)(c.L + ra + ((14 + i) % 12) * 2048);
#pragma unroll
            for (int i = 0; i < 6; ++i) *(LAS v4u*)(c.L + ra + ((14 + i) % 12) * 2048) = t[i] ^ SGN;
            SB();
        }
        pin_a<0, 25>(W1);
        STAMP2(PS0 + 14);
        STAMP(13);
        g2_pair<0>(c, ad, W2, hb, gb, csq, ga.r, xres, c.rws);
        pin_a<0, 25>(W1);
        STAMP(14);
        v4u t[7];
        st.ad = ad.l16 + w * 7168;
        STAMP4(6);
        bar();
        STAMP4(7);
        STAMP(17);
        g1_main<false, 0, PS1, 0x9AAAAAAu>(c, ad, W1, ga.acc, [&](auto FC) {
            constexpr int f = decltype(FC)::value;
            stats(M1{}, M0{}, FC, ga);
            if constexpr (f >= 12 && f < 19) t[f - 12] = x3_ld1<1, f - 12>(c, ad);
            if constexpr (f == 24) x3_wait<1>(c, ad, t);
            if constexpr (f >= 25) { x3_put1<1, 2 * (f - 25)>(c, ad, t[2 * (f - 25)]); x3_put1<1, 2 * (f - 25) + 1>(c, ad, t[2 * (f - 25) + 1]); }
            if constexpr (f == 27) x3_put1<1, 6>(c, ad, t[6]);
            if constexpr (f >= 26) csq[f - 26] = epi_cs(c, ad, f - 26);
        });
        STAMP4(8);
        STAMP4(9);
        STAMP4(10);
        STAMP(18);
        bar();
        STAMP4(11);
        g1_main<false, 1, PS1 + 7, 0x9AAAAAAu>(c, ad, W1, gb.acc, [&](auto FC) {
            stats(M1{}, M1{}, FC, gb);
            side_b(M1{}, FC);
        });
        (void)E2;
        STAMP(19);
        STAMP2(PS1 + 14);
        STAMP(20);
        const __attribute__((address_space(4))) char* ka = (const __attribute__((address_space(4))) char*)__builtin_amdgcn_kernarg_segment_ptr();
        asm volatile("" : "+s"(ka));
        g2_pair<1>(c, ad, W2, hb, gb, csq, ga.r, xres, mkrs(*(void* const __attribute__((address_space(4)))*)(ka + 72), TSZ));
        STAMP(21);
    }
    asm volatile(".Lfk_end:");
}

extern "C" void kernel_launch(void* const* d_in, const int* in_sizes, int n_in,
                              void* d_out, int out_size, void* d_ws, size_t ws_size,
                              hipStream_t stream) {
    (void)hipFuncSetAttribute((const void*)fused_kernel, hipFuncAttributeMaxDynamicSharedMemorySize, L_TOT);
    fused_kernel<<<256, 256, L_TOT, stream>>>((const u16*)d_in[0], (const u16*)d_in[1], (const u16*)d_in[2], (const u16*)d_in[3], (const u16*)d_in[4],
                                              (const u16*)d_in[5], (const u16*)d_in[7], (const u16*)d_in[8], (unsigned char*)d_ws, (u16*)d_out);
}
```

```cpp
#include <hip/hip_runtime.h>
#include <stdint.h>
#include <type_traits>

typedef unsigned short u16;
typedef unsigned u32;
typedef u32 v4u __attribute__((ext_vector_type(4)));
typedef u32 v2u __attribute__((ext_vector_type(2)));
typedef float f32x2 __attribute__((ext_vector_type(2)));
typedef float f32x4 __attribute__((ext_vector_type(4)));
typedef float f32x16 __attribute__((ext_vector_type(16)));
typedef __bf16 bf16x8 __attribute__((ext_vector_type(8)));
typedef __bf16 bf16x2 __attribute__((ext_vector_type(2)));
typedef short s16x4 __attribute__((ext_vector_type(4)));
typedef short s16x2 __attribute__((ext_vector_type(2)));
typedef unsigned short u16x2 __attribute__((ext_vector_type(2)));
#define LAS __attribute__((address_space(3)))
#define GAS __attribute__((address_space(1)))
#define DI __device__ __forceinline__
#define RLX_AGENT __ATOMIC_RELAXED, __HIP_MEMORY_SCOPE_AGENT
#define SB() __builtin_amdgcn_sched_barrier(0)
#define NOUNROLL _Pragma("unroll 1")
#define MEMBAR() asm volatile("" ::: "memory")
template <class T> __device__ __forceinline__ void LV(T& x) { asm volatile("" : "+v"(x)); }
template <class T> __device__ __forceinline__ void LA(T& x) { asm volatile("" : "+a"(x)); }
#define ANY(x) (__builtin_amdgcn_ballot_w64(x) != 0ull)

constexpr int S = 2048, D = 448, F = 1792;
constexpr float EPS = 1e-5f;
constexpr u32 TSZ = S * D * 2;
constexpr u32 OFF_EP  = 0;
constexpr u32 OFF_FL  = 4096;
constexpr u32 OFF_ST0 = OFF_FL + 16384;
constexpr u32 OFF_CSG = OFF_ST0 + 16384;
constexpr u32 OFF_ST  = OFF_CSG + 16384;
constexpr u32 OFF_KVG = 1048576;
constexpr u32 OFF_X0G = OFF_KVG + 2 * TSZ;
constexpr u32 OFF_X1  = OFF_X0G + 2 * TSZ;
constexpr u32 OFF_YT  = OFF_X1 + TSZ;
constexpr u32 OFF_X3  = OFF_YT + TSZ;
constexpr u32 OFF_H   = OFF_X3 + TSZ;
constexpr u32 WS_BYTES = OFF_H + 64 * 112 * 1024;
static_assert(OFF_ST + 64 * 24 * 512 <= OFF_KVG, "ws map");
#define STAMP(i) do {} while (0)
#define STAMP2(i) do {} while (0)
#define STAMP4(i) do {} while (0)
#define STAMP3(i) do {} while (0)
constexpr int RSRC_W3 = 0x00020000;
constexpr int AUX_SC1 = 16;
constexpr unsigned SPIN_CAP = 1u << 14;
constexpr u32 SGN = 0x80008000u;

template <int I, int N, class Fn> DI void sfor(Fn&& fn) { if constexpr (I < N) { fn(std::integral_constant<int, I>{}); sfor<I + 1, N>(fn); } }

DI float bflo(u32 x) { return __uint_as_float(x << 16); }
DI float bfhi(u32 x) { return __uint_as_float(x & 0xffff0000u); }
DI u32 pk(float a, float b) { f32x2 v = {a, b}; return __builtin_bit_cast(u32, __builtin_convertvector(v, bf16x2)); }
DI float rbf(float a) { return bflo(pk(a, a)); }
DI __amdgpu_buffer_rsrc_t mkrs(const void* p, u32 bytes) { return __builtin_amdgcn_make_buffer_rsrc((void*)p, 0, (int)bytes, RSRC_W3); }
DI f32x4 mfma16(v4u a, v4u b, f32x4 c) { return __builtin_amdgcn_mfma_f32_16x16x32_bf16(__builtin_bit_cast(bf16x8, a), __builtin_bit_cast(bf16x8, b), c, 0, 0, 0); }
DI f32x16 mfma32(v4u a, v4u b, f32x16 c) { return __builtin_amdgcn_mfma_f32_32x32x16_bf16(__builtin_bit_cast(bf16x8, a), __builtin_bit_cast(bf16x8, b), c, 0, 0, 0); }
DI f32x16 zero16() { f32x16 z; for (int i = 0; i < 16; ++i) z[i] = 0.f; return z; }
DI v2u trd(LAS char* p) { return __builtin_bit_cast(v2u, __builtin_amdgcn_ds_read_tr16_b64_v4i16((LAS s16x4*)p)); }
DI v4u trd2(LAS char* p, int second) { v2u a = trd(p), b = trd(p + second); return v4u{a.x, a.y, b.x, b.y}; }
DI float dot2(u32 a, u32 b, float c) { return __builtin_amdgcn_fdot2_f32_bf16(__builtin_bit_cast(bf16x2, a), __builtin_bit_cast(bf16x2, b), c, false); }
DI v4u ld128(__amdgpu_buffer_rsrc_t rs, u32 vo, u32 so) { return __builtin_amdgcn_raw_buffer_load_b128(rs, vo, so, AUX_SC1); }
template <bool SC1, int OFF = 0> DI v4u ld128_asm(__amdgpu_buffer_rsrc_t rs, u32 vo, u32 so) {
    v4u r;
    if constexpr (SC1) asm volatile("buffer_load_dwordx4 %0, %1, %2, %3 offen offset:%4 sc1" : "=v"(r) : "v"(vo), "s"(rs), "s"(so), "i"(OFF) : "memory");
    else asm volatile("buffer_load_dwordx4 %0, %1, %2, %3 offen offset:%4" : "=v"(r) : "v"(vo), "s"(rs), "s"(so), "i"(OFF) : "memory");
    return r;
}
template <int OFF = 0> DI void st128(v4u v, __amdgpu_buffer_rsrc_t rs, u32 vo, u32 so) {
    asm volatile("buffer_store_dwordx4 %0, %1, %2, %3 offen offset:%4 sc1\n\ts_nop 1" :: "v"(v), "v"(vo), "s"(rs), "s"(so), "i"(OFF) : "memory");
}
DI int lane_id() { int l; asm volatile("v_mbcnt_lo_u32_b32 %0, -1, 0\n\tv_mbcnt_hi_u32_b32 %0, -1, %0" : "=v"(l)); return l; }
DI float hsum(float a) {
    const u32 v = __float_as_uint(a);
    auto t = __builtin_amdgcn_permlane32_swap(v, v, false, false);
    return __uint_as_float(t[0]) + __uint_as_float(t[1]);
}
DI float ln_sd(float var) { return rbf(__builtin_amdgcn_sqrtf(rbf(rbf(var) + EPS))); }
DI void ln_scalars(float s1, float s2, float& m, float& r) {
    m = s1 * (1.0f / D);
    r = __builtin_amdgcn_rsqf(fmaxf(__builtin_fmaf(-m, m, s2 * (1.0f / D)), 0.f) + EPS);
}
DI u32 xor_or(u32 a, u32 b, u32 c) { return __builtin_amdgcn_bitop3_b32(a, b, c, 0xBE); }
DI u32 tag_bad(const v4u& g, u32 tA, u32 tB, u32 acc) { return xor_or(g.w, tB, xor_or(g.y, tA, acc)); }
template <bool SC1> DI void dma16(u32 ldsaddr, u32 voff, __amdgpu_buffer_rsrc_t rs, u32 soff) {
    if constexpr (SC1) asm volatile("s_mov_b32 m0, %0\n\ts_nop 0\n\tbuffer_load_dwordx4 %1, %2, %3 offen sc1 lds" :: "s"(ldsaddr), "v"(voff), "s"(rs), "s"(soff) : "memory", "m0");
    else asm volatile("s_mov_b32 m0, %0\n\ts_nop 0\n\tbuffer_load_dwordx4 %1, %2, %3 offen lds" :: "s"(ldsaddr), "v"(voff), "s"(rs), "s"(soff) : "memory", "m0");
}
template <int LI> DI void dma_pair(u32 ldsbase, u32 vo0, u32 vo1, __amdgpu_buffer_rsrc_t rs, u32 soff) {
    asm volatile("s_add_u32 m0, %0, %5\n\ts_nop 0\n\tbuffer_load_dwordx4 %1, %3, %4 offen lds\n\tbuffer_load_dwordx4 %2, %3, %4 offen offset:1024 lds"
                 :: "s"(ldsbase), "v"(vo0), "v"(vo1), "s"(rs), "s"(soff), "i"(LI) : "memory", "m0", "scc");
}
DI void touch(u32 junk, u32 voff, __amdgpu_buffer_rsrc_t rs, u32 soff) {
    asm volatile("s_mov_b32 m0, %0\n\ts_nop 0\n\tbuffer_load_dword %1, %2, %3 offen lds" :: "s"(junk), "v"(voff), "s"(rs), "s"(soff) : "memory", "m0");
}
#define WAIT_VM(n) asm volatile("s_waitcnt vmcnt(%0)" :: "i"(n) : "memory")
#define WAIT_LGKM(n) asm volatile("s_waitcnt lgkmcnt(%0)" :: "i"(n) : "memory")
DI void bar() { asm volatile("s_waitcnt lgkmcnt(0)\n\ts_barrier" ::: "memory"); }
DI int wrap7(int x) { return x >= 7 ? x - 7 : x; }
template <int N, int K> DI void ilv() { if constexpr (N > 0) { __builtin_amdgcn_sched_group_barrier(0x8, 1, 0); __builtin_amdgcn_sched_group_barrier(0x366, K, 0); ilv<N - 1, K>(); } }

constexpr int L_X = 0;
constexpr int L_W = 57344;
constexpr int L_M = L_W + 98304;
constexpr int L_SP = L_M;
constexpr int L_CS = L_M + 2048;
constexpr int L_S2 = L_M + 3072;
constexpr int L_JK = L_M + 4096;
constexpr int L_TOT = L_M + 8192;
constexpr int T_K = 0, T_V = 9216, T_X = 17408, T_S = L_JK;
constexpr int CV_B = 25600;
constexpr int CV_HE = 0, CV_HO = 4160, CV_KV = CV_HO + 4160, CV_CH = CV_KV + 4608;
constexpr int CV_ST = 16384;
static_assert(CV_B + 2 * CV_CH <= L_W, "lds map");

struct Ctx {
    LAS char* L;
    __amdgpu_buffer_rsrc_t rws, rw1, rw2;
    GAS u32* tmo;
    int w, j, grp, kb0, gi;
    u32 lbase, tagA, tagB;
};
DI Ctx fresh(const Ctx& c0) { Ctx c = c0; asm volatile("" : "+s"(c.w), "+s"(c.j), "+s"(c.grp), "+s"(c.kb0), "+s"(c.gi)); return c; }
DI bool spin_fail(const Ctx& c, unsigned& spins, u32 code) {
    if (++spins > SPIN_CAP) { __hip_atomic_store(c.tmo, code, RLX_AGENT); return true; }
    __builtin_amdgcn_s_sleep(1);
    return false;
}
DI u32 xtr_of(int lane) { return (u32)(((lane >> 5) * 4 + ((lane >> 2) & 3)) * 64 + ((lane >> 4) & 1) * 32 + (lane & 3) * 8); }
DI u32 wl1_of(const Ctx& c, int lane) {
    const int rr = (lane >> 5) * 4 + ((lane >> 2) & 3);
    return (u32)(L_W + c.w * 24576 + rr * 128 + ((rr >> 1) & 1) * 64 + ((lane >> 4) & 1) * 32 + (lane & 3) * 8);
}
DI u32 wl2_of(const Ctx& c, int lane) { return wl1_of(c, lane) ^ (u32)((c.w >> 1) * 64 + (c.w & 1) * 32); }
DI int member(const Ctx& c, int m) {
    if (m == 0) return c.j;
    int t = m - 1 + 2 * c.gi; t = t >= 6 ? t - 6 : t; t = t >= 6 ? t - 6 : t;
    return wrap7(c.j + 1 + t);
}
typedef v4u W1T[28][2];
typedef v4u W2T[16][2];
typedef f32x16 AccT[2];

DI constexpr int k_mem(int f) { return f < 4 ? 0 : 1 + (f - 4) % 6; }
DI constexpr int k_sub(int f) { return f < 4 ? f : (f - 4) / 6; }
DI constexpr int w2_step_of(int n) { return n < 40 ? 2 * (n - 28) : (n < 44 ? n - 16 : 2 * (n - 44) + 1); }
template <int N> DI u32 w_soff(const Ctx& c) {
    if constexpr (N < 28) return (N % 7 + 7 * ((N / 7 + c.kb0) & 3)) * 16 * (F * 2) + (256 * c.j + 64 * c.w) * 2;
    else return (256 * member(c, k_mem(w2_step_of(N))) + 64 * c.w + 16 * k_sub(w2_step_of(N))) * (D * 2) + 128 * c.j;
}
template <int N> DI u32 w_voff(int lane) { return (lane >> 3) * ((N < 28 ? F : D) * 2) + ((lane & 7) ^ (((lane >> 4) & 1) << 2)) * 16; }
struct WSt { u32 ring, vo0, vo1; };
template <int N> DI WSt w_st(const Ctx& c, int lane) { WSt t; t.ring = c.lbase + L_W + c.w * 24576; t.vo0 = w_voff<N>(lane); t.vo1 = t.vo0 + 8 * ((N < 28 ? F : D) * 2) - 1024; return t; }
template <int N> DI void w_dma(const Ctx& c, const WSt& t, u32 so) { dma_pair<(N % 12) * 2048>(t.ring, t.vo0, t.vo1, N < 28 ? c.rw1 : c.rw2, so); }
DI v4u w_frag(const Ctx& c, u32 wl, int slot, int t) { return trd2(c.L + (wl ^ (u32)(t * 64)) + slot * 2048, 1024); }
template <int N> DI void w_read1(const Ctx& c, u32 wl, W1T& W1, W2T& W2) {
#pragma unroll
    for (int t = 0; t < 2; ++t) {
        if constexpr (N < 28) W1[N][t] = w_frag(c, wl, N % 12, t);
        else W2[N - 28][t] = w_frag(c, wl, N % 12, t);
    }
}
template <int N> DI void w_neg(W2T& W2) {
#pragma unroll
    for (int t = 0; t < 2; ++t) { LV(W2[N - 28][t]); W2[N - 28][t].z ^= SGN; W2[N - 28][t].w ^= SGN; asm volatile("" : "+v"(W2[N - 28][t])); }
}
template <int N0, int N1> DI void w_read(const Ctx& c0, W1T& W1, W2T& W2) {
    const Ctx c = fresh(c0);
    const u32 wl = wl1_of(c, lane_id());
    sfor<N0, N1>([&](auto NC) { w_read1<decltype(NC)::value>(c, wl, W1, W2); });
}

constexpr int PS0 = 0, PS1 = 33;
template <int N0, int N1> DI void pin_a(W1T& W1) {
    if constexpr (N0 < N1) { asm volatile("" : "+a"(W1[N0][0]), "+a"(W1[N0][1])); pin_a<N0 + 1, N1>(W1); }
}
struct G1Out { AccT acc; float mb, r; };
DI constexpr bool reg_start(unsigned long long end, int f) { return f == 0 || ((end >> (f - 1)) & 1ull); }
DI constexpr int reg_len(unsigned long long end, int f) { int n = 1; while (!reg_start(end, f - n + 1)) ++n; return n; }
struct Ad {
    u32 l16;
    u32 ca;
    u32 hst;
    u32 x3o;
    u32 sto;
    u32 so[7];
};
constexpr u32 ST_HG = 3 * 4096;
DI u32 tok32(const Ad& a) { return a.l16 & 496u; }

template <bool TR, int HALF, int S0, unsigned END, class Side>
DI void g1_main(const Ctx& c, Ad& a, const W1T& W1, AccT& acc, Side&& side) {
    const u32 lp = TR ? xtr_of(a.l16 >> 4) : a.l16;
    int kb = c.kb0; asm volatile("" : "+s"(kb));
    u32 la = 0;
    auto ldb = [&](int f) {
        if (f % 7 == 0) la = lp + ((f / 7 + kb) & 3) * 7168;
        LAS char* p = c.L + la + (L_X + HALF * 28672 + (f % 7) * 1024);
        if constexpr (TR) return trd2(p, 512);
        else return *(LAS v4u*)p;
    };
    v4u B[28];
    B[0] = ldb(0); B[1] = ldb(1); B[2] = ldb(2);
    SB();
    sfor<0, 28>([&](auto FC) {
        constexpr int f = decltype(FC)::value;
        if constexpr (f % 4 == 0) STAMP2(S0 + f / 4);
        if constexpr (reg_start(END, f)) asm volatile("" : "+v"(a.l16), "+v"(a.ca));
        acc[0] = mfma32(W1[f][0], B[f], f == 0 ? zero16() : acc[0]);
        acc[1] = mfma32(W1[f][1], B[f], f == 0 ? zero16() : acc[1]);
        if constexpr (f + 3 < 28) B[f + 3] = ldb(f + 3);
        side(FC);
        if constexpr ((END >> f) & 1u) { ilv<2 * reg_len(END, f), 5>(); SB(); }
    });
}
struct St1 { u32 ad; v4u fr[2]; float s1, s2; };
template <bool TR, int HALF, int K> DI void st1_read(const Ctx& c, St1& s) {
    if constexpr (TR) s.fr[K & 1] = trd2(c.L + s.ad + (L_X + HALF * 28672 + K * 1024), 512);
    else s.fr[K & 1] = *(LAS v4u*)(c.L + s.ad + (L_X + HALF * 28672 + K * 1024));
}
template <int K, int E0, int E1> DI void st1_acc(St1& s) {
#pragma unroll
    for (int e = E0; e < E1; ++e) { if (e == E0) LV(s.fr[K & 1]); const u32 x = s.fr[K & 1][e >> 1]; if (e & 1) s.s2 = dot2(x, x, s.s2); else s.s1 = dot2(x, 0x3f803f80u, s.s1); }
}
DI void part_put(const Ctx& c, const Ad& a, int off, float s1, float s2) { *(LAS f32x2*)(c.L + off + tok32(a) * 2 + c.w * 8) = f32x2{hsum(s1), hsum(s2)}; }
DI f32x2 part_get(const Ctx& c, const Ad& a, int off) {
    const f32x4 u = *(LAS f32x4*)(c.L + off + tok32(a) * 2), v = *(LAS f32x4*)(c.L + off + tok32(a) * 2 + 16);
    return f32x2{(u[0] + u[2]) + (v[0] + v[2]), (u[1] + u[3]) + (v[1] + v[3])};
}
DI void frag_sums(const v4u& p, float& s1, float& s2) {
    s1 = 0.f; s2 = 0.f;
#pragma unroll
    for (int e = 0; e < 4; ++e) { s1 = dot2(p[e], 0x3f803f80u, s1); s2 = dot2(p[e], p[e], s2); }
}
template <int HALF, int KIND> DI void stat_pub(const Ctx& c, const Ad& a, int off, int who) {
    if (c.w == who) {
        f32x2 t = part_get(c, a, off);
        const bool z = c.j == 6 && a.l16 >= 512u;
        st128(v4u{z ? 0u : __float_as_uint(t[0]), c.tagA, z ? 0u : __float_as_uint(t[1]), c.tagB}, c.rws, z ? a.l16 : tok32(a), a.sto + HALF * ST_HG + KIND * 4096 + c.j * 512);
    }
}
template <bool DO> DI void stat_pub_if(const Ctx& c, const Ad& a) { if constexpr (DO) stat_pub<0, 0>(c, a, L_S2, 0); }
struct Gath { v4u g[4]; };
template <int HALF, int KIND, int I> DI void gath_ld(const Ctx& c, const Ad& a, Gath& g) {
    g.g[I] = ld128(c.rws, a.l16 + I * 1024, a.sto + HALF * ST_HG + KIND * 4096);
}
DI bool gath_bad(const Ctx& c, const Gath& g) {
    u32 d = 0;
#pragma unroll
    for (int i = 0; i < 4; ++i) d = tag_bad(g.g[i], c.tagA, c.tagB, d);
    return ANY(d != 0u);
}
template <int HALF, int KIND> DI void gath_wait(const Ctx& c, const Ad& a, Gath& g) {
    if (gath_bad(c, g)) {
        unsigned spins = 0;
        NOUNROLL do { MEMBAR(); sfor<0, 4>([&](auto IC) { gath_ld<HALF, KIND, decltype(IC)::value>(c, a, g); }); } while (gath_bad(c, g) && !spin_fail(c, spins, 2u));
    }
}
DI void gath_ln(const Gath& g, float& m, float& r) {
    const float t1 = (__uint_as_float(g.g[0].x) + __uint_as_float(g.g[1].x)) + (__uint_as_float(g.g[2].x) + __uint_as_float(g.g[3].x));
    const float t2 = (__uint_as_float(g.g[0].z) + __uint_as_float(g.g[1].z)) + (__uint_as_float(g.g[2].z) + __uint_as_float(g.g[3].z));
    ln_scalars(hsum(t1), hsum(t2), m, r);
}
DI f32x4 epi_cs(const Ctx& c, const Ad& a, int E) { return *(LAS f32x4*)(c.L + a.ca + E * 32); }
template <int HALF, int MLP, int E>
DI void g1_epi(const Ctx& c, const Ad& a, G1Out& o, f32x4& cs, v4u& fr) {
    constexpr int t = E >> 2, r0 = 4 * (E & 3), g = E & 1;
    constexpr bool neg = (g ^ MLP) != 0;
    float h[4];
    LV(cs); LA(o.acc[t]);
#pragma unroll
    for (int e = 0; e < 4; ++e) h[e] = __builtin_fmaf(-o.mb, cs[e], o.acc[t][r0 + e]);
#pragma unroll
    for (int e = 0; e < 2; ++e) {
        const u32 p = __builtin_bit_cast(u32, __builtin_elementwise_max(__builtin_bit_cast(s16x2, pk(h[2 * e], h[2 * e + 1])), s16x2{0, 0}));
        fr[2 * g + e] = neg ? (p | SGN) : p;
    }
    if (g == 0) LV(fr);
}
template <int HALF, int K> DI void h_store(const Ctx& c, const Ad& a, const v4u& fr) { st128<K * 1024>(fr, c.rws, a.l16, a.hst + HALF * 114688); }

#define NW0_ 0
#define NT0_ 0
constexpr int NT0 = NT0_;
#define NW1_ 2
constexpr int NW1 = NW1_;
#define KP_ 4
constexpr int KP = KP_;
#define TLBW_ 1
constexpr int TLBW = TLBW_;
#define SLP0 0
#define HT1 0
#define HT2 13
#define SLP1 16
constexpr int NW0 = NW0_;
#define EPE 8
constexpr int HR = 10, GL = 14, GC = 20;
template <int MLP> DI bool h_bad(const v4u& a, const v4u& b) {
    const u32 lo = MLP == 0 ? (a.x | a.y | b.x | b.y) : ~(a.x & a.y & b.x & b.y);
    const u32 hi = MLP == 0 ? ~(a.z & a.w & b.z & b.w) : (a.z | a.w | b.z | b.w);
    return ((lo | hi) & SGN) != 0u;
}
DI u32 h_base(const Ctx& c, int half) { return OFF_H + ((c.grp * 2 + half) * 112 + 4 * c.w) * 1024; }
template <bool ASM> DI v2u gate_ld(const Ctx& c, const Ad& a, int half) {
    const u32 vo = min(a.l16, 96u) * 1024, so = a.hst - c.j * 16384 + half * 114688 + 4;
    if constexpr (ASM) { v2u r; asm volatile("buffer_load_dwordx2 %0, %1, %2, %3 offen sc1" : "=v"(r) : "v"(vo), "s"(c.rws), "s"(so) : "memory"); return r; }
    else return __builtin_amdgcn_raw_buffer_load_b64(c.rws, vo, so, AUX_SC1);
}
template <int MLP> DI bool gate_bad(v2u x) { return ((MLP == 0 ? (x.x | ~x.y) : (~x.x | x.y)) & SGN) != 0u; }
template <int MLP> DI void gate_wait(const Ctx& c, const Ad& a, int half, v2u x) {
    if (ANY(gate_bad<MLP>(x))) {
        unsigned spins = 0;
        NOUNROLL do { MEMBAR(); x = gate_ld<false>(c, a, half); } while (ANY(gate_bad<MLP>(x)) && !spin_fail(c, spins, 1u));
    }
}
template <int N> DI v4u h_next(const Ctx& c, Ad& a) {
    constexpr int f = N >= 28 ? N - 28 : N;
    if constexpr (N >= 32 && N < 38) a.so[k_mem(f)] += 114688u;
    v4u r = __builtin_amdgcn_raw_buffer_load_b128(c.rws, a.l16 + k_sub(f) * 1024, a.so[k_mem(f)], AUX_SC1);
    if constexpr (N >= 50) a.so[k_mem(f)] -= 114688u;
    return r;
}
template <int MLP, int F_> DI void h_check(const Ctx& c0, v4u& a, v4u& b) {
    if (__builtin_expect(ANY(h_bad<MLP>(a, b)), 0)) {
        const Ctx c = fresh(c0);
        constexpr int half = F_ >= 28, f = F_ - 28 * half;
        const u32 vo = lane_id() * 16 + k_sub(f) * 1024, s0 = h_base(c, half) + member(c, k_mem(f)) * 16384, s1 = h_base(c, half) + member(c, k_mem(f + 1)) * 16384;
        unsigned spins = 0;
        NOUNROLL do {
            MEMBAR();
            a = ld128(c.rws, vo, s0); b = ld128(c.rws, vo, s1);
        } while (ANY(h_bad<MLP>(a, b)) && !spin_fail(c, spins, 3u));
    }
}
template <int HALF, int P> DI void red_write(const Ctx& c, const Ad& a, const AccT& acc) {
    LAS char* dst = c.L + (a.l16 + (P ^ c.w) * 6144) + (L_X + (1 - HALF) * 28672 + (P - 1) * 2048);
    const f32x16& x = acc[P >> 1];
    constexpr int r0 = (P & 1) * 8;
    *(LAS f32x4*)dst = f32x4{x[r0], x[r0 + 1], x[r0 + 2], x[r0 + 3]};
    *(LAS f32x4*)(dst + 1024) = f32x4{x[r0 + 4], x[r0 + 5], x[r0 + 6], x[r0 + 7]};
}
template <int HALF> DI void red_read(const Ctx& c, const Ad& a, f32x4 (&t)[6]) {
    LAS char* src = c.L + (a.l16 + c.w * 6144) + (L_X + (1 - HALF) * 28672);
#pragma unroll
    for (int i = 0; i < 6; ++i) t[i] = *(LAS f32x4*)(src + i * 1024);
}
template <int K> DI void post_half(const f32x4& o, float r, v4u& xres) {
#pragma unroll
    for (int e = 0; e < 2; ++e) xres[2 * K + e] = pk(__builtin_fmaf(o[2 * e], r, bflo(xres[2 * K + e])), __builtin_fmaf(o[2 * e + 1], r, bfhi(xres[2 * K + e])));
}
template <int HALF> DI void out_store(const Ctx& c, const Ad& a, const v4u& x, __amdgpu_buffer_rsrc_t rout) {
    auto r0 = __builtin_amdgcn_permlane32_swap(x.x, x.z, false, false);
    auto r1 = __builtin_amdgcn_permlane32_swap(x.y, x.w, false, false);
    const v4u v = {r0[0], r1[0], r0[1], r1[1]};
    const u32 vo = tok32(a) * (D / 8) + (a.l16 >> 9) * 16, so = (u32)((c.grp * 64 + HALF * 32) * D + 64 * c.j + 16 * c.w) * 2;
    asm volatile("buffer_store_dwordx4 %0, %1, %2, %3 offen\n\ts_nop 1" :: "v"(v), "v"(vo), "s"(rout), "s"(so) : "memory");
}
template <int HALF, int MLP> DI v4u xres_ld(const Ctx& c, const Ad& a) {
    if constexpr (MLP == 0) return trd2(c.L + (xtr_of(a.l16 >> 4) + (4 * c.j + c.w) * 1024) + (L_X + HALF * 28672), 512);
    else return *(LAS v4u*)(c.L + (a.l16 + (4 * c.j + c.w) * 1024) + (L_X + HALF * 28672));
}
template <int HALF> DI v4u y_ld(const Ctx& c, const Ad& a) { return trd2(c.L + (xtr_of(a.l16 >> 4) + c.w * 1024) + (L_X + HALF * 28672 + 24576), 512); }
DI void x3_make(v4u& x, const v4u& yf, float m, float r) {
    const float n = -m * r;
#pragma unroll
    for (int e = 0; e < 4; ++e) {
        const float p = bflo(x[e]), q = bfhi(x[e]);
        x[e] = pk(__builtin_fmaf(__builtin_fmaf(p, r, n), bflo(yf[e]), p), __builtin_fmaf(__builtin_fmaf(q, r, n), bfhi(yf[e]), q));
    }
}
template <int HALF> DI void x3_store(const Ctx& c, const Ad& a, const v4u& x) { st128(x, c.rws, a.l16, a.x3o + HALF * 28672); }
template <int HALF, int I> DI v4u x3_ld1(const Ctx& c, const Ad& a) {
    return ld128(c.rws, a.l16 + (I & 3) * 1024, a.x3o + (6 * c.w - 4 * c.j + (I & 4)) * 1024 + HALF * 28672);
}
DI u32 max3u(u32 a, u32 b, u32 c) { u32 r; asm("v_max3_u32 %0, %1, %2, %3" : "=v"(r) : "v"(a), "v"(b), "v"(c)); return r; }
DI bool x3_bad(const v4u (&t)[7]) {
    u32 mx = 0;
#pragma unroll
    for (int i = 0; i < 7; ++i) mx = max3u(max3u(mx, t[i].x, t[i].y), t[i].z, t[i].w);
    return ANY(mx == ~0u);
}
template <bool EQ> DI void gate_spin(const Ctx& c, u32 vo, u32 so, u32 word, u32 code) {
    unsigned spins = 0;
    NOUNROLL for (;;) {
        MEMBAR();
        const u32 x = __builtin_amdgcn_raw_buffer_load_b32(c.rws, vo, so, AUX_SC1);
        if (!ANY(EQ ? x == word : x != word)) break;
        if (spin_fail(c, spins, code)) break;
    }
}
template <int HALF> DI void x3_wait(const Ctx& c, const Ad& a, v4u (&t)[7]) {
    if (x3_bad(t)) {
        unsigned spins = 0;
        NOUNROLL do { MEMBAR(); sfor<0, 7>([&](auto IC) { t[decltype(IC)::value] = x3_ld1<HALF, decltype(IC)::value>(c, a); }); } while (x3_bad(t) && !spin_fail(c, spins, 4u));
    }
}
template <int HALF, int I> DI void x3_put1(const Ctx& c, const Ad& a, const v4u& t) { *(LAS v4u*)(c.L + (a.l16 + c.w * 7168) + (L_X + HALF * 28672 + I * 1024)) = t; }

template <int MLP>
DI void g2_pair(const Ctx& c, Ad& a, const W2T& W2, v4u (&hb)[56], G1Out& gb, f32x4 (&csq)[3], float r0, v4u (&xres)[2], __amdgpu_buffer_rsrc_t out) {
    const u32 wl0 = wl2_of(c, a.l16 >> 4), wl1 = wl0 ^ 64u;
    AccT acc[2];
    v2u gate = {0, 0};
    v4u wl[2], fr, yf;
    f32x4 o[2], u[2];
    float s1, s2;
    Gath gs;
    const float ra_ = MLP ? -r0 : r0, rb_ = MLP ? -gb.r : gb.r;
    auto wres = [&](int slot, int k) { return trd2(c.L + (k ? wl1 : wl0) + slot * 2048, 1024); };
    auto fetch = [&](auto NC) {
        constexpr int N = decltype(NC)::value;
        if constexpr (N < 56) {
            if constexpr (N >= 28 && N < 32) hb[N] = *(LAS v4u*)(c.L + (a.l16 + c.w * 4096) + (L_X + (N - 28) * 1024));
            else hb[N] = h_next<N>(c, a);
        }
    };
    auto rd = [&](int i) { return *(LAS f32x4*)(c.L + (a.l16 + c.w * 6144) + (L_X + 28672 + i * 1024)); };
    auto own = [&](int k) { return f32x4{acc[0][0][4 * k], acc[0][0][4 * k + 1], acc[0][0][4 * k + 2], acc[0][0][4 * k + 3]}; };
    wl[0] = wres(8, 0); wl[1] = wres(8, 1);
    SB();
    sfor<0, 28>([&](auto PC) {
        constexpr int F_ = 2 * decltype(PC)::value, H = F_ >= 28, f = F_ - 28 * H;
        constexpr bool res = f < 24;
        constexpr int r0_ = f < 24 ? f >> 1 : f - 12;
        if constexpr (F_ == 24 || F_ == 30 || (MLP == 0 && F_ == 36)) { bar(); SB(); stat_pub_if<MLP == 0 && F_ == 36>(c, a); }
        if constexpr (F_ % 4 == 0) STAMP2((MLP ? PS1 : PS0) + 15 + F_ / 4);
        asm volatile("" : "+v"(a.l16), "+v"(a.ca));
        acc[H][0] = mfma32(W2[r0_][0], hb[F_], f == 0 ? zero16() : acc[H][0]);
        acc[H][1] = mfma32(W2[r0_][1], hb[F_], f == 0 ? zero16() : acc[H][1]);
        fetch(std::integral_constant<int, F_ + (F_ < 8 ? HR - 2 : HR)>{});
        fetch(std::integral_constant<int, F_ + 1 + (F_ < 8 ? HR - 2 : HR)>{});
        if constexpr (F_ == 6) { fetch(std::integral_constant<int, F_ + HR>{}); fetch(std::integral_constant<int, F_ + HR + 1>{}); }
        if constexpr (F_ < 6) csq[(F_ + 2) % 3] = epi_cs(c, a, F_ + 2);
        if constexpr (F_ < 8) g1_epi<1, MLP, F_>(c, a, gb, csq[F_ % 3], fr);
        if constexpr (F_ < 6) csq[F_ % 3] = epi_cs(c, a, F_ + 3);
        if constexpr (F_ < 8) g1_epi<1, MLP, F_ + 1>(c, a, gb, csq[(F_ + 1) % 3], fr);
        if constexpr (F_ == 32) { LA(acc[0][0]); LV(o[0]); LV(u[0]); LV(u[1]); o[0] = (o[0] + own(0)) + (u[0] + u[1]); post_half<0>(o[0], ra_, xres[0]); LV(xres[0]); }
        if constexpr (F_ == 34) { LA(acc[0][0]); LV(o[0]); LV(u[0]); LV(u[1]); o[0] = (o[0] + own(1)) + (u[0] + u[1]); post_half<1>(o[0], ra_, xres[0]); LV(xres[0]); }
        acc[H][0] = mfma32(res ? wl[0] : W2[r0_ + 1][0], hb[F_ + 1], acc[H][0]);
        acc[H][1] = mfma32((res || f == 26) ? wl[1] : W2[r0_ + 1][1], hb[F_ + 1], acc[H][1]);
        if constexpr (f < 22) { wl[0] = wres((9 + (f >> 1)) % 12, 0); wl[1] = wres((9 + (f >> 1)) % 12, 1); }
        if constexpr (f == 24) wl[1] = *(LAS v4u*)(c.L + (a.l16 + c.w * 1024) + L_JK);
        if constexpr (F_ == 26) { wl[0] = wres(8, 0); wl[1] = wres(8, 1); }
        if constexpr (F_ == 22) xres[1] = xres_ld<1, MLP>(c, a);
        if constexpr (F_ == 30) { u[0] = rd(0); u[1] = rd(2); o[0] = rd(4); }
        if constexpr (F_ == 32) { u[0] = rd(1); u[1] = rd(3); o[0] = rd(5); }
        if constexpr (MLP == 0 && F_ == 54) yf = y_ld<0>(c, a);
        if constexpr (F_ == GL) gate = gate_ld<false>(c, a, 1);
        if constexpr (F_ < 8) { *(LAS v4u*)(c.L + (a.l16 + c.w * 4096) + (L_X + (F_ / 2) * 1024)) = fr; h_store<1, F_ / 2>(c, a, fr); }
        if constexpr (MLP == 0 && (F_ == 24 || F_ == 26))
            dma16<true>(c.lbase + L_X + (F_ - 24) / 2 * 28672 + 24576 + c.w * 1024, a.l16, c.rws, OFF_YT + (c.grp * 2 + (F_ - 24) / 2) * 28672 + c.j * 4096 + c.w * 1024);
        if constexpr (F_ == 28) { red_write<0, 1>(c, a, acc[0]); red_write<0, 2>(c, a, acc[0]); red_write<0, 3>(c, a, acc[0]); }
        if constexpr (MLP == 0 && F_ == 34) { frag_sums(xres[0], s1, s2); part_put(c, a, L_S2, s1, s2); }
        if constexpr (MLP == 1 && F_ == 36) out_store<0>(c, a, xres[0], out);
        ilv<4, 5>();
        if constexpr (F_ + 2 < 56 && f + 2 != 28 && f + 2 >= 4) h_check<MLP, F_ + 2>(c, hb[F_ + 2], hb[F_ + 3]);
        if constexpr (F_ == GC) gate_wait<MLP>(c, a, 1, gate);
        SB();
    });
    static_assert(GC + 2 + HR <= 32 && (GC & 1) == 0 && (HR & 1) == 0, "half B's first foreign fragment is requested behind the gate");
    STAMP2((MLP ? PS1 : PS0) + 29);
    if constexpr (MLP == 0) STAMP4(0);
    red_write<1, 1>(c, a, acc[1]); red_write<1, 2>(c, a, acc[1]); red_write<1, 3>(c, a, acc[1]);
    if constexpr (MLP == 0) sfor<0, 4>([&](auto IC) { gath_ld<0, 0, decltype(IC)::value>(c, a, gs); });
    bar();
    if constexpr (MLP == 0) STAMP4(1);
    f32x4 t[6];
    red_read<1>(c, a, t);
    o[0] = (f32x4{acc[1][0][0], acc[1][0][1], acc[1][0][2], acc[1][0][3]} + t[0]) + (t[2] + t[4]);
    o[1] = (f32x4{acc[1][0][4], acc[1][0][5], acc[1][0][6], acc[1][0][7]} + t[1]) + (t[3] + t[5]);
    post_half<0>(o[0], rb_, xres[1]); post_half<1>(o[1], rb_, xres[1]);
    if constexpr (MLP == 0) {
        frag_sums(xres[1], s1, s2); part_put(c, a, L_S2, s1, s2);
        STAMP4(2);
        bar();
        stat_pub<1, 0>(c, a, L_S2, 1);
        STAMP4(3);
        gath_wait<0, 0>(c, a, gs);
        gath_ln(gs, s1, s2);
        x3_make(xres[0], yf, s1, s2);
        x3_store<0>(c, a, xres[0]);
        STAMP4(4);
        yf = y_ld<1>(c, a);
        __builtin_amdgcn_s_sleep(SLP1);
        v4u x[7];
        unsigned spins = 0;
        NOUNROLL do {
            MEMBAR();
            sfor<0, 4>([&](auto IC) { gath_ld<1, 0, decltype(IC)::value>(c, a, gs); });
            sfor<0, 7>([&](auto IC) { x[decltype(IC)::value] = x3_ld1<0, decltype(IC)::value>(c, a); });
        } while ((gath_bad(c, gs) || x3_bad(x)) && !spin_fail(c, spins, 4u));
        STAMP4(5);
        gath_ln(gs, s1, s2);
        x3_make(xres[1], yf, s1, s2);
        x3_store<1>(c, a, xres[1]);
        sfor<0, 7>([&](auto IC) { x3_put1<0, decltype(IC)::value>(c, a, x[decltype(IC)::value]); });
    } else out_store<1>(c, a, xres[1], out);
}

__global__ void __launch_bounds__(256, 1) fused_kernel(const u16* __restrict__ x0, const u16* __restrict__ kin, const u16* __restrict__ vin,
                                                       const u16* __restrict__ sk, const u16* __restrict__ hk, const u16* __restrict__ Dv,
                                                       const u16* __restrict__ w1, const u16* __restrict__ w2,
                                                       unsigned char* __restrict__ ws, u16* __restrict__ out) {
    extern __shared__ __attribute__((aligned(16))) char smem[];
    Ctx c;
    c.L = (LAS char*)smem;
    c.lbase = (u32)(uintptr_t)c.L;
    const int b = blockIdx.x, tid = threadIdx.x;
    c.w = __builtin_amdgcn_readfirstlane(tid >> 6);
    c.rws = mkrs(ws, WS_BYTES);
    c.tmo = (GAS u32*)(ws + 256);
    STAMP(0);
    STAMP3(0);

    if (b < 32) {
        const v2u ep = __builtin_amdgcn_raw_buffer_load_b64(c.rws, 0, OFF_EP, AUX_SC1);
        v4u xr[2][7];
#pragma unroll
        for (int h = 0; h < 2; ++h)
#pragma unroll
            for (int i = 0; i < 7; ++i) xr[h][i] = *(const v4u*)(x0 + (b * 64 + 2 * (tid >> 3) + h) * D + (i * 8 + (tid & 7)) * 8);
        MEMBAR();
        {
            unsigned long long pc; u32 back, len;
            asm volatile("s_getpc_b64 %0\n.Lfk_here:\n\ts_mov_b32 %1, .Lfk_here-.Lfk_begin\n\ts_mov_b32 %2, .Lfk_end-.Lfk_begin" : "=s"(pc), "=s"(back), "=s"(len));
            if (c.w < 2) touch(c.lbase + L_JK, (tid & 63) * 128, mkrs((const void*)(pc - back), len), ((b >> 3) * 2 + c.w) * 8192);
        }
        u32 res[2];
#pragma unroll
        for (int h = 0; h < 2; ++h) {
            float s1 = 0.f, s2 = 0.f;
#pragma unroll
            for (int i = 0; i < 7; ++i)
#pragma unroll
                for (int e = 0; e < 4; ++e) { s1 = dot2(xr[h][i][e], 0x3f803f80u, s1); s2 = dot2(xr[h][i][e], xr[h][i][e], s2); }
#pragma unroll
            for (int o = 1; o < 8; o <<= 1) { s1 += __shfl_xor(s1, o); s2 += __shfl_xor(s2, o); }
            const float m = s1 * (1.0f / D);
            res[h] = pk(m, ln_sd(fmaxf(__builtin_fmaf(-m, m, s2 * (1.0f / D)), 0.f)));
        }
        if ((tid & 7) == 0) st128(v4u{res[0], ep.x + 1u, res[1], ep.y + 1u}, c.rws, (u32)(b * 32 + (tid >> 3)) * 16, OFF_ST0);
        const int rg = tid >> 3, c8 = min(tid & 7, 6);
        v4u wr[14];
#pragma unroll
        for (int i = 0; i < 14; ++i) wr[i] = *(const v4u*)(w1 + (rg * 14 + i) * F + b * 56 + c8 * 8);
        float cs[8] = {0, 0, 0, 0, 0, 0, 0, 0};
#pragma unroll
        for (int i = 0; i < 14; ++i)
#pragma unroll
            for (int e = 0; e < 4; ++e) { cs[2 * e] += bflo(wr[i][e]); cs[2 * e + 1] += bfhi(wr[i][e]); }
        LAS float* sf = (LAS float*)c.L;
#pragma unroll
        for (int e = 0; e < 8; ++e) sf[rg * 65 + (tid & 7) * 8 + e] = cs[e];
        bar();
        if (tid < 56) {
            float t = 0.f;
#pragma unroll
            for (int i = 0; i < 32; ++i) t += sf[i * 65 + tid];
            const float t2 = __shfl_down(t, 1);
            if ((tid & 1) == 0) st128(v4u{__float_as_uint(t), ep.x + 1u, __float_as_uint(t2), ep.y + 1u}, c.rws, (u32)(b * 28 + (tid >> 1)) * 16, OFF_CSG);
        }
        WAIT_VM(0);
        bar();
        {
            const __amdgpu_buffer_rsrc_t rw1 = mkrs(w1, D * F * 2), rw2 = mkrs(w2, D * F * 2);
#pragma unroll
            for (int i = 0; i < HT1; ++i) touch(c.lbase + L_JK, (tid & 63) * 128, rw1, (u32)(112 + i * 16 + (b >> 3) * 4 + c.w) * 8192);
#pragma unroll
            for (int i = 0; i < HT2; ++i) touch(c.lbase + L_JK, (tid & 63) * 128, rw2, (u32)(i * 16 + (b >> 3) * 4 + c.w) * 8192);
        }
        WAIT_VM(0);
        return;
    }
    asm volatile(".Lfk_begin:");
    const int wb = b - 32;
    { const int xcd = wb & 7, idx = wb >> 3; c.grp = xcd * 4 + idx / 7; c.j = idx % 7; }
    c.gi = c.grp & 3;
    const int w = c.w;
    c.rw1 = mkrs(w1, D * F * 2); c.rw2 = mkrs(w2, D * F * 2);
    c.kb0 = (w + c.gi + 2) & 3;
    const int cp = (wb & 7) * 28 + (wb >> 3);
    W1T W1; W2T W2;

    u32 dval;
    {
        const int g = wb % 7, rb = wb / 7, lane = tid & 63;
        const int rg = (lane & 7) * 2 + ((lane >> 4) & 1), cl = ((lane >> 3) & 1) + 2 * (lane >> 5), cq = 4 * w + cl;
        const __amdgpu_buffer_rsrc_t rk = mkrs(kin, TSZ), rv = mkrs(vin, TSZ), rx = mkrs(x0, TSZ), rh = mkrs(hk, TSZ), rs_ = mkrs(sk, D * 8), rd = mkrs(Dv, D * 2);
        STAMP3(1);
        const u32 tb = (u32)((rb * 64) * 896 + g * 128);
        const u32 tv0 = (u32)((lane >> 3) * 896) + (((lane & 7) ^ ((lane >> 5) + 2 * w)) & 7) * 16;
        dma16<false>(c.lbase + T_X + w * 1024, tv0, rx, tb + w * 7168);
        dma16<false>(c.lbase + T_X + (w + 4) * 1024, tv0, rx, tb + (w + 4) * 7168);
        dma16<false>(c.lbase + T_K + w * 1024, tv0 + tb + (w - 1) * 7168, rk, 0u);
        dma16<false>(c.lbase + T_K + (w + 4) * 1024, tv0, rk, tb + (w + 3) * 7168);
        dma16<false>(c.lbase + T_V + w * 1024, tv0, rv, tb + w * 7168);
        dma16<false>(c.lbase + T_V + (w + 4) * 1024, tv0, rv, tb + (w + 4) * 7168);
        if (w == 0) dma16<false>(c.lbase + T_K + 8192, tv0, rk, tb + 7 * 7168);
        else dma16<false>(c.lbase + T_S + (w - 1) * 1024, (u32)lane * 16, rs_, g * 512);
        v4u ep4 = ld128_asm<true>(c.rws, 0u, OFF_EP);
        v4u he[2];
        he[0] = ld128_asm<false>(rh, tid * 16, cp * 8192); he[1] = ld128_asm<false>(rh, tid * 16, cp * 8192 + 4096);
        v4u dv4 = ld128_asm<false>(rd, 0u, cp * 4);
        {
            const WSt wt = w_st<0>(c, lane);
            sfor<0, NW0>([&](auto NC) { w_dma<decltype(NC)::value>(c, wt, w_soff<decltype(NC)::value>(c)); });
        }
        if constexpr (TLBW) {
            if (w == 0) touch(c.lbase + L_JK + 3072, (u32)lane * 524288u, c.rws, 4096u);
            else touch(c.lbase + L_JK + 3072, (u32)lane * 262144u, w == 1 ? c.rw1 : w == 2 ? c.rw2 : mkrs(out, TSZ), 0u);
        }
        if constexpr (NT0) touch(c.lbase + L_JK + 3072, (u32)lane * 128, c.rw1, (u32)((wb >> 3) * 4 + w) * 8192);
        STAMP3(2);
        asm volatile("s_waitcnt vmcnt(%1)" : "+v"(ep4) : "i"(3 + NT0 + TLBW + 2 * NW0) : "memory");
        c.tagA = __builtin_amdgcn_readfirstlane(ep4.x) + 1u; c.tagB = __builtin_amdgcn_readfirstlane(ep4.y) + 1u;
        bar();
        STAMP(1); STAMP3(3);
        const u32 a1 = (u32)(rg * 512 + (((cq >> 1) ^ rg) & 7) * 16 + (cq & 1) * 8);
        const u32 ak1 = (u32)(rg * 512 + 1024 + (((cq >> 1) ^ (rg + 2)) & 7) * 16 + (cq & 1) * 8);
        const u32 ak0 = (u32)(rg * 512 + 512 + (((cq >> 1) ^ (rg + 1)) & 7) * 16 + (cq & 1) * 8);
        v2u kr[7], vr[4], xr[4];
#pragma unroll
        for (int i = 0; i < 4; ++i) xr[i] = *(LAS v2u*)(c.L + T_X + a1 + i * 128);
        const v4u w01 = *(LAS v4u*)(c.L + T_S + cq * 32), w23 = *(LAS v4u*)(c.L + T_S + cq * 32 + 16);
#pragma unroll
        for (int i = 0; i < 3; ++i) kr[i] = *(LAS v2u*)(c.L + T_K + ak0 + (i + 1) * 128);
#pragma unroll
        for (int i = 0; i < 4; ++i) { kr[3 + i] = *(LAS v2u*)(c.L + T_K + ak1 + i * 128); vr[i] = *(LAS v2u*)(c.L + T_V + a1 + i * 128); }
#pragma unroll
        for (int h = 0; h < 2; ++h) {
            auto r0 = __builtin_amdgcn_permlane16_swap(__builtin_amdgcn_perm(xr[1][h], xr[0][h], 0x05040100u), __builtin_amdgcn_perm(xr[1][h], xr[0][h], 0x07060302u), false, false);
            auto r1 = __builtin_amdgcn_permlane16_swap(__builtin_amdgcn_perm(xr[3][h], xr[2][h], 0x05040100u), __builtin_amdgcn_perm(xr[3][h], xr[2][h], 0x07060302u), false, false);
            st128(v4u{r0[0], r1[0], r0[1], r1[1]}, c.rws, (u32)((cl * 4 + ((lane >> 4) & 1)) * 4096 + (lane & 7) * 16), OFF_X0G + (g * 64 + 16 * w + 2 * h) * 4096 + rb * 128);
        }
        STAMP3(27);
        v4u kg[4];
#pragma unroll
        for (int e = 0; e < 4; ++e) {
            const v4u& wq = e < 2 ? w01 : w23;
            const float w0 = bflo(wq[(e & 1) * 2]), w1_ = bfhi(wq[(e & 1) * 2]), w2_ = bflo(wq[(e & 1) * 2 + 1]), w3 = bfhi(wq[(e & 1) * 2 + 1]);
            float kk[7], o[4];
#pragma unroll
            for (int i = 0; i < 7; ++i) kk[i] = (e & 1) ? bfhi(kr[i][e >> 1]) : bflo(kr[i][e >> 1]);
#pragma unroll
            for (int i = 0; i < 4; i += 2) {
                u32 a_ = pk(kk[i] * w3, kk[i + 1] * w3), b_ = pk(kk[i + 1] * w2_, kk[i + 2] * w2_);
                a_ = pk(bflo(a_) + bflo(b_), bfhi(a_) + bfhi(b_)); b_ = pk(kk[i + 2] * w1_, kk[i + 3] * w1_);
                a_ = pk(bflo(a_) + bflo(b_), bfhi(a_) + bfhi(b_)); b_ = pk(kk[i + 3] * w0, kk[i + 4] * w0);
                a_ = pk(bflo(a_) + bflo(b_), bfhi(a_) + bfhi(b_));
                o[i] = bflo(a_) * ((e & 1) ? bfhi(vr[i][e >> 1]) : bflo(vr[i][e >> 1]));
                o[i + 1] = bfhi(a_) * ((e & 1) ? bfhi(vr[i + 1][e >> 1]) : bflo(vr[i + 1][e >> 1]));
            }
            kg[e] = v4u{pk(o[3], o[2]), c.tagA, pk(o[1], o[0]), c.tagB};
        }
        STAMP3(28);
        asm volatile("s_waitcnt vmcnt(0)\n\ts_barrier" : "+v"(he[0]), "+v"(he[1]), "+v"(dv4), "+v"(kg[0]), "+v"(kg[1]), "+v"(kg[2]), "+v"(kg[3]) :: "memory");
        STAMP3(29);
#pragma unroll
        for (int e = 0; e < 4; ++e) st128(kg[e], c.rws, (u32)(cl * 4 * 8192 + (15 - rg) * 16), OFF_KVG + ((g * 64 + 16 * w + e) * 512 + 496 - 16 * rb) * 16);
        {
            const WSt wt = w_st<0>(c, lane);
            sfor<NW0, NW1>([&](auto NC) { w_dma<decltype(NC)::value>(c, wt, w_soff<decltype(NC)::value>(c)); });
        }
        STAMP(2); STAMP3(4);
        dval = (w >> 1) ? dv4.x >> 16 : dv4.x & 0xffffu;
#pragma unroll
        for (int cc = 0; cc < 2; ++cc) {
            LAS char* cb = c.L + CV_B + cc * CV_CH;
            *(LAS v4u*)(cb + CV_HE + 32 + tid * 16) = he[cc];
            if (tid < 2) { *(LAS v4u*)(cb + CV_HE + tid * 16) = v4u{0, 0, 0, 0}; *(LAS v4u*)(cb + CV_HO + tid * 16) = v4u{0, 0, 0, 0}; }
            if (tid < 32) *(LAS v4u*)(cb + CV_KV + 4096 + tid * 16) = v4u{0, 0, 0, 0};
        }
        bar();
#pragma unroll
        for (int cc = 0; cc < 2; ++cc) {
            const v4u e = he[cc];
            const u32 pv = *(LAS u32*)(c.L + CV_B + cc * CV_CH + CV_HE + 28 + tid * 16);
            *(LAS v4u*)(c.L + CV_B + cc * CV_CH + CV_HO + 32 + tid * 16) = v4u{__builtin_amdgcn_alignbit(e.x, pv, 16), __builtin_amdgcn_alignbit(e.y, e.x, 16),
                                                                            __builtin_amdgcn_alignbit(e.z, e.y, 16), __builtin_amdgcn_alignbit(e.w, e.z, 16)};
        }
        bar();
        STAMP3(5);
        SB();
    }
    {
        const int lane = lane_id(), i16 = lane & 15, q = lane >> 4, cc = w >> 1, p = w & 1, ch = 2 * cp + cc;
        LAS char* cb = c.L + CV_B + cc * CV_CH;
        const int o0 = i16 + 1 + 32 * p + 8 * q;
        LAS char* ab = cb + ((o0 & 1) ? CV_HO + 4 : CV_HE) + (o0 >> 1) * 4;
        v4u A[32];
        v4u AD;
        {
            const int jj = p ? -1 : 15 - i16 - 8 * q;
            const u32 dv = dval << ((jj & 1) * 16);
#pragma unroll
            for (int d = 0; d < 4; ++d) AD[d] = (jj >= 0 && (jj >> 1) == d) ? dv : 0u;
        }
        const Ctx cs_ = fresh(c);
        const u32 wl = wl1_of(cs_, lane);
        const WSt wt = w_st<0>(cs_, lane);
        v4u sg[4], g[4];
        SB();
        sfor<0, 8>([&](auto NC) {
            constexpr int n = decltype(NC)::value;
#pragma unroll
            for (int t = n * 4; t < n * 4 + 4; ++t) { LAS u32* pp = (LAS u32*)(ab + t * 128); A[t] = v4u{pp[0], pp[1], pp[2], pp[3]}; }
            SB();
        });
        STAMP(3); STAMP3(6);
        {
            __builtin_amdgcn_s_sleep(SLP0);
            unsigned spins = 0;
            auto bad4 = [&](const v4u (&x)[4]) { u32 d = 0; for (int t = 0; t < 4; ++t) d = tag_bad(x[t], c.tagA, c.tagB, d); return ANY(d != 0u); };
            NOUNROLL do {
                MEMBAR();
#pragma unroll
                for (int i = 0; i < 4; ++i) g[i] = ld128(c.rws, tid * 16, OFF_KVG + cp * 16384 + i * 4096);
            } while (bad4(g) && !spin_fail(c, spins, 5u));
            STAMP3(30);
#pragma unroll
            for (int i = 0; i < 4; ++i) *(LAS v2u*)(c.L + CV_B + (i >> 1) * CV_CH + CV_KV + (tid + 256 * (i & 1)) * 8) = v2u{g[i].x, g[i].z};
        }
        bar();
        STAMP(4); STAMP3(7);
        LAS char* bb = cb + CV_KV + (2032 - 16 * i16 + 32 * p + 8 * q) * 2;
        f32x4 acc[8];
        float xs[4][4], qs[4][4];
        v4u B[32];
        v2u xg[4];
        B[0] = *(LAS v4u*)(bb + 3 * 128); B[1] = *(LAS v4u*)(bb + 2 * 128); B[2] = *(LAS v4u*)(bb + 128);
        const u32 s4 = (u32)(256 * p + 4 * i16 + q), xo = OFF_X0G + ch * 4096;
        SB();
        sfor<0, 32>([&](auto OC) {
            constexpr int oi = decltype(OC)::value, om = oi - 3;
            constexpr int nm = 8 - (om > 0 ? (om + 3) / 4 : 0) + ((om >= 0 && (om & 3) == 0) ? 1 : 0);
            if constexpr (oi % 4 == 0) STAMP3(8 + oi / 4);
            if constexpr (oi == 9) { bar(); SB(); }
            if constexpr (oi + 3 < 32) B[oi + 3] = *(LAS v4u*)(bb - (om + 3) * 128);
#pragma unroll
            for (int cb_ = 7; cb_ >= 0; --cb_)
                if (4 * cb_ - om >= 0) acc[cb_] = mfma16(A[4 * cb_ - om], B[oi], oi == 0 ? f32x4{0, 0, 0, 0} : acc[cb_]);
            if constexpr (om >= 0 && (om & 3) == 0) acc[om >> 2] = mfma16(AD, B[oi], acc[om >> 2]);
            if constexpr (oi < 2) { sg[2 * oi] = ld128_asm<true>(c.rws, tid * 16, OFF_ST0 + oi * 8192); sg[2 * oi + 1] = ld128_asm<true>(c.rws, tid * 16, OFF_ST0 + oi * 8192 + 4096); }
            if constexpr (oi == 2 || oi == 3) {
                asm volatile("buffer_load_dwordx2 %0, %2, %3, %4 offen offset:%5 sc1\n\tbuffer_load_dwordx2 %1, %2, %3, %4 offen offset:%6 sc1"
                             : "=&v"(xg[2 * oi - 4]), "=&v"(xg[2 * oi - 3]) : "v"(s4 * 8), "s"(c.rws), "s"(xo), "i"((oi - 2) * 1024), "i"((oi - 2) * 1024 + 512) : "memory");
            }
            if constexpr (oi < NW1) w_read1<oi>(cs_, wl, W1, W2);
            if constexpr (oi >= 4 && (oi & 1) == 0 && NW1 + (oi - 4) / 2 < 12) w_dma<NW1 + (oi - 4) / 2>(cs_, wt, w_soff<NW1 + (oi - 4) / 2>(cs_));
            if constexpr (oi == 7) {
                asm volatile("s_waitcnt vmcnt(4)" : "+v"(sg[0]), "+v"(sg[1]), "+v"(sg[2]), "+v"(sg[3]), "+v"(xg[0]), "+v"(xg[1]), "+v"(xg[2]), "+v"(xg[3]) :: "memory");
                auto bad = [&]() { u32 d = 0; for (int t = 0; t < 4; ++t) d = tag_bad(sg[t], c.tagA, c.tagB, d); return ANY(d != 0u); };
                if (bad()) {
                    unsigned spins = 0;
                    NOUNROLL do {
                        MEMBAR();
#pragma unroll
                        for (int t = 0; t < 4; ++t) sg[t] = ld128(c.rws, tid * 16, OFF_ST0 + t * 4096);
                    } while (bad() && !spin_fail(c, spins, 6u));
                }
#pragma unroll
                for (int t = 0; t < 4; ++t) *(LAS v2u*)(c.L + CV_ST + (tid + 256 * t) * 8) = v2u{sg[t].x, sg[t].z};
            }
            if constexpr (oi >= 9 && oi < 13) sg[oi - 9] = *(LAS v4u*)(c.L + CV_ST + s4 * 16 + (oi - 9) * 1024);
            if constexpr (oi >= 11 && oi < 19) {
                constexpr int t = (oi - 11) >> 1, hf = (oi - 11) & 1;
                LV(xg[t]); LV(sg[t]);
                const u32 xw = xg[t][hf];
                const u32 sa = sg[t][2 * hf], sb = sg[t][2 * hf + 1];
                xs[t][2 * hf] = bflo(xw); xs[t][2 * hf + 1] = bfhi(xw);
                qs[t][2 * hf] = (xs[t][2 * hf] - bflo(sa)) * __builtin_amdgcn_rcpf(bfhi(sa));
                qs[t][2 * hf + 1] = (xs[t][2 * hf + 1] - bflo(sb)) * __builtin_amdgcn_rcpf(bfhi(sb));
                asm volatile("" : "+v"(qs[t][2 * hf]), "+v"(qs[t][2 * hf + 1]));
            }
            if constexpr (oi == 23 || oi == 25) {
                const v4u ff = {~0u, ~0u, ~0u, ~0u};
                st128(ff, c.rws, lane * 16, OFF_X3 + (cs_.grp * 56 + (oi - 23) * 14 + 4 * cs_.j + cs_.w) * 1024);
            }
            ilv<nm, 2>();
            SB();
        });
        STAMP(5); STAMP3(16);
        LAS char* ex = c.L;
        f32x4 fin[4];
        if (p == 0) {
#pragma unroll
            for (int t = 0; t < 4; ++t) { *(LAS f32x4*)(ex + w * 4096 + t * 1024 + lane * 16) = acc[4 + t]; fin[t] = acc[t]; }
        } else {
#pragma unroll
            for (int t = 0; t < 4; ++t) { *(LAS f32x4*)(ex + w * 4096 + t * 1024 + lane * 16) = acc[t]; fin[t] = acc[4 + t]; }
        }
        bar();
        STAMP3(17);
        const u32 toff = (u32)((32 * p + 8 * (q & 1) + (i16 >> 1)) * 28672 + ch * 64 + (i16 & 1) * 32 + (q >> 1) * 16);
        v2u y[4], x1[4];
#pragma unroll
        for (int t = 0; t < 4; ++t) {
            fin[t] += *(LAS f32x4*)(ex + (w ^ 1) * 4096 + t * 1024 + lane * 16);
            y[t] = v2u{pk(fin[t][0], fin[t][1]), pk(fin[t][2], fin[t][3])};
            x1[t] = v2u{pk(__builtin_fmaf(qs[t][0], bflo(y[t].x), xs[t][0]), __builtin_fmaf(qs[t][1], bfhi(y[t].x), xs[t][1])),
                        pk(__builtin_fmaf(qs[t][2], bflo(y[t].y), xs[t][2]), __builtin_fmaf(qs[t][3], bfhi(y[t].y), xs[t][3]))};
        }
        auto merge = [&](const v2u& a, const v2u& b_) {
            auto r0 = __builtin_amdgcn_permlane16_swap(a.x, b_.x, false, false);
            auto r1 = __builtin_amdgcn_permlane16_swap(a.y, b_.y, false, false);
            return v4u{r0[0], r1[0], r0[1], r1[1]};
        };
        st128(merge(x1[0], x1[1]), c.rws, toff, OFF_X1);
        st128(merge(x1[2], x1[3]), c.rws, toff, OFF_X1 + 16 * 28672);
        st128(merge(y[0], y[1]), c.rws, toff, OFF_YT);
        st128(merge(y[2], y[3]), c.rws, toff, OFF_YT + 16 * 28672);
        STAMP(6); STAMP3(18);
        sfor<12, 12 + NW1>([&](auto NC) { w_dma<decltype(NC)::value>(cs_, wt, w_soff<decltype(NC)::value>(cs_)); });
        WAIT_VM(6 + 2 * NW1);
        sfor<NW1, 12>([&](auto NC) { w_read1<decltype(NC)::value>(cs_, wl, W1, W2); });
        WAIT_LGKM(0);
        sfor<12 + NW1, 20>([&](auto NC) { w_dma<decltype(NC)::value>(cs_, wt, w_soff<decltype(NC)::value>(cs_)); });
        STAMP3(19);
        WAIT_VM(16);
        bar();
        if (tid < 8) __builtin_amdgcn_raw_buffer_store_b64(v2u{c.tagA, c.tagB}, c.rws, (u32)(tid * 2048 + wb * 8), OFF_FL, AUX_SC1);
        MEMBAR();
        STAMP(7);
        sfor<20, 24>([&](auto NC) { w_dma<decltype(NC)::value>(cs_, wt, w_soff<decltype(NC)::value>(cs_)); });
        WAIT_VM(12);
        sfor<12, 18>([&](auto NC) { w_read1<decltype(NC)::value>(cs_, wl, W1, W2); });
        WAIT_LGKM(0);
        sfor<24, 28>([&](auto NC) { w_dma<decltype(NC)::value>(cs_, wt, w_soff<decltype(NC)::value>(cs_)); });
        STAMP3(21);
    }
    Ad ad;
    ad.l16 = lane_id() * 16;
    ad.hst = OFF_H + (c.grp * 224 + 16 * c.j + 4 * w) * 1024;
    ad.x3o = OFF_X3 + (c.grp * 56 + 4 * c.j + w) * 1024;
    ad.sto = OFF_ST + c.grp * 2 * ST_HG;
    asm volatile("" : "+s"(ad.hst), "+s"(ad.x3o), "+s"(ad.sto));
    const u32 swl = wl2_of(c, ad.l16 >> 4);
    const WSt w2t = w_st<28>(c, ad.l16 >> 4);
    u32 wso[7];
#pragma unroll
    for (int m = 0; m < 7; ++m) {
        const int mm = member(c, m);
        wso[m] = (256 * mm + 64 * w) * (D * 2) + 128 * c.j; ad.so[m] = ad.hst + (mm - c.j) * 16384;
        asm volatile("" : "+s"(wso[m]), "+s"(ad.so[m]));
    }
    auto w2_dma = [&](auto NC) { constexpr int n = decltype(NC)::value; w_dma<n>(c, w2t, wso[k_mem(w2_step_of(n))] + k_sub(w2_step_of(n)) * 16 * (D * 2)); };
    {
        v4u cg = ld128_asm<true>(c.rws, tok32(ad), OFF_CSG + c.j * 2048 + w * 512);
        sfor<28, 30>(w2_dma);
        WAIT_VM(12);
        w_read<18, 24>(c, W1, W2);
        STAMP(8); STAMP3(22);
        if (w == 0) {
            unsigned spins = 0;
            NOUNROLL for (;;) {
                MEMBAR();
                const v4u f0 = ld128(c.rws, ad.l16, OFF_FL + (wb & 7) * 2048);
                const v4u f1 = ld128(c.rws, 1024 + min(ad.l16, 752u), OFF_FL + (wb & 7) * 2048);
                const u32 d = xor_or(f0.x, c.tagA, xor_or(f0.z, c.tagA, xor_or(f1.x, c.tagA, xor_or(f1.z, c.tagA, tag_bad(f0, c.tagB, c.tagB, tag_bad(f1, c.tagB, c.tagB, 0u))))));
                if (!ANY(d != 0u)) break;
                if (spin_fail(c, spins, 7u)) break;
            }
            if (wb == 0 && ad.l16 == 0) __builtin_amdgcn_raw_buffer_store_b64(v2u{c.tagA, c.tagB}, c.rws, 0, OFF_EP, AUX_SC1);
        }
        bar();
        STAMP(9); STAMP3(23);
        {
            const u32 xb = c.lbase + L_X + w * 7168, xs_ = OFF_X1 + c.grp * 57344 + w * 7168;
            sfor<0, 14>([&](auto IC) {
                constexpr int i = decltype(IC)::value, o = (i / 7) * 28672 + (i % 7) * 1024;
                dma16<true>(xb + o, ad.l16, c.rws, xs_ + o);
            });
        }
        STAMP3(24);
        WAIT_VM(14);
        w_read<24, 28>(c, W1, W2);
        w_read1<28>(c, swl, W1, W2); w_read1<29>(c, swl, W1, W2);
        w_neg<28>(W2); w_neg<29>(W2);
        LV(cg);
        if (ANY(tag_bad(cg, c.tagA, c.tagB, 0u) != 0u)) {
            unsigned spins = 0;
            NOUNROLL do { MEMBAR(); cg = ld128(c.rws, tok32(ad), OFF_CSG + c.j * 2048 + w * 512); } while (ANY(tag_bad(cg, c.tagA, c.tagB, 0u) != 0u) && !spin_fail(c, spins, 8u));
        }
        *(LAS v2u*)(c.L + L_CS + w * 256 + (tok32(ad) >> 1)) = v2u{cg.x, cg.z};
        WAIT_VM(7);
        bar();
        STAMP(10); STAMP3(25);
    }
    STAMP3(26);
    pin_a<0, 25>(W1);
    v4u hb[56], xres[2];
    G1Out ga, gb;
    v2u gate;
    f32x4 csq[3];
    St1 st;
    ad.ca = L_CS + (w * 64 + (ad.l16 >> 9) * 4) * 4;
    using M0 = std::integral_constant<int, 0>; using M1 = std::integral_constant<int, 1>;
    {
        st.ad = xtr_of(ad.l16 >> 4) + w * 7168;
        auto stats = [&](auto MC, auto HC, auto FC, G1Out& o) {
            constexpr int H = decltype(HC)::value, f = decltype(FC)::value;
            constexpr bool TR = decltype(MC)::value == 0;
            if constexpr (f == 0) { st.s1 = 0.f; st.s2 = 0.f; }
            if constexpr (f >= 2 && f < 23) { constexpr int ph = (f - 2) % 3, k = (f - 2) / 3; st1_acc<k, ph * 3, ph == 2 ? 8 : ph * 3 + 3>(st); }
            if constexpr (f % 3 == 0 && f < 21) st1_read<TR, H, f / 3>(c, st);
            if constexpr (f == 23) part_put(c, ad, L_SP + H * 1024, st.s1, st.s2);
            if constexpr (f == 24) { SB(); asm volatile("s_waitcnt lgkmcnt(%0)\n\ts_barrier" :: "i"(TR ? 2 : 1) : "memory"); }
            if constexpr (f == 25) { const f32x2 t = part_get(c, ad, L_SP + H * 1024); ln_scalars(t[0], t[1], o.mb, o.r); asm volatile("" : "+v"(o.mb), "+v"(o.r)); }
        };
        auto stage = [&](auto GC_, auto XC) {
            constexpr int g = decltype(GC_)::value, extra = decltype(XC)::value;
            if constexpr (g >= 15 && g < 43 && (g & 1)) w_neg<30 + (g - 15) / 2>(W2);
            if constexpr (g == 41) *(LAS v4u*)(c.L + (ad.l16 + w * 1024) + L_JK) = W2[15][1];
            if constexpr (g >= 13 && g < 41 && (g & 1)) { WAIT_VM(12 + extra); w_read1<30 + (g - 13) / 2>(c, swl, W1, W2); }
            if constexpr (g < 52 && (g & 1) == 0) w2_dma(std::integral_constant<int, 30 + g / 2>{});
        };
        constexpr unsigned E2 = 0xAAAAAAAu;
        g1_main<true, 0, PS0, 0x9AAAAAAu>(c, ad, W1, ga.acc, [&](auto FC) {
            constexpr int f = decltype(FC)::value;
            stage(FC, M0{});
            stats(M0{}, M0{}, FC, ga);
            if constexpr (f >= 26) csq[f - 26] = epi_cs(c, ad, f - 26);
        });
        STAMP(11);
        auto side_b = [&](auto MC, auto FC) {
            constexpr int f = decltype(FC)::value, M = decltype(MC)::value;
            if constexpr (f < 6) csq[(f + 2) % 3] = epi_cs(c, ad, f + 2);
            if constexpr (f < 8) g1_epi<0, M, f>(c, ad, ga, csq[f % 3], hb[f >> 1]);
            if constexpr (f < 8 && (f & 1)) h_store<0, f / 2>(c, ad, hb[f >> 1]);
            if constexpr (f == 18) gate = gate_ld<true>(c, ad, 0);
            if constexpr (f == 19) xres[0] = xres_ld<0, M>(c, ad);
            if constexpr (f == 23) {
                SB();
                asm volatile("s_waitcnt vmcnt(%1)" : "+v"(gate) : "i"(M == 0 ? 4 : 0) : "memory");
                gate_wait<M>(c, ad, 0, gate);
            }
            if constexpr (f >= 24) hb[f - 20] = h_next<f - 20>(c, ad);
            if constexpr (f >= 26) csq[f - 26] = epi_cs(c, ad, f - 26);
        };
        const u32 ra = L_W + w * 24576 + 1024 + ad.l16;
        v4u rm;
        g1_main<true, 1, PS0 + 7, 0x9AAAAAAu>(c, ad, W1, gb.acc, [&](auto FC) {
            constexpr int f = decltype(FC)::value;
            stage(std::integral_constant<int, 28 + f>{}, std::integral_constant<int, (f < 8 ? f / 2 : 4)>{});
            if constexpr (f >= 16 && f < 28 && (f & 1) == 0) { LV(rm); *(LAS v4u*)(c.L + ra + ((8 + (f - 16) / 2) % 12) * 2048) = rm ^ SGN; }
            if constexpr (f >= 14 && f < 26 && (f & 1) == 0) { WAIT_VM(f < 24 ? 14 : 12); rm = *(LAS v4u*)(c.L + ra + ((8 + (f - 14) / 2) % 12) * 2048); }
            stats(M0{}, M1{}, FC, gb);
            side_b(M0{}, FC);
        });
        STAMP(12);
        {
            WAIT_VM(4);
            v4u t[6];
#pragma unroll
            for (int i = 0; i < 6; ++i) t[i] = *(LAS v4u*)(c.L + ra + ((14 + i) % 12) * 2048);
#pragma unroll
            for (int i = 0; i < 6; ++i) *(LAS v4u*)(c.L + ra + ((14 + i) % 12) * 2048) = t[i] ^ SGN;
            SB();
        }
        pin_a<0, 25>(W1);
        STAMP2(PS0 + 14);
        STAMP(13);
        g2_pair<0>(c, ad, W2, hb, gb, csq, ga.r, xres, c.rws);
        pin_a<0, 25>(W1);
        STAMP(14);
        v4u t[7];
        st.ad = ad.l16 + w * 7168;
        STAMP4(6);
        bar();
        STAMP4(7);
        STAMP(17);
        g1_main<false, 0, PS1, 0x9AAAAAAu>(c, ad, W1, ga.acc, [&](auto FC) {
            constexpr int f = decltype(FC)::value;
            stats(M1{}, M0{}, FC, ga);
            if constexpr (f >= 12 && f < 19) t[f - 12] = x3_ld1<1, f - 12>(c, ad);
            if constexpr (f == 24) x3_wait<1>(c, ad, t);
            if constexpr (f >= 25) { x3_put1<1, 2 * (f - 25)>(c, ad, t[2 * (f - 25)]); x3_put1<1, 2 * (f - 25) + 1>(c, ad, t[2 * (f - 25) + 1]); }
            if constexpr (f == 27) x3_put1<1, 6>(c, ad, t[6]);
            if constexpr (f >= 26) csq[f - 26] = epi_cs(c, ad, f - 26);
        });
        STAMP4(8);
        STAMP4(9);
        STAMP4(10);
        STAMP(18);
        bar();
        STAMP4(11);
        g1_main<false, 1, PS1 + 7, 0x9AAAAAAu>(c, ad, W1, gb.acc, [&](auto FC) {
            stats(M1{}, M1{}, FC, gb);
            side_b(M1{}, FC);
        });
        (void)E2;
        STAMP(19);
        STAMP2(PS1 + 14);
        STAMP(20);
        const __attribute__((address_space(4))) char* ka = (const __attribute__((address_space(4))) char*)__builtin_amdgcn_kernarg_segment_ptr();
        asm volatile("" : "+s"(ka));
        g2_pair<1>(c, ad, W2, hb, gb, csq, ga.r, xres, mkrs(*(void* const __attribute__((address_space(4)))*)(ka + 72), TSZ));
        STAMP(21);
    }
    asm volatile(".Lfk_end:");
}

extern "C" void kernel_launch(void* const* d_in, const int* in_sizes, int n_in,
                              void* d_out, int out_size, void* d_ws, size_t ws_size,
                              hipStream_t stream) {
    (void)hipFuncSetAttribute((const void*)fused_kernel, hipFuncAttributeMaxDynamicSharedMemorySize, L_TOT);
    fused_kernel<<<256, 256, L_TOT, stream>>>((const u16*)d_in[0], (const u16*)d_in[1], (const u16*)d_in[2], (const u16*)d_in[3], (const u16*)d_in[4],
                                              (const u16*)d_in[5], (const u16*)d_in[7], (const u16*)d_in[8], (unsigned char*)d_ws, (u16*)d_out);
}
```

```cpp
#include <hip/hip_runtime.h>
#include <stdint.h>
#include <type_traits>

typedef unsigned short u16;
typedef unsigned u32;
typedef u32 v4u __attribute__((ext_vector_type(4)));
typedef u32 v2u __attribute__((ext_vector_type(2)));
typedef float f32x2 __attribute__((ext_vector_type(2)));
typedef float f32x4 __attribute__((ext_vector_type(4)));
typedef float f32x16 __attribute__((ext_vector_type(16)));
typedef __bf16 bf16x8 __attribute__((ext_vector_type(8)));
typedef __bf16 bf16x2 __attribute__((ext_vector_type(2)));
typedef short s16x4 __attribute__((ext_vector_type(4)));
typedef short s16x2 __attribute__((ext_vector_type(2)));
typedef unsigned short u16x2 __attribute__((ext_vector_type(2)));
#define LAS __attribute__((address_space(3)))
#define GAS __attribute__((address_space(1)))
#define DI __device__ __forceinline__
#define RLX_AGENT __ATOMIC_RELAXED, __HIP_MEMORY_SCOPE_AGENT
#define SB() __builtin_amdgcn_sched_barrier(0)
#define NOUNROLL _Pragma("unroll 1")
#define MEMBAR() asm volatile("" ::: "memory")
template <class T> __device__ __forceinline__ void LV(T& x) { asm volatile("" : "+v"(x)); }
template <class T> __device__ __forceinline__ void LA(T& x) { asm volatile("" : "+a"(x)); }
#define ANY(x) (__builtin_amdgcn_ballot_w64(x) != 0ull)

constexpr int S = 2048, D = 448, F = 1792;
constexpr float EPS = 1e-5f;
constexpr u32 TSZ = S * D * 2;
constexpr u32 OFF_EP  = 0;
constexpr u32 OFF_FL  = 4096;
constexpr u32 OFF_ST0 = OFF_FL + 16384;
constexpr u32 OFF_CSG = OFF_ST0 + 16384;
constexpr u32 OFF_ST  = OFF_CSG + 16384;
constexpr u32 OFF_KVG = 1048576;
constexpr u32 OFF_X0G = OFF_KVG + 2 * TSZ;
constexpr u32 OFF_X1  = OFF_X0G + 2 * TSZ;
constexpr u32 OFF_YT  = OFF_X1 + TSZ;
constexpr u32 OFF_X3  = OFF_YT + TSZ;
constexpr u32 OFF_H   = OFF_X3 + TSZ;
constexpr u32 WS_BYTES = OFF_H + 64 * 112 * 1024;
static_assert(OFF_ST + 64 * 24 * 512 <= OFF_KVG, "ws map");
constexpr int RSRC_W3 = 0x00020000;
constexpr int AUX_SC1 = 16;
constexpr unsigned SPIN_CAP = 1u << 14;
constexpr u32 SGN = 0x80008000u;

template <int I, int N, class Fn> DI void sfor(Fn&& fn) { if constexpr (I < N) { fn(std::integral_constant<int, I>{}); sfor<I + 1, N>(fn); } }

DI float bflo(u32 x) { return __uint_as_float(x << 16); }
DI float bfhi(u32 x) { return __uint_as_float(x & 0xffff0000u); }
DI u32 pk(float a, float b) { f32x2 v = {a, b}; return __builtin_bit_cast(u32, __builtin_convertvector(v, bf16x2)); }
DI float rbf(float a) { return bflo(pk(a, a)); }
DI __amdgpu_buffer_rsrc_t mkrs(const void* p, u32 bytes) { return __builtin_amdgcn_make_buffer_rsrc((void*)p, 0, (int)bytes, RSRC_W3); }
DI f32x4 mfma16(v4u a, v4u b, f32x4 c) { return __builtin_amdgcn_mfma_f32_16x16x32_bf16(__builtin_bit_cast(bf16x8, a), __builtin_bit_cast(bf16x8, b), c, 0, 0, 0); }
DI f32x16 mfma32(v4u a, v4u b, f32x16 c) { return __builtin_amdgcn_mfma_f32_32x32x16_bf16(__builtin_bit_cast(bf16x8, a), __builtin_bit_cast(bf16x8, b), c, 0, 0, 0); }
DI f32x16 zero16() { f32x16 z; for (int i = 0; i < 16; ++i) z[i] = 0.f; return z; }
DI v2u trd(LAS char* p) { return __builtin_bit_cast(v2u, __builtin_amdgcn_ds_read_tr16_b64_v4i16((LAS s16x4*)p)); }
DI v4u trd2(LAS char* p, int second) { v2u a = trd(p), b = trd(p + second); return v4u{a.x, a.y, b.x, b.y}; }
DI float dot2(u32 a, u32 b, float c) { return __builtin_amdgcn_fdot2_f32_bf16(__builtin_bit_cast(bf16x2, a), __builtin_bit_cast(bf16x2, b), c, false); }
DI v4u ld128(__amdgpu_buffer_rsrc_t rs, u32 vo, u32 so) { return __builtin_amdgcn_raw_buffer_load_b128(rs, vo, so, AUX_SC1); }
template <bool SC1, int OFF = 0> DI v4u ld128_asm(__amdgpu_buffer_rsrc_t rs, u32 vo, u32 so) {
    v4u r;
    if constexpr (SC1) asm volatile("buffer_load_dwordx4 %0, %1, %2, %3 offen offset:%4 sc1" : "=v"(r) : "v"(vo), "s"(rs), "s"(so), "i"(OFF) : "memory");
    else asm volatile("buffer_load_dwordx4 %0, %1, %2, %3 offen offset:%4" : "=v"(r) : "v"(vo), "s"(rs), "s"(so), "i"(OFF) : "memory");
    return r;
}
template <int OFF = 0> DI void st128(v4u v, __amdgpu_buffer_rsrc_t rs, u32 vo, u32 so) {
    asm volatile("buffer_store_dwordx4 %0, %1, %2, %3 offen offset:%4 sc1\n\ts_nop 1" :: "v"(v), "v"(vo), "s"(rs), "s"(so), "i"(OFF) : "memory");
}
DI int lane_id() { int l; asm volatile("v_mbcnt_lo_u32_b32 %0, -1, 0\n\tv_mbcnt_hi_u32_b32 %0, -1, %0" : "=v"(l)); return l; }
DI float hsum(float a) {
    const u32 v = __float_as_uint(a);
    auto t = __builtin_amdgcn_permlane32_swap(v, v, false, false);
    return __uint_as_float(t[0]) + __uint_as_float(t[1]);
}
DI float ln_sd(float var) { return rbf(__builtin_amdgcn_sqrtf(rbf(rbf(var) + EPS))); }
DI void ln_scalars(float s1, float s2, float& m, float& r) {
    m = s1 * (1.0f / D);
    r = __builtin_amdgcn_rsqf(fmaxf(__builtin_fmaf(-m, m, s2 * (1.0f / D)), 0.f) + EPS);
}
DI u32 xor_or(u32 a, u32 b, u32 c) { return __builtin_amdgcn_bitop3_b32(a, b, c, 0xBE); }
DI u32 tag_bad(const v4u& g, u32 tA, u32 tB, u32 acc) { return xor_or(g.w, tB, xor_or(g.y, tA, acc)); }
template <bool SC1> DI void dma16(u32 ldsaddr, u32 voff, __amdgpu_buffer_rsrc_t rs, u32 soff) {
    if constexpr (SC1) asm volatile("s_mov_b32 m0, %0\n\ts_nop 0\n\tbuffer_load_dwordx4 %1, %2, %3 offen sc1 lds" :: "s"(ldsaddr), "v"(voff), "s"(rs), "s"(soff) : "memory", "m0");
    else asm volatile("s_mov_b32 m0, %0\n\ts_nop 0\n\tbuffer_load_dwordx4 %1, %2, %3 offen lds" :: "s"(ldsaddr), "v"(voff), "s"(rs), "s"(soff) : "memory", "m0");
}
template <int LI> DI void dma_pair(u32 ldsbase, u32 vo0, u32 vo1, __amdgpu_buffer_rsrc_t rs, u32 soff) {
    asm volatile("s_add_u32 m0, %0, %5\n\ts_nop 0\n\tbuffer_load_dwordx4 %1, %3, %4 offen lds\n\tbuffer_load_dwordx4 %2, %3, %4 offen offset:1024 lds"
                 :: "s"(ldsbase), "v"(vo0), "v"(vo1), "s"(rs), "s"(soff), "i"(LI) : "memory", "m0", "scc");
}
DI void touch(u32 junk, u32 voff, __amdgpu_buffer_rsrc_t rs, u32 soff) {
    asm volatile("s_mov_b32 m0, %0\n\ts_nop 0\n\tbuffer_load_dword %1, %2, %3 offen lds" :: "s"(junk), "v"(voff), "s"(rs), "s"(soff) : "memory", "m0");
}
#define WAIT_VM(n) asm volatile("s_waitcnt vmcnt(%0)" :: "i"(n) : "memory")
#define WAIT_LGKM(n) asm volatile("s_waitcnt lgkmcnt(%0)" :: "i"(n) : "memory")
DI void bar() { asm volatile("s_waitcnt lgkmcnt(0)\n\ts_barrier" ::: "memory"); }
DI int wrap7(int x) { return x >= 7 ? x - 7 : x; }
template <int N, int K> DI void ilv() { if constexpr (N > 0) { __builtin_amdgcn_sched_group_barrier(0x8, 1, 0); __builtin_amdgcn_sched_group_barrier(0x366, K, 0); ilv<N - 1, K>(); } }

constexpr int L_X = 0;
constexpr int L_W = 57344;
constexpr int L_M = L_W + 98304;
constexpr int L_SP = L_M;
constexpr int L_CS = L_M + 2048;
constexpr int L_S2 = L_M + 3072;
constexpr int L_JK = L_M + 4096;
constexpr int L_TOT = L_M + 8192;
constexpr int T_K = 0, T_V = 9216, T_X = 17408, T_S = L_JK;
constexpr int CV_B = 25600;
constexpr int CV_HE = 0, CV_HO = 4160, CV_KV = CV_HO + 4160, CV_CH = CV_KV + 4608;
constexpr int CV_ST = 16384;
static_assert(CV_B + 2 * CV_CH <= L_W, "lds map");

struct Ctx {
    LAS char* L;
    __amdgpu_buffer_rsrc_t rws, rw1, rw2;
    GAS u32* tmo;
    int w, j, grp, kb0, gi;
    u32 lbase, tagA, tagB;
};
DI Ctx fresh(const Ctx& c0) { Ctx c = c0; asm volatile("" : "+s"(c.w), "+s"(c.j), "+s"(c.grp), "+s"(c.kb0), "+s"(c.gi)); return c; }
DI bool spin_fail(const Ctx& c, unsigned& spins, u32 code) {
    if (++spins > SPIN_CAP) { __hip_atomic_store(c.tmo, code, RLX_AGENT); return true; }
    __builtin_amdgcn_s_sleep(1);
    return false;
}
DI u32 xtr_of(int lane) { return (u32)(((lane >> 5) * 4 + ((lane >> 2) & 3)) * 64 + ((lane >> 4) & 1) * 32 + (lane & 3) * 8); }
DI u32 wl1_of(const Ctx& c, int lane) {
    const int rr = (lane >> 5) * 4 + ((lane >> 2) & 3);
    return (u32)(L_W + c.w * 24576 + rr * 128 + ((rr >> 1) & 1) * 64 + ((lane >> 4) & 1) * 32 + (lane & 3) * 8);
}
DI u32 wl2_of(const Ctx& c, int lane) { return wl1_of(c, lane) ^ (u32)((c.w >> 1) * 64 + (c.w & 1) * 32); }
DI int member(const Ctx& c, int m) {
    if (m == 0) return c.j;
    int t = m - 1 + 2 * c.gi; t = t >= 6 ? t - 6 : t; t = t >= 6 ? t - 6 : t;
    return wrap7(c.j + 1 + t);
}
typedef v4u W1T[28][2];
typedef v4u W2T[16][2];
typedef f32x16 AccT[2];

DI constexpr int k_mem(int f) { return f < 4 ? 0 : 1 + (f - 4) % 6; }
DI constexpr int k_sub(int f) { return f < 4 ? f : (f - 4) / 6; }
DI constexpr int w2_step_of(int n) { return n < 40 ? 2 * (n - 28) : (n < 44 ? n - 16 : 2 * (n - 44) + 1); }
template <int N> DI u32 w_soff(const Ctx& c) {
    if constexpr (N < 28) return (N % 7 + 7 * ((N / 7 + c.kb0) & 3)) * 16 * (F * 2) + (256 * c.j + 64 * c.w) * 2;
    else return (256 * member(c, k_mem(w2_step_of(N))) + 64 * c.w + 16 * k_sub(w2_step_of(N))) * (D * 2) + 128 * c.j;
}
template <int N> DI u32 w_voff(int lane) { return (lane >> 3) * ((N < 28 ? F : D) * 2) + ((lane & 7) ^ (((lane >> 4) & 1) << 2)) * 16; }
struct WSt { u32 ring, vo0, vo1; };
template <int N> DI WSt w_st(const Ctx& c, int lane) { WSt t; t.ring = c.lbase + L_W + c.w * 24576; t.vo0 = w_voff<N>(lane); t.vo1 = t.vo0 + 8 * ((N < 28 ? F : D) * 2) - 1024; return t; }
template <int N> DI void w_dma(const Ctx& c, const WSt& t, u32 so) { dma_pair<(N % 12) * 2048>(t.ring, t.vo0, t.vo1, N < 28 ? c.rw1 : c.rw2, so); }
DI v4u w_frag(const Ctx& c, u32 wl, int slot, int t) { return trd2(c.L + (wl ^ (u32)(t * 64)) + slot * 2048, 1024); }
template <int N> DI void w_read1(const Ctx& c, u32 wl, W1T& W1, W2T& W2) {
#pragma unroll
    for (int t = 0; t < 2; ++t) {
        if constexpr (N < 28) W1[N][t] = w_frag(c, wl, N % 12, t);
        else W2[N - 28][t] = w_frag(c, wl, N % 12, t);
    }
}
template <int N> DI void w_neg(W2T& W2) {
#pragma unroll
    for (int t = 0; t < 2; ++t) { LV(W2[N - 28][t]); W2[N - 28][t].z ^= SGN; W2[N - 28][t].w ^= SGN; asm volatile("" : "+v"(W2[N - 28][t])); }
}
template <int N0, int N1> DI void w_read(const Ctx& c0, W1T& W1, W2T& W2) {
    const Ctx c = fresh(c0);
    const u32 wl = wl1_of(c, lane_id());
    sfor<N0, N1>([&](auto NC) { w_read1<decltype(NC)::value>(c, wl, W1, W2); });
}

constexpr int PS0 = 0, PS1 = 33;
template <int N0, int N1> DI void pin_a(W1T& W1) {
    if constexpr (N0 < N1) { asm volatile("" : "+a"(W1[N0][0]), "+a"(W1[N0][1])); pin_a<N0 + 1, N1>(W1); }
}
struct G1Out { AccT acc; float mb, r; };
DI constexpr bool reg_start(unsigned long long end, int f) { return f == 0 || ((end >> (f - 1)) & 1ull); }
DI constexpr int reg_len(unsigned long long end, int f) { int n = 1; while (!reg_start(end, f - n + 1)) ++n; return n; }
struct Ad {
    u32 l16;
    u32 ca;
    u32 hst;
    u32 x3o;
    u32 sto;
    u32 so[7];
};
constexpr u32 ST_HG = 3 * 4096;
DI u32 tok32(const Ad& a) { return a.l16 & 496u; }

template <bool TR, int HALF, int S0, unsigned END, class Side>
DI void g1_main(const Ctx& c, Ad& a, const W1T& W1, AccT& acc, Side&& side) {
    const u32 lp = TR ? xtr_of(a.l16 >> 4) : a.l16;
    int kb = c.kb0; asm volatile("" : "+s"(kb));
    u32 la = 0;
    auto ldb = [&](int f) {
        if (f % 7 == 0) la = lp + ((f / 7 + kb) & 3) * 7168;
        LAS char* p = c.L + la + (L_X + HALF * 28672 + (f % 7) * 1024);
        if constexpr (TR) return trd2(p, 512);
        else return *(LAS v4u*)p;
    };
    v4u B[28];
    B[0] = ldb(0); B[1] = ldb(1); B[2] = ldb(2);
    SB();
    sfor<0, 28>([&](auto FC) {
        constexpr int f = decltype(FC)::value;
        if constexpr (reg_start(END, f)) asm volatile("" : "+v"(a.l16), "+v"(a.ca));
        acc[0] = mfma32(W1[f][0], B[f], f == 0 ? zero16() : acc[0]);
        acc[1] = mfma32(W1[f][1], B[f], f == 0 ? zero16() : acc[1]);
        if constexpr (f + 3 < 28) B[f + 3] = ldb(f + 3);
        side(FC);
        if constexpr ((END >> f) & 1u) { ilv<2 * reg_len(END, f), 5>(); SB(); }
    });
}
struct St1 { u32 ad; v4u fr[2]; float s1, s2; };
template <bool TR, int HALF, int K> DI void st1_read(const Ctx& c, St1& s) {
    if constexpr (TR) s.fr[K & 1] = trd2(c.L + s.ad + (L_X + HALF * 28672 + K * 1024), 512);
    else s.fr[K & 1] = *(LAS v4u*)(c.L + s.ad + (L_X + HALF * 28672 + K * 1024));
}
template <int K, int E0, int E1> DI void st1_acc(St1& s) {
#pragma unroll
    for (int e = E0; e < E1; ++e) { if (e == E0) LV(s.fr[K & 1]); const u32 x = s.fr[K & 1][e >> 1]; if (e & 1) s.s2 = dot2(x, x, s.s2); else s.s1 = dot2(x, 0x3f803f80u, s.s1); }
}
DI void part_put(const Ctx& c, const Ad& a, int off, float s1, float s2) { *(LAS f32x2*)(c.L + off + tok32(a) * 2 + c.w * 8) = f32x2{hsum(s1), hsum(s2)}; }
DI f32x2 part_get(const Ctx& c, const Ad& a, int off) {
    const f32x4 u = *(LAS f32x4*)(c.L + off + tok32(a) * 2), v = *(LAS f32x4*)(c.L + off + tok32(a) * 2 + 16);
    return f32x2{(u[0] + u[2]) + (v[0] + v[2]), (u[1] + u[3]) + (v[1] + v[3])};
}
DI void frag_sums(const v4u& p, float& s1, float& s2) {
    s1 = 0.f; s2 = 0.f;
#pragma unroll
    for (int e = 0; e < 4; ++e) { s1 = dot2(p[e], 0x3f803f80u, s1); s2 = dot2(p[e], p[e], s2); }
}
template <int HALF, int KIND> DI void stat_pub(const Ctx& c, const Ad& a, int off, int who) {
    if (c.w == who) {
        f32x2 t = part_get(c, a, off);
        const bool z = c.j == 6 && a.l16 >= 512u;
        st128(v4u{z ? 0u : __float_as_uint(t[0]), c.tagA, z ? 0u : __float_as_uint(t[1]), c.tagB}, c.rws, z ? a.l16 : tok32(a), a.sto + HALF * ST_HG + KIND * 4096 + c.j * 512);
    }
}
template <bool DO> DI void stat_pub_if(const Ctx& c, const Ad& a) { if constexpr (DO) stat_pub<0, 0>(c, a, L_S2, 0); }
struct Gath { v4u g[4]; };
template <int HALF, int KIND, int I> DI void gath_ld(const Ctx& c, const Ad& a, Gath& g) {
    g.g[I] = ld128(c.rws, a.l16 + I * 1024, a.sto + HALF * ST_HG + KIND * 4096);
}
DI bool gath_bad(const Ctx& c, const Gath& g) {
    u32 d = 0;
#pragma unroll
    for (int i = 0; i < 4; ++i) d = tag_bad(g.g[i], c.tagA, c.tagB, d);
    return ANY(d != 0u);
}
template <int HALF, int KIND> DI void gath_wait(const Ctx& c, const Ad& a, Gath& g) {
    if (gath_bad(c, g)) {
        unsigned spins = 0;
        NOUNROLL do { MEMBAR(); sfor<0, 4>([&](auto IC) { gath_ld<HALF, KIND, decltype(IC)::value>(c, a, g); }); } while (gath_bad(c, g) && !spin_fail(c, spins, 2u));
    }
}
DI void gath_ln(const Gath& g, float& m, float& r) {
    const float t1 = (__uint_as_float(g.g[0].x) + __uint_as_float(g.g[1].x)) + (__uint_as_float(g.g[2].x) + __uint_as_float(g.g[3].x));
    const float t2 = (__uint_as_float(g.g[0].z) + __uint_as_float(g.g[1].z)) + (__uint_as_float(g.g[2].z) + __uint_as_float(g.g[3].z));
    ln_scalars(hsum(t1), hsum(t2), m, r);
}
DI f32x4 epi_cs(const Ctx& c, const Ad& a, int E) { return *(LAS f32x4*)(c.L + a.ca + E * 32); }
template <int HALF, int MLP, int E>
DI void g1_epi(const Ctx& c, const Ad& a, G1Out& o, f32x4& cs, v4u& fr) {
    constexpr int t = E >> 2, r0 = 4 * (E & 3), g = E & 1;
    constexpr bool neg = (g ^ MLP) != 0;
    float h[4];
    LV(cs); LA(o.acc[t]);
#pragma unroll
    for (int e = 0; e < 4; ++e) h[e] = __builtin_fmaf(-o.mb, cs[e], o.acc[t][r0 + e]);
#pragma unroll
    for (int e = 0; e < 2; ++e) {
        const u32 p = __builtin_bit_cast(u32, __builtin_elementwise_max(__builtin_bit_cast(s16x2, pk(h[2 * e], h[2 * e + 1])), s16x2{0, 0}));
        fr[2 * g + e] = neg ? (p | SGN) : p;
    }
    if (g == 0) LV(fr);
}
template <int HALF, int K> DI void h_store(const Ctx& c, const Ad& a, const v4u& fr) { st128<K * 1024>(fr, c.rws, a.l16, a.hst + HALF * 114688); }

#define NW0_ 0
#define NT0_ 0
constexpr int NT0 = NT0_;
#define NW1_ 4
constexpr int NW1 = NW1_;
#define KP_ 4
constexpr int KP = KP_;
#define TLBW_ 1
constexpr int TLBW = TLBW_;
#define SLP0 0
#define HT1 0
#define HT2 13
#define SLP1 16
constexpr int NW0 = NW0_;
#define EPE 8
constexpr int HR = 10, GL = 14, GC = 20;
template <int MLP> DI bool h_bad(const v4u& a, const v4u& b) {
    const u32 lo = MLP == 0 ? (a.x | a.y | b.x | b.y) : ~(a.x & a.y & b.x & b.y);
    const u32 hi = MLP == 0 ? ~(a.z & a.w & b.z & b.w) : (a.z | a.w | b.z | b.w);
    return ((lo | hi) & SGN) != 0u;
}
DI u32 h_base(const Ctx& c, int half) { return OFF_H + ((c.grp * 2 + half) * 112 + 4 * c.w) * 1024; }
template <bool ASM> DI v2u gate_ld(const Ctx& c, const Ad& a, int half) {
    const u32 vo = min(a.l16, 96u) * 1024, so = a.hst - c.j * 16384 + half * 114688 + 4;
    if constexpr (ASM) { v2u r; asm volatile("buffer_load_dwordx2 %0, %1, %2, %3 offen sc1" : "=v"(r) : "v"(vo), "s"(c.rws), "s"(so) : "memory"); return r; }
    else return __builtin_amdgcn_raw_buffer_load_b64(c.rws, vo, so, AUX_SC1);
}
template <int MLP> DI bool gate_bad(v2u x) { return ((MLP == 0 ? (x.x | ~x.y) : (~x.x | x.y)) & SGN) != 0u; }
template <int MLP> DI void gate_wait(const Ctx& c, const Ad& a, int half, v2u x) {
    if (ANY(gate_bad<MLP>(x))) {
        unsigned spins = 0;
        NOUNROLL do { MEMBAR(); x = gate_ld<false>(c, a, half); } while (ANY(gate_bad<MLP>(x)) && !spin_fail(c, spins, 1u));
    }
}
template <int N> DI v4u h_next(const Ctx& c, Ad& a) {
    constexpr int f = N >= 28 ? N - 28 : N;
    if constexpr (N >= 32 && N < 38) a.so[k_mem(f)] += 114688u;
    v4u r = __builtin_amdgcn_raw_buffer_load_b128(c.rws, a.l16 + k_sub(f) * 1024, a.so[k_mem(f)], AUX_SC1);
    if constexpr (N >= 50) a.so[k_mem(f)] -= 114688u;
    return r;
}
template <int MLP, int F_> DI void h_check(const Ctx& c0, v4u& a, v4u& b) {
    if (__builtin_expect(ANY(h_bad<MLP>(a, b)), 0)) {
        const Ctx c = fresh(c0);
        constexpr int half = F_ >= 28, f = F_ - 28 * half;
        const u32 vo = lane_id() * 16 + k_sub(f) * 1024, s0 = h_base(c, half) + member(c, k_mem(f)) * 16384, s1 = h_base(c, half) + member(c, k_mem(f + 1)) * 16384;
        unsigned spins = 0;
        NOUNROLL do {
            MEMBAR();
            a = ld128(c.rws, vo, s0); b = ld128(c.rws, vo, s1);
        } while (ANY(h_bad<MLP>(a, b)) && !spin_fail(c, spins, 3u));
    }
}
template <int HALF, int P> DI void red_write(const Ctx& c, const Ad& a, const AccT& acc) {
    LAS char* dst = c.L + (a.l16 + (P ^ c.w) * 6144) + (L_X + (1 - HALF) * 28672 + (P - 1) * 2048);
    const f32x16& x = acc[P >> 1];
    constexpr int r0 = (P & 1) * 8;
    *(LAS f32x4*)dst = f32x4{x[r0], x[r0 + 1], x[r0 + 2], x[r0 + 3]};
    *(LAS f32x4*)(dst + 1024) = f32x4{x[r0 + 4], x[r0 + 5], x[r0 + 6], x[r0 + 7]};
}
template <int HALF> DI void red_read(const Ctx& c, const Ad& a, f32x4 (&t)[6]) {
    LAS char* src = c.L + (a.l16 + c.w * 6144) + (L_X + (1 - HALF) * 28672);
#pragma unroll
    for (int i = 0; i < 6; ++i) t[i] = *(LAS f32x4*)(src + i * 1024);
}
template <int K> DI void post_half(const f32x4& o, float r, v4u& xres) {
#pragma unroll
    for (int e = 0; e < 2; ++e) xres[2 * K + e] = pk(__builtin_fmaf(o[2 * e], r, bflo(xres[2 * K + e])), __builtin_fmaf(o[2 * e + 1], r, bfhi(xres[2 * K + e])));
}
template <int HALF> DI void out_store(const Ctx& c, const Ad& a, const v4u& x, __amdgpu_buffer_rsrc_t rout) {
    auto r0 = __builtin_amdgcn_permlane32_swap(x.x, x.z, false, false);
    auto r1 = __builtin_amdgcn_permlane32_swap(x.y, x.w, false, false);
    const v4u v = {r0[0], r1[0], r0[1], r1[1]};
    const u32 vo = tok32(a) * (D / 8) + (a.l16 >> 9) * 16, so = (u32)((c.grp * 64 + HALF * 32) * D + 64 * c.j + 16 * c.w) * 2;
    asm volatile("buffer_store_dwordx4 %0, %1, %2, %3 offen\n\ts_nop 1" :: "v"(v), "v"(vo), "s"(rout), "s"(so) : "memory");
}
template <int HALF, int MLP> DI v4u xres_ld(const Ctx& c, const Ad& a) {
    if constexpr (MLP == 0) return trd2(c.L + (xtr_of(a.l16 >> 4) + (4 * c.j + c.w) * 1024) + (L_X + HALF * 28672), 512);
    else return *(LAS v4u*)(c.L + (a.l16 + (4 * c.j + c.w) * 1024) + (L_X + HALF * 28672));
}
template <int HALF> DI v4u y_ld(const Ctx& c, const Ad& a) { return trd2(c.L + (xtr_of(a.l16 >> 4) + c.w * 1024) + (L_X + HALF * 28672 + 24576), 512); }
DI void x3_make(v4u& x, const v4u& yf, float m, float r) {
    const float n = -m * r;
#pragma unroll
    for (int e = 0; e < 4; ++e) {
        const float p = bflo(x[e]), q = bfhi(x[e]);
        x[e] = pk(__builtin_fmaf(__builtin_fmaf(p, r, n), bflo(yf[e]), p), __builtin_fmaf(__builtin_fmaf(q, r, n), bfhi(yf[e]), q));
    }
}
template <int HALF> DI void x3_store(const Ctx& c, const Ad& a, const v4u& x) { st128(x, c.rws, a.l16, a.x3o + HALF * 28672); }
template <int HALF, int I> DI v4u x3_ld1(const Ctx& c, const Ad& a) {
    return ld128(c.rws, a.l16 + (I & 3) * 1024, a.x3o + (6 * c.w - 4 * c.j + (I & 4)) * 1024 + HALF * 28672);
}
DI u32 max3u(u32 a, u32 b, u32 c) { u32 r; asm("v_max3_u32 %0, %1, %2, %3" : "=v"(r) : "v"(a), "v"(b), "v"(c)); return r; }
DI bool x3_bad(const v4u (&t)[7]) {
    u32 mx = 0;
#pragma unroll
    for (int i = 0; i < 7; ++i) mx = max3u(max3u(mx, t[i].x, t[i].y), t[i].z, t[i].w);
    return ANY(mx == ~0u);
}
template <bool EQ> DI void gate_spin(const Ctx& c, u32 vo, u32 so, u32 word, u32 code) {
    unsigned spins = 0;
    NOUNROLL for (;;) {
        MEMBAR();
        const u32 x = __builtin_amdgcn_raw_buffer_load_b32(c.rws, vo, so, AUX_SC1);
        if (!ANY(EQ ? x == word : x != word)) break;
        if (spin_fail(c, spins, code)) break;
    }
}
template <int HALF> DI void x3_wait(const Ctx& c, const Ad& a, v4u (&t)[7]) {
    if (x3_bad(t)) {
        unsigned spins = 0;
        NOUNROLL do { MEMBAR(); sfor<0, 7>([&](auto IC) { t[decltype(IC)::value] = x3_ld1<HALF, decltype(IC)::value>(c, a); }); } while (x3_bad(t) && !spin_fail(c, spins, 4u));
    }
}
template <int HALF, int I> DI void x3_put1(const Ctx& c, const Ad& a, const v4u& t) { *(LAS v4u*)(c.L + (a.l16 + c.w * 7168) + (L_X + HALF * 28672 + I * 1024)) = t; }

template <int MLP>
DI void g2_pair(const Ctx& c, Ad& a, const W2T& W2, v4u (&hb)[56], G1Out& gb, f32x4 (&csq)[3], float r0, v4u (&xres)[2], __amdgpu_buffer_rsrc_t out) {
    const u32 wl0 = wl2_of(c, a.l16 >> 4), wl1 = wl0 ^ 64u;
    AccT acc[2];
    v2u gate = {0, 0};
    v4u wl[2], fr, yf;
    f32x4 o[2], u[2];
    float s1, s2;
    Gath gs;
    const float ra_ = MLP ? -r0 : r0, rb_ = MLP ? -gb.r : gb.r;
    auto wres = [&](int slot, int k) { return trd2(c.L + (k ? wl1 : wl0) + slot * 2048, 1024); };
    auto fetch = [&](auto NC) {
        constexpr int N = decltype(NC)::value;
        if constexpr (N < 56) {
            if constexpr (N >= 28 && N < 32) hb[N] = *(LAS v4u*)(c.L + (a.l16 + c.w * 4096) + (L_X + (N - 28) * 1024));
            else hb[N] = h_next<N>(c, a);
        }
    };
    auto rd = [&](int i) { return *(LAS f32x4*)(c.L + (a.l16 + c.w * 6144) + (L_X + 28672 + i * 1024)); };
    auto own = [&](int k) { return f32x4{acc[0][0][4 * k], acc[0][0][4 * k + 1], acc[0][0][4 * k + 2], acc[0][0][4 * k + 3]}; };
    wl[0] = wres(8, 0); wl[1] = wres(8, 1);
    SB();
    sfor<0, 28>([&](auto PC) {
        constexpr int F_ = 2 * decltype(PC)::value, H = F_ >= 28, f = F_ - 28 * H;
        constexpr bool res = f < 24;
        constexpr int r0_ = f < 24 ? f >> 1 : f - 12;
        if constexpr (F_ == 24 || F_ == 30 || (MLP == 0 && F_ == 36)) { bar(); SB(); stat_pub_if<MLP == 0 && F_ == 36>(c, a); }
        asm volatile("" : "+v"(a.l16), "+v"(a.ca));
        acc[H][0] = mfma32(W2[r0_][0], hb[F_], f == 0 ? zero16() : acc[H][0]);
        acc[H][1] = mfma32(W2[r0_][1], hb[F_], f == 0 ? zero16() : acc[H][1]);
        fetch(std::integral_constant<int, F_ + (F_ < 8 ? HR - 2 : HR)>{});
        fetch(std::integral_constant<int, F_ + 1 + (F_ < 8 ? HR - 2 : HR)>{});
        if constexpr (F_ == 6) { fetch(std::integral_constant<int, F_ + HR>{}); fetch(std::integral_constant<int, F_ + HR + 1>{}); }
        if constexpr (F_ < 6) csq[(F_ + 2) % 3] = epi_cs(c, a, F_ + 2);
        if constexpr (F_ < 8) g1_epi<1, MLP, F_>(c, a, gb, csq[F_ % 3], fr);
        if constexpr (F_ < 6) csq[F_ % 3] = epi_cs(c, a, F_ + 3);
        if constexpr (F_ < 8) g1_epi<1, MLP, F_ + 1>(c, a, gb, csq[(F_ + 1) % 3], fr);
        if constexpr (F_ == 32) { LA(acc[0][0]); LV(o[0]); LV(u[0]); LV(u[1]); o[0] = (o[0] + own(0)) + (u[0] + u[1]); post_half<0>(o[0], ra_, xres[0]); LV(xres[0]); }
        if constexpr (F_ == 34) { LA(acc[0][0]); LV(o[0]); LV(u[0]); LV(u[1]); o[0] = (o[0] + own(1)) + (u[0] + u[1]); post_half<1>(o[0], ra_, xres[0]); LV(xres[0]); }
        acc[H][0] = mfma32(res ? wl[0] : W2[r0_ + 1][0], hb[F_ + 1], acc[H][0]);
        acc[H][1] = mfma32((res || f == 26) ? wl[1] : W2[r0_ + 1][1], hb[F_ + 1], acc[H][1]);
        if constexpr (f < 22) { wl[0] = wres((9 + (f >> 1)) % 12, 0); wl[1] = wres((9 + (f >> 1)) % 12, 1); }
        if constexpr (f == 24) wl[1] = *(LAS v4u*)(c.L + (a.l16 + c.w * 1024) + L_JK);
        if constexpr (F_ == 26) { wl[0] = wres(8, 0); wl[1] = wres(8, 1); }
        if constexpr (F_ == 22) xres[1] = xres_ld<1, MLP>(c, a);
        if constexpr (F_ == 30) { u[0] = rd(0); u[1] = rd(2); o[0] = rd(4); }
        if constexpr (F_ == 32) { u[0] = rd(1); u[1] = rd(3); o[0] = rd(5); }
        if constexpr (MLP == 0 && F_ == 54) yf = y_ld<0>(c, a);
        if constexpr (F_ == GL) gate = gate_ld<false>(c, a, 1);
        if constexpr (F_ < 8) { *(LAS v4u*)(c.L + (a.l16 + c.w * 4096) + (L_X + (F_ / 2) * 1024)) = fr; h_store<1, F_ / 2>(c, a, fr); }
        if constexpr (MLP == 0 && (F_ == 24 || F_ == 26))
            dma16<true>(c.lbase + L_X + (F_ - 24) / 2 * 28672 + 24576 + c.w * 1024, a.l16, c.rws, OFF_YT + (c.grp * 2 + (F_ - 24) / 2) * 28672 + c.j * 4096 + c.w * 1024);
        if constexpr (F_ == 28) { red_write<0, 1>(c, a, acc[0]); red_write<0, 2>(c, a, acc[0]); red_write<0, 3>(c, a, acc[0]); }
        if constexpr (MLP == 0 && F_ == 34) { frag_sums(xres[0], s1, s2); part_put(c, a, L_S2, s1, s2); }
        if constexpr (MLP == 1 && F_ == 36) out_store<0>(c, a, xres[0], out);
        ilv<4, 5>();
        if constexpr (F_ + 2 < 56 && f + 2 != 28 && f + 2 >= 4) h_check<MLP, F_ + 2>(c, hb[F_ + 2], hb[F_ + 3]);
        if constexpr (F_ == GC) gate_wait<MLP>(c, a, 1, gate);
        SB();
    });
    static_assert(GC + 2 + HR <= 32 && (GC & 1) == 0 && (HR & 1) == 0, "half B's first foreign fragment is requested behind the gate");

    red_write<1, 1>(c, a, acc[1]); red_write<1, 2>(c, a, acc[1]); red_write<1, 3>(c, a, acc[1]);
    if constexpr (MLP == 0) sfor<0, 4>([&](auto IC) { gath_ld<0, 0, decltype(IC)::value>(c, a, gs); });
    bar();
    f32x4 t[6];
    red_read<1>(c, a, t);
    o[0] = (f32x4{acc[1][0][0], acc[1][0][1], acc[1][0][2], acc[1][0][3]} + t[0]) + (t[2] + t[4]);
    o[1] = (f32x4{acc[1][0][4], acc[1][0][5], acc[1][0][6], acc[1][0][7]} + t[1]) + (t[3] + t[5]);
    post_half<0>(o[0], rb_, xres[1]); post_half<1>(o[1], rb_, xres[1]);
    if constexpr (MLP == 0) {
        frag_sums(xres[1], s1, s2); part_put(c, a, L_S2, s1, s2);

        bar();
        stat_pub<1, 0>(c, a, L_S2, 1);

        gath_wait<0, 0>(c, a, gs);
        gath_ln(gs, s1, s2);
        x3_make(xres[0], yf, s1, s2);
        x3_store<0>(c, a, xres[0]);

        yf = y_ld<1>(c, a);
        __builtin_amdgcn_s_sleep(SLP1);
        v4u x[7];
        unsigned spins = 0;
        NOUNROLL do {
            MEMBAR();
            sfor<0, 4>([&](auto IC) { gath_ld<1, 0, decltype(IC)::value>(c, a, gs); });
            sfor<0, 7>([&](auto IC) { x[decltype(IC)::value] = x3_ld1<0, decltype(IC)::value>(c, a); });
        } while ((gath_bad(c, gs) || x3_bad(x)) && !spin_fail(c, spins, 4u));

        gath_ln(gs, s1, s2);
        x3_make(xres[1], yf, s1, s2);
        x3_store<1>(c, a, xres[1]);
        sfor<0, 7>([&](auto IC) { x3_put1<0, decltype(IC)::value>(c, a, x[decltype(IC)::value]); });
    } else out_store<1>(c, a, xres[1], out);
}

__global__ void __launch_bounds__(256, 1) fused_kernel(const u16* __restrict__ x0, const u16* __restrict__ kin, const u16* __restrict__ vin,
                                                       const u16* __restrict__ sk, const u16* __restrict__ hk, const u16* __restrict__ Dv,
                                                       const u16* __restrict__ w1, const u16* __restrict__ w2,
                                                       unsigned char* __restrict__ ws, u16* __restrict__ out) {
    extern __shared__ __attribute__((aligned(16))) char smem[];
    Ctx c;
    c.L = (LAS char*)smem;
    c.lbase = (u32)(uintptr_t)c.L;
    const int b = blockIdx.x, tid = threadIdx.x;
    c.w = __builtin_amdgcn_readfirstlane(tid >> 6);
    c.rws = mkrs(ws, WS_BYTES);
    c.tmo = (GAS u32*)(ws + 256);

    if (b < 32) {
        const v2u ep = __builtin_amdgcn_raw_buffer_load_b64(c.rws, 0, OFF_EP, AUX_SC1);
        v4u xr[2][7];
#pragma unroll
        for (int h = 0; h < 2; ++h)
#pragma unroll
            for (int i = 0; i < 7; ++i) xr[h][i] = *(const v4u*)(x0 + (b * 64 + 2 * (tid >> 3) + h) * D + (i * 8 + (tid & 7)) * 8);
        MEMBAR();
        {
            unsigned long long pc; u32 back, len;
            asm volatile("s_getpc_b64 %0\n.Lfk_here:\n\ts_mov_b32 %1, .Lfk_here-.Lfk_begin\n\ts_mov_b32 %2, .Lfk_end-.Lfk_begin" : "=s"(pc), "=s"(back), "=s"(len));
            if (c.w < 2) touch(c.lbase + L_JK, (tid & 63) * 128, mkrs((const void*)(pc - back), len), ((b >> 3) * 2 + c.w) * 8192);
        }
        u32 res[2];
#pragma unroll
        for (int h = 0; h < 2; ++h) {
            float s1 = 0.f, s2 = 0.f;
#pragma unroll
            for (int i = 0; i < 7; ++i)
#pragma unroll
                for (int e = 0; e < 4; ++e) { s1 = dot2(xr[h][i][e], 0x3f803f80u, s1); s2 = dot2(xr[h][i][e], xr[h][i][e], s2); }
#pragma unroll
            for (int o = 1; o < 8; o <<= 1) { s1 += __shfl_xor(s1, o); s2 += __shfl_xor(s2, o); }
            const float m = s1 * (1.0f / D);
            res[h] = pk(m, ln_sd(fmaxf(__builtin_fmaf(-m, m, s2 * (1.0f / D)), 0.f)));
        }
        if ((tid & 7) == 0) st128(v4u{res[0], ep.x + 1u, res[1], ep.y + 1u}, c.rws, (u32)(b * 32 + (tid >> 3)) * 16, OFF_ST0);
        const int rg = tid >> 3, c8 = min(tid & 7, 6);
        v4u wr[14];
#pragma unroll
        for (int i = 0; i < 14; ++i) wr[i] = *(const v4u*)(w1 + (rg * 14 + i) * F + b * 56 + c8 * 8);
        float cs[8] = {0, 0, 0, 0, 0, 0, 0, 0};
#pragma unroll
        for (int i = 0; i < 14; ++i)
#pragma unroll
            for (int e = 0; e < 4; ++e) { cs[2 * e] += bflo(wr[i][e]); cs[2 * e + 1] += bfhi(wr[i][e]); }
        LAS float* sf = (LAS float*)c.L;
#pragma unroll
        for (int e = 0; e < 8; ++e) sf[rg * 65 + (tid & 7) * 8 + e] = cs[e];
        bar();
        if (tid < 56) {
            float t = 0.f;
#pragma unroll
            for (int i = 0; i < 32; ++i) t += sf[i * 65 + tid];
            const float t2 = __shfl_down(t, 1);
            if ((tid & 1) == 0) st128(v4u{__float_as_uint(t), ep.x + 1u, __float_as_uint(t2), ep.y + 1u}, c.rws, (u32)(b * 28 + (tid >> 1)) * 16, OFF_CSG);
        }
        WAIT_VM(0);
        bar();
        {
            const __amdgpu_buffer_rsrc_t rw1 = mkrs(w1, D * F * 2), rw2 = mkrs(w2, D * F * 2);
#pragma unroll
            for (int i = 0; i < HT1; ++i) touch(c.lbase + L_JK, (tid & 63) * 128, rw1, (u32)(112 + i * 16 + (b >> 3) * 4 + c.w) * 8192);
#pragma unroll
            for (int i = 0; i < HT2; ++i) touch(c.lbase + L_JK, (tid & 63) * 128, rw2, (u32)(i * 16 + (b >> 3) * 4 + c.w) * 8192);
        }
        WAIT_VM(0);
        return;
    }
    asm volatile(".Lfk_begin:");
    const int wb = b - 32;
    { const int xcd = wb & 7, idx = wb >> 3; c.grp = xcd * 4 + idx / 7; c.j = idx % 7; }
    c.gi = c.grp & 3;
    const int w = c.w;
    c.rw1 = mkrs(w1, D * F * 2); c.rw2 = mkrs(w2, D * F * 2);
    c.kb0 = (w + c.gi + 2) & 3;
    const int cp = (wb & 7) * 28 + (wb >> 3);
    W1T W1; W2T W2;

    u32 dval;
    {
        const int g = wb % 7, rb = wb / 7, lane = tid & 63;
        const int rg = (lane & 7) * 2 + ((lane >> 4) & 1), cl = ((lane >> 3) & 1) + 2 * (lane >> 5), cq = 4 * w + cl;
        const __amdgpu_buffer_rsrc_t rk = mkrs(kin, TSZ), rv = mkrs(vin, TSZ), rx = mkrs(x0, TSZ), rh = mkrs(hk, TSZ), rs_ = mkrs(sk, D * 8), rd = mkrs(Dv, D * 2);

        const u32 tb = (u32)((rb * 64) * 896 + g * 128);
        const u32 tv0 = (u32)((lane >> 3) * 896) + (((lane & 7) ^ ((lane >> 5) + 2 * w)) & 7) * 16;
        dma16<false>(c.lbase + T_X + w * 1024, tv0, rx, tb + w * 7168);
        dma16<false>(c.lbase + T_X + (w + 4) * 1024, tv0, rx, tb + (w + 4) * 7168);
        dma16<false>(c.lbase + T_K + w * 1024, tv0 + tb + (w - 1) * 7168, rk, 0u);
        dma16<false>(c.lbase + T_K + (w + 4) * 1024, tv0, rk, tb + (w + 3) * 7168);
        dma16<false>(c.lbase + T_V + w * 1024, tv0, rv, tb + w * 7168);
        dma16<false>(c.lbase + T_V + (w + 4) * 1024, tv0, rv, tb + (w + 4) * 7168);
        if (w == 0) dma16<false>(c.lbase + T_K + 8192, tv0, rk, tb + 7 * 7168);
        else dma16<false>(c.lbase + T_S + (w - 1) * 1024, (u32)lane * 16, rs_, g * 512);
        v4u ep4 = ld128_asm<true>(c.rws, 0u, OFF_EP);
        v4u he[2];
        he[0] = ld128_asm<false>(rh, tid * 16, cp * 8192); he[1] = ld128_asm<false>(rh, tid * 16, cp * 8192 + 4096);
        v4u dv4 = ld128_asm<false>(rd, 0u, cp * 4);
        {
            const WSt wt = w_st<0>(c, lane);
            sfor<0, NW0>([&](auto NC) { w_dma<decltype(NC)::value>(c, wt, w_soff<decltype(NC)::value>(c)); });
        }
        if constexpr (TLBW) {
            if (w == 0) touch(c.lbase + L_JK + 3072, (u32)lane * 524288u, c.rws, 4096u);
            else touch(c.lbase + L_JK + 3072, (u32)lane * 262144u, w == 1 ? c.rw1 : w == 2 ? c.rw2 : mkrs(out, TSZ), 0u);
        }
        if constexpr (NT0) touch(c.lbase + L_JK + 3072, (u32)lane * 128, c.rw1, (u32)((wb >> 3) * 4 + w) * 8192);

        asm volatile("s_waitcnt vmcnt(%1)" : "+v"(ep4) : "i"(3 + NT0 + TLBW + 2 * NW0) : "memory");
        c.tagA = __builtin_amdgcn_readfirstlane(ep4.x) + 1u; c.tagB = __builtin_amdgcn_readfirstlane(ep4.y) + 1u;
        bar();

        const u32 a1 = (u32)(rg * 512 + (((cq >> 1) ^ rg) & 7) * 16 + (cq & 1) * 8);
        const u32 ak1 = (u32)(rg * 512 + 1024 + (((cq >> 1) ^ (rg + 2)) & 7) * 16 + (cq & 1) * 8);
        const u32 ak0 = (u32)(rg * 512 + 512 + (((cq >> 1) ^ (rg + 1)) & 7) * 16 + (cq & 1) * 8);
        v2u kr[7], vr[4], xr[4];
#pragma unroll
        for (int i = 0; i < 4; ++i) xr[i] = *(LAS v2u*)(c.L + T_X + a1 + i * 128);
        const v4u w01 = *(LAS v4u*)(c.L + T_S + cq * 32), w23 = *(LAS v4u*)(c.L + T_S + cq * 32 + 16);
#pragma unroll
        for (int i = 0; i < 3; ++i) kr[i] = *(LAS v2u*)(c.L + T_K + ak0 + (i + 1) * 128);
#pragma unroll
        for (int i = 0; i < 4; ++i) { kr[3 + i] = *(LAS v2u*)(c.L + T_K + ak1 + i * 128); vr[i] = *(LAS v2u*)(c.L + T_V + a1 + i * 128); }
#pragma unroll
        for (int h = 0; h < 2; ++h) {
            auto r0 = __builtin_amdgcn_permlane16_swap(__builtin_amdgcn_perm(xr[1][h], xr[0][h], 0x05040100u), __builtin_amdgcn_perm(xr[1][h], xr[0][h], 0x07060302u), false, false);
            auto r1 = __builtin_amdgcn_permlane16_swap(__builtin_amdgcn_perm(xr[3][h], xr[2][h], 0x05040100u), __builtin_amdgcn_perm(xr[3][h], xr[2][h], 0x07060302u), false, false);
            st128(v4u{r0[0], r1[0], r0[1], r1[1]}, c.rws, (u32)((cl * 4 + ((lane >> 4) & 1)) * 4096 + (lane & 7) * 16), OFF_X0G + (g * 64 + 16 * w + 2 * h) * 4096 + rb * 128);
        }

        v4u kg[4];
#pragma unroll
        for (int e = 0; e < 4; ++e) {
            const v4u& wq = e < 2 ? w01 : w23;
            const float w0 = bflo(wq[(e & 1) * 2]), w1_ = bfhi(wq[(e & 1) * 2]), w2_ = bflo(wq[(e & 1) * 2 + 1]), w3 = bfhi(wq[(e & 1) * 2 + 1]);
            float kk[7], o[4];
#pragma unroll
            for (int i = 0; i < 7; ++i) kk[i] = (e & 1) ? bfhi(kr[i][e >> 1]) : bflo(kr[i][e >> 1]);
#pragma unroll
            for (int i = 0; i < 4; i += 2) {
                u32 a_ = pk(kk[i] * w3, kk[i + 1] * w3), b_ = pk(kk[i + 1] * w2_, kk[i + 2] * w2_);
                a_ = pk(bflo(a_) + bflo(b_), bfhi(a_) + bfhi(b_)); b_ = pk(kk[i + 2] * w1_, kk[i + 3] * w1_);
                a_ = pk(bflo(a_) + bflo(b_), bfhi(a_) + bfhi(b_)); b_ = pk(kk[i + 3] * w0, kk[i + 4] * w0);
                a_ = pk(bflo(a_) + bflo(b_), bfhi(a_) + bfhi(b_));
                o[i] = bflo(a_) * ((e & 1) ? bfhi(vr[i][e >> 1]) : bflo(vr[i][e >> 1]));
                o[i + 1] = bfhi(a_) * ((e & 1) ? bfhi(vr[i + 1][e >> 1]) : bflo(vr[i + 1][e >> 1]));
            }
            kg[e] = v4u{pk(o[3], o[2]), c.tagA, pk(o[1], o[0]), c.tagB};
        }

        asm volatile("s_waitcnt vmcnt(0)\n\ts_barrier" : "+v"(he[0]), "+v"(he[1]), "+v"(dv4), "+v"(kg[0]), "+v"(kg[1]), "+v"(kg[2]), "+v"(kg[3]) :: "memory");

#pragma unroll
        for (int e = 0; e < 4; ++e) st128(kg[e], c.rws, (u32)(cl * 4 * 8192 + (15 - rg) * 16), OFF_KVG + ((g * 64 + 16 * w + e) * 512 + 496 - 16 * rb) * 16);
        {
            const WSt wt = w_st<0>(c, lane);
            sfor<NW0, NW1>([&](auto NC) { w_dma<decltype(NC)::value>(c, wt, w_soff<decltype(NC)::value>(c)); });
        }

        dval = (w >> 1) ? dv4.x >> 16 : dv4.x & 0xffffu;
#pragma unroll
        for (int cc = 0; cc < 2; ++cc) {
            LAS char* cb = c.L + CV_B + cc * CV_CH;
            *(LAS v4u*)(cb + CV_HE + 32 + tid * 16) = he[cc];
            if (tid < 2) { *(LAS v4u*)(cb + CV_HE + tid * 16) = v4u{0, 0, 0, 0}; *(LAS v4u*)(cb + CV_HO + tid * 16) = v4u{0, 0, 0, 0}; }
            if (tid < 32) *(LAS v4u*)(cb + CV_KV + 4096 + tid * 16) = v4u{0, 0, 0, 0};
        }
        bar();
#pragma unroll
        for (int cc = 0; cc < 2; ++cc) {
            const v4u e = he[cc];
            const u32 pv = *(LAS u32*)(c.L + CV_B + cc * CV_CH + CV_HE + 28 + tid * 16);
            *(LAS v4u*)(c.L + CV_B + cc * CV_CH + CV_HO + 32 + tid * 16) = v4u{__builtin_amdgcn_alignbit(e.x, pv, 16), __builtin_amdgcn_alignbit(e.y, e.x, 16),
                                                                            __builtin_amdgcn_alignbit(e.z, e.y, 16), __builtin_amdgcn_alignbit(e.w, e.z, 16)};
        }
        bar();

        SB();
    }
    {
        const int lane = lane_id(), i16 = lane & 15, q = lane >> 4, cc = w >> 1, p = w & 1, ch = 2 * cp + cc;
        LAS char* cb = c.L + CV_B + cc * CV_CH;
        const int o0 = i16 + 1 + 32 * p + 8 * q;
        LAS char* ab = cb + ((o0 & 1) ? CV_HO + 4 : CV_HE) + (o0 >> 1) * 4;
        v4u A[32];
        v4u AD;
        {
            const int jj = p ? -1 : 15 - i16 - 8 * q;
            const u32 dv = dval << ((jj & 1) * 16);
#pragma unroll
            for (int d = 0; d < 4; ++d) AD[d] = (jj >= 0 && (jj >> 1) == d) ? dv : 0u;
        }
        const Ctx cs_ = fresh(c);
        const u32 wl = wl1_of(cs_, lane);
        const WSt wt = w_st<0>(cs_, lane);
        v4u sg[4], g[4];
        SB();
        sfor<0, 8>([&](auto NC) {
            constexpr int n = decltype(NC)::value;
#pragma unroll
            for (int t = n * 4; t < n * 4 + 4; ++t) { LAS u32* pp = (LAS u32*)(ab + t * 128); A[t] = v4u{pp[0], pp[1], pp[2], pp[3]}; }
            SB();
        });

        {
            __builtin_amdgcn_s_sleep(SLP0);
            unsigned spins = 0;
            auto bad4 = [&](const v4u (&x)[4]) { u32 d = 0; for (int t = 0; t < 4; ++t) d = tag_bad(x[t], c.tagA, c.tagB, d); return ANY(d != 0u); };
            NOUNROLL do {
                MEMBAR();
#pragma unroll
                for (int i = 0; i < 4; ++i) g[i] = ld128(c.rws, tid * 16, OFF_KVG + cp * 16384 + i * 4096);
            } while (bad4(g) && !spin_fail(c, spins, 5u));

#pragma unroll
            for (int i = 0; i < 4; ++i) *(LAS v2u*)(c.L + CV_B + (i >> 1) * CV_CH + CV_KV + (tid + 256 * (i & 1)) * 8) = v2u{g[i].x, g[i].z};
        }
        bar();

        LAS char* bb = cb + CV_KV + (2032 - 16 * i16 + 32 * p + 8 * q) * 2;
        f32x4 acc[8];
        float xs[4][4], qs[4][4];
        v4u B[32];
        v2u xg[4];
        B[0] = *(LAS v4u*)(bb + 3 * 128); B[1] = *(LAS v4u*)(bb + 2 * 128); B[2] = *(LAS v4u*)(bb + 128);
        const u32 s4 = (u32)(256 * p + 4 * i16 + q), xo = OFF_X0G + ch * 4096;
        SB();
        sfor<0, 32>([&](auto OC) {
            constexpr int oi = decltype(OC)::value, om = oi - 3;
            constexpr int nm = 8 - (om > 0 ? (om + 3) / 4 : 0) + ((om >= 0 && (om & 3) == 0) ? 1 : 0);
            if constexpr (oi == 9) { bar(); SB(); }
            if constexpr (oi + 3 < 32) B[oi + 3] = *(LAS v4u*)(bb - (om + 3) * 128);
#pragma unroll
            for (int cb_ = 7; cb_ >= 0; --cb_)
                if (4 * cb_ - om >= 0) acc[cb_] = mfma16(A[4 * cb_ - om], B[oi], oi == 0 ? f32x4{0, 0, 0, 0} : acc[cb_]);
            if constexpr (om >= 0 && (om & 3) == 0) acc[om >> 2] = mfma16(AD, B[oi], acc[om >> 2]);
            if constexpr (oi < 2) { sg[2 * oi] = ld128_asm<true>(c.rws, tid * 16, OFF_ST0 + oi * 8192); sg[2 * oi + 1] = ld128_asm<true>(c.rws, tid * 16, OFF_ST0 + oi * 8192 + 4096); }
            if constexpr (oi == 2 || oi == 3) {
                asm volatile("buffer_load_dwordx2 %0, %2, %3, %4 offen offset:%5 sc1\n\tbuffer_load_dwordx2 %1, %2, %3, %4 offen offset:%6 sc1"
                             : "=&v"(xg[2 * oi - 4]), "=&v"(xg[2 * oi - 3]) : "v"(s4 * 8), "s"(c.rws), "s"(xo), "i"((oi - 2) * 1024), "i"((oi - 2) * 1024 + 512) : "memory");
            }
            if constexpr (oi < NW1) w_read1<oi>(cs_, wl, W1, W2);
            if constexpr (oi >= 4 && (oi & 1) == 0 && NW1 + (oi - 4) / 2 < 12) w_dma<NW1 + (oi - 4) / 2>(cs_, wt, w_soff<NW1 + (oi - 4) / 2>(cs_));
            if constexpr (oi == 7) {
                asm volatile("s_waitcnt vmcnt(4)" : "+v"(sg[0]), "+v"(sg[1]), "+v"(sg[2]), "+v"(sg[3]), "+v"(xg[0]), "+v"(xg[1]), "+v"(xg[2]), "+v"(xg[3]) :: "memory");
                auto bad = [&]() { u32 d = 0; for (int t = 0; t < 4; ++t) d = tag_bad(sg[t], c.tagA, c.tagB, d); return ANY(d != 0u); };
                if (bad()) {
                    unsigned spins = 0;
                    NOUNROLL do {
                        MEMBAR();
#pragma unroll
                        for (int t = 0; t < 4; ++t) sg[t] = ld128(c.rws, tid * 16, OFF_ST0 + t * 4096);
                    } while (bad() && !spin_fail(c, spins, 6u));
                }
#pragma unroll
                for (int t = 0; t < 4; ++t) *(LAS v2u*)(c.L + CV_ST + (tid + 256 * t) * 8) = v2u{sg[t].x, sg[t].z};
            }
            if constexpr (oi >= 9 && oi < 13) sg[oi - 9] = *(LAS v4u*)(c.L + CV_ST + s4 * 16 + (oi - 9) * 1024);
            if constexpr (oi >= 11 && oi < 19) {
                constexpr int t = (oi - 11) >> 1, hf = (oi - 11) & 1;
                LV(xg[t]); LV(sg[t]);
                const u32 xw = xg[t][hf];
                const u32 sa = sg[t][2 * hf], sb = sg[t][2 * hf + 1];
                xs[t][2 * hf] = bflo(xw); xs[t][2 * hf + 1] = bfhi(xw);
                qs[t][2 * hf] = (xs[t][2 * hf] - bflo(sa)) * __builtin_amdgcn_rcpf(bfhi(sa));
                qs[t][2 * hf + 1] = (xs[t][2 * hf + 1] - bflo(sb)) * __builtin_amdgcn_rcpf(bfhi(sb));
                asm volatile("" : "+v"(qs[t][2 * hf]), "+v"(qs[t][2 * hf + 1]));
            }
            if constexpr (oi == 23 || oi == 25) {
                const v4u ff = {~0u, ~0u, ~0u, ~0u};
                st128(ff, c.rws, lane * 16, OFF_X3 + (cs_.grp * 56 + (oi - 23) * 14 + 4 * cs_.j + cs_.w) * 1024);
            }
            ilv<nm, 2>();
            SB();
        });

        LAS char* ex = c.L;
        f32x4 fin[4];
        if (p == 0) {
#pragma unroll
            for (int t = 0; t < 4; ++t) { *(LAS f32x4*)(ex + w * 4096 + t * 1024 + lane * 16) = acc[4 + t]; fin[t] = acc[t]; }
        } else {
#pragma unroll
            for (int t = 0; t < 4; ++t) { *(LAS f32x4*)(ex + w * 4096 + t * 1024 + lane * 16) = acc[t]; fin[t] = acc[4 + t]; }
        }
        bar();

        const u32 toff = (u32)((32 * p + 8 * (q & 1) + (i16 >> 1)) * 28672 + ch * 64 + (i16 & 1) * 32 + (q >> 1) * 16);
        v2u y[4], x1[4];
#pragma unroll
        for (int t = 0; t < 4; ++t) {
            fin[t] += *(LAS f32x4*)(ex + (w ^ 1) * 4096 + t * 1024 + lane * 16);
            y[t] = v2u{pk(fin[t][0], fin[t][1]), pk(fin[t][2], fin[t][3])};
            x1[t] = v2u{pk(__builtin_fmaf(qs[t][0], bflo(y[t].x), xs[t][0]), __builtin_fmaf(qs[t][1], bfhi(y[t].x), xs[t][1])),
                        pk(__builtin_fmaf(qs[t][2], bflo(y[t].y), xs[t][2]), __builtin_fmaf(qs[t][3], bfhi(y[t].y), xs[t][3]))};
        }
        auto merge = [&](const v2u& a, const v2u& b_) {
            auto r0 = __builtin_amdgcn_permlane16_swap(a.x, b_.x, false, false);
            auto r1 = __builtin_amdgcn_permlane16_swap(a.y, b_.y, false, false);
            return v4u{r0[0], r1[0], r0[1], r1[1]};
        };
        st128(merge(x1[0], x1[1]), c.rws, toff, OFF_X1);
        st128(merge(x1[2], x1[3]), c.rws, toff, OFF_X1 + 16 * 28672);
        st128(merge(y[0], y[1]), c.rws, toff, OFF_YT);
        st128(merge(y[2], y[3]), c.rws, toff, OFF_YT + 16 * 28672);

        sfor<12, 12 + NW1>([&](auto NC) { w_dma<decltype(NC)::value>(cs_, wt, w_soff<decltype(NC)::value>(cs_)); });
        WAIT_VM(6 + 2 * NW1);
        sfor<NW1, 12>([&](auto NC) { w_read1<decltype(NC)::value>(cs_, wl, W1, W2); });
        WAIT_LGKM(0);
        sfor<12 + NW1, 20>([&](auto NC) { w_dma<decltype(NC)::value>(cs_, wt, w_soff<decltype(NC)::value>(cs_)); });

        WAIT_VM(16);
        bar();
        if (tid < 8) __builtin_amdgcn_raw_buffer_store_b64(v2u{c.tagA, c.tagB}, c.rws, (u32)(tid * 2048 + wb * 8), OFF_FL, AUX_SC1);
        MEMBAR();

        sfor<20, 24>([&](auto NC) { w_dma<decltype(NC)::value>(cs_, wt, w_soff<decltype(NC)::value>(cs_)); });
        WAIT_VM(12);
        sfor<12, 18>([&](auto NC) { w_read1<decltype(NC)::value>(cs_, wl, W1, W2); });
        WAIT_LGKM(0);
        sfor<24, 28>([&](auto NC) { w_dma<decltype(NC)::value>(cs_, wt, w_soff<decltype(NC)::value>(cs_)); });

    }
    Ad ad;
    ad.l16 = lane_id() * 16;
    ad.hst = OFF_H + (c.grp * 224 + 16 * c.j + 4 * w) * 1024;
    ad.x3o = OFF_X3 + (c.grp * 56 + 4 * c.j + w) * 1024;
    ad.sto = OFF_ST + c.grp * 2 * ST_HG;
    asm volatile("" : "+s"(ad.hst), "+s"(ad.x3o), "+s"(ad.sto));
    const u32 swl = wl2_of(c, ad.l16 >> 4);
    const WSt w2t = w_st<28>(c, ad.l16 >> 4);
    u32 wso[7];
#pragma unroll
    for (int m = 0; m < 7; ++m) {
        const int mm = member(c, m);
        wso[m] = (256 * mm + 64 * w) * (D * 2) + 128 * c.j; ad.so[m] = ad.hst + (mm - c.j) * 16384;
        asm volatile("" : "+s"(wso[m]), "+s"(ad.so[m]));
    }
    auto w2_dma = [&](auto NC) { constexpr int n = decltype(NC)::value; w_dma<n>(c, w2t, wso[k_mem(w2_step_of(n))] + k_sub(w2_step_of(n)) * 16 * (D * 2)); };
    {
        v4u cg = ld128_asm<true>(c.rws, tok32(ad), OFF_CSG + c.j * 2048 + w * 512);
        sfor<28, 30>(w2_dma);
        WAIT_VM(12);
        w_read<18, 24>(c, W1, W2);

        if (w == 0) {
            unsigned spins = 0;
            NOUNROLL for (;;) {
                MEMBAR();
                const v4u f0 = ld128(c.rws, ad.l16, OFF_FL + (wb & 7) * 2048);
                const v4u f1 = ld128(c.rws, 1024 + min(ad.l16, 752u), OFF_FL + (wb & 7) * 2048);
                const u32 d = xor_or(f0.x, c.tagA, xor_or(f0.z, c.tagA, xor_or(f1.x, c.tagA, xor_or(f1.z, c.tagA, tag_bad(f0, c.tagB, c.tagB, tag_bad(f1, c.tagB, c.tagB, 0u))))));
                if (!ANY(d != 0u)) break;
                if (spin_fail(c, spins, 7u)) break;
            }
            if (wb == 0 && ad.l16 == 0) __builtin_amdgcn_raw_buffer_store_b64(v2u{c.tagA, c.tagB}, c.rws, 0, OFF_EP, AUX_SC1);
        }
        bar();

        {
            const u32 xb = c.lbase + L_X + w * 7168, xs_ = OFF_X1 + c.grp * 57344 + w * 7168;
            sfor<0, 14>([&](auto IC) {
                constexpr int i = decltype(IC)::value, o = (i / 7) * 28672 + (i % 7) * 1024;
                dma16<true>(xb + o, ad.l16, c.rws, xs_ + o);
            });
        }

        WAIT_VM(14);
        w_read<24, 28>(c, W1, W2);
        w_read1<28>(c, swl, W1, W2); w_read1<29>(c, swl, W1, W2);
        w_neg<28>(W2); w_neg<29>(W2);
        LV(cg);
        if (ANY(tag_bad(cg, c.tagA, c.tagB, 0u) != 0u)) {
            unsigned spins = 0;
            NOUNROLL do { MEMBAR(); cg = ld128(c.rws, tok32(ad), OFF_CSG + c.j * 2048 + w * 512); } while (ANY(tag_bad(cg, c.tagA, c.tagB, 0u) != 0u) && !spin_fail(c, spins, 8u));
        }
        *(LAS v2u*)(c.L + L_CS + w * 256 + (tok32(ad) >> 1)) = v2u{cg.x, cg.z};
        WAIT_VM(7);
        bar();

    }

    pin_a<0, 25>(W1);
    v4u hb[56], xres[2];
    G1Out ga, gb;
    v2u gate;
    f32x4 csq[3];
    St1 st;
    ad.ca = L_CS + (w * 64 + (ad.l16 >> 9) * 4) * 4;
    using M0 = std::integral_constant<int, 0>; using M1 = std::integral_constant<int, 1>;
    {
        st.ad = xtr_of(ad.l16 >> 4) + w * 7168;
        auto stats = [&](auto MC, auto HC, auto FC, G1Out& o) {
            constexpr int H = decltype(HC)::value, f = decltype(FC)::value;
            constexpr bool TR = decltype(MC)::value == 0;
            if constexpr (f == 0) { st.s1 = 0.f; st.s2 = 0.f; }
            if constexpr (f >= 2 && f < 23) { constexpr int ph = (f - 2) % 3, k = (f - 2) / 3; st1_acc<k, ph * 3, ph == 2 ? 8 : ph * 3 + 3>(st); }
            if constexpr (f % 3 == 0 && f < 21) st1_read<TR, H, f / 3>(c, st);
            if constexpr (f == 23) part_put(c, ad, L_SP + H * 1024, st.s1, st.s2);
            if constexpr (f == 24) { SB(); asm volatile("s_waitcnt lgkmcnt(%0)\n\ts_barrier" :: "i"(TR ? 2 : 1) : "memory"); }
            if constexpr (f == 25) { const f32x2 t = part_get(c, ad, L_SP + H * 1024); ln_scalars(t[0], t[1], o.mb, o.r); asm volatile("" : "+v"(o.mb), "+v"(o.r)); }
        };
        auto stage = [&](auto GC_, auto XC) {
            constexpr int g = decltype(GC_)::value, extra = decltype(XC)::value;
            if constexpr (g >= 15 && g < 43 && (g & 1)) w_neg<30 + (g - 15) / 2>(W2);
            if constexpr (g == 41) *(LAS v4u*)(c.L + (ad.l16 + w * 1024) + L_JK) = W2[15][1];
            if constexpr (g >= 13 && g < 41 && (g & 1)) { WAIT_VM(12 + extra); w_read1<30 + (g - 13) / 2>(c, swl, W1, W2); }
            if constexpr (g < 52 && (g & 1) == 0) w2_dma(std::integral_constant<int, 30 + g / 2>{});
        };
        constexpr unsigned E2 = 0xAAAAAAAu;
        g1_main<true, 0, PS0, 0x9AAAAAAu>(c, ad, W1, ga.acc, [&](auto FC) {
            constexpr int f = decltype(FC)::value;
            stage(FC, M0{});
            stats(M0{}, M0{}, FC, ga);
            if constexpr (f >= 26) csq[f - 26] = epi_cs(c, ad, f - 26);
        });

        auto side_b = [&](auto MC, auto FC) {
            constexpr int f = decltype(FC)::value, M = decltype(MC)::value;
            if constexpr (f < 6) csq[(f + 2) % 3] = epi_cs(c, ad, f + 2);
            if constexpr (f < 8) g1_epi<0, M, f>(c, ad, ga, csq[f % 3], hb[f >> 1]);
            if constexpr (f < 8 && (f & 1)) h_store<0, f / 2>(c, ad, hb[f >> 1]);
            if constexpr (f == 18) gate = gate_ld<true>(c, ad, 0);
            if constexpr (f == 19) xres[0] = xres_ld<0, M>(c, ad);
            if constexpr (f == 23) {
                SB();
                asm volatile("s_waitcnt vmcnt(%1)" : "+v"(gate) : "i"(M == 0 ? 4 : 0) : "memory");
                gate_wait<M>(c, ad, 0, gate);
            }
            if constexpr (f >= 24) hb[f - 20] = h_next<f - 20>(c, ad);
            if constexpr (f >= 26) csq[f - 26] = epi_cs(c, ad, f - 26);
        };
        const u32 ra = L_W + w * 24576 + 1024 + ad.l16;
        v4u rm;
        g1_main<true, 1, PS0 + 7, 0x9AAAAAAu>(c, ad, W1, gb.acc, [&](auto FC) {
            constexpr int f = decltype(FC)::value;
            stage(std::integral_constant<int, 28 + f>{}, std::integral_constant<int, (f < 8 ? f / 2 : 4)>{});
            if constexpr (f >= 16 && f < 28 && (f & 1) == 0) { LV(rm); *(LAS v4u*)(c.L + ra + ((8 + (f - 16) / 2) % 12) * 2048) = rm ^ SGN; }
            if constexpr (f >= 14 && f < 26 && (f & 1) == 0) { WAIT_VM(f < 24 ? 14 : 12); rm = *(LAS v4u*)(c.L + ra + ((8 + (f - 14) / 2) % 12) * 2048); }
            stats(M0{}, M1{}, FC, gb);
            side_b(M0{}, FC);
        });

        {
            WAIT_VM(4);
            v4u t[6];
#pragma unroll
            for (int i = 0; i < 6; ++i) t[i] = *(LAS v4u*)(c.L + ra + ((14 + i) % 12) * 2048);
#pragma unroll
            for (int i = 0; i < 6; ++i) *(LAS v4u*)(c.L + ra + ((14 + i) % 12) * 2048) = t[i] ^ SGN;
            SB();
        }
        pin_a<0, 25>(W1);

        g2_pair<0>(c, ad, W2, hb, gb, csq, ga.r, xres, c.rws);
        pin_a<0, 25>(W1);

        v4u t[7];
        st.ad = ad.l16 + w * 7168;

        bar();

        g1_main<false, 0, PS1, 0x9AAAAAAu>(c, ad, W1, ga.acc, [&](auto FC) {
            constexpr int f = decltype(FC)::value;
            stats(M1{}, M0{}, FC, ga);
            if constexpr (f >= 12 && f < 19) t[f - 12] = x3_ld1<1, f - 12>(c, ad);
            if constexpr (f == 24) x3_wait<1>(c, ad, t);
            if constexpr (f >= 25) { x3_put1<1, 2 * (f - 25)>(c, ad, t[2 * (f - 25)]); x3_put1<1, 2 * (f - 25) + 1>(c, ad, t[2 * (f - 25) + 1]); }
            if constexpr (f == 27) x3_put1<1, 6>(c, ad, t[6]);
            if constexpr (f >= 26) csq[f - 26] = epi_cs(c, ad, f - 26);
        });

        bar();

        g1_main<false, 1, PS1 + 7, 0x9AAAAAAu>(c, ad, W1, gb.acc, [&](auto FC) {
            stats(M1{}, M1{}, FC, gb);
            side_b(M1{}, FC);
        });
        (void)E2;

        const __attribute__((address_space(4))) char* ka = (const __attribute__((address_space(4))) char*)__builtin_amdgcn_kernarg_segment_ptr();
        asm volatile("" : "+s"(ka));
        g2_pair<1>(c, ad, W2, hb, gb, csq, ga.r, xres, mkrs(*(void* const __attribute__((address_space(4)))*)(ka + 72), TSZ));

    }
    asm volatile(".Lfk_end:");
}

extern "C" void kernel_launch(void* const* d_in, const int* in_sizes, int n_in,
                              void* d_out, int out_size, void* d_ws, size_t ws_size,
                              hipStream_t stream) {
    (void)hipFuncSetAttribute((const void*)fused_kernel, hipFuncAttributeMaxDynamicSharedMemorySize, L_TOT);
    fused_kernel<<<256, 256, L_TOT, stream>>>((const u16*)d_in[0], (const u16*)d_in[1], (const u16*)d_in[2], (const u16*)d_in[3], (const u16*)d_in[4],
                                              (const u16*)d_in[5], (const u16*)d_in[7], (const u16*)d_in[8], (unsigned char*)d_ws, (u16*)d_out);
}
```

```cpp
#include <hip/hip_runtime.h>
#include <stdint.h>
#include <type_traits>

typedef unsigned short u16;
typedef unsigned u32;
typedef u32 v4u __attribute__((ext_vector_type(4)));
typedef u32 v2u __attribute__((ext_vector_type(2)));
typedef float f32x2 __attribute__((ext_vector_type(2)));
typedef float f32x4 __attribute__((ext_vector_type(4)));
typedef float f32x16 __attribute__((ext_vector_type(16)));
typedef __bf16 bf16x8 __attribute__((ext_vector_type(8)));
typedef __bf16 bf16x2 __attribute__((ext_vector_type(2)));
typedef short s16x4 __attribute__((ext_vector_type(4)));
typedef short s16x2 __attribute__((ext_vector_type(2)));
typedef unsigned short u16x2 __attribute__((ext_vector_type(2)));
#define LAS __attribute__((address_space(3)))
#define GAS __attribute__((address_space(1)))
#define DI __device__ __forceinline__
#define RLX_AGENT __ATOMIC_RELAXED, __HIP_MEMORY_SCOPE_AGENT
#define SB() __builtin_amdgcn_sched_barrier(0)
#define NOUNROLL _Pragma("unroll 1")
#define MEMBAR() asm volatile("" ::: "memory")
template <class T> __device__ __forceinline__ void LV(T& x) { asm volatile("" : "+v"(x)); }
template <class T> __device__ __forceinline__ void LA(T& x) { asm volatile("" : "+a"(x)); }
#define ANY(x) (__builtin_amdgcn_ballot_w64(x) != 0ull)

constexpr int S = 2048, D = 448, F = 1792;
constexpr float EPS = 1e-5f;
constexpr u32 TSZ = S * D * 2;
constexpr u32 OFF_EP  = 0;
constexpr u32 OFF_FL  = 4096;
constexpr u32 OFF_ST0 = OFF_FL + 16384;
constexpr u32 OFF_CSG = OFF_ST0 + 16384;
constexpr u32 OFF_ST  = OFF_CSG + 16384;
constexpr u32 OFF_KVG = 1048576;
constexpr u32 OFF_X0G = OFF_KVG + 2 * TSZ;
constexpr u32 OFF_X1  = OFF_X0G + 2 * TSZ;
constexpr u32 OFF_YT  = OFF_X1 + TSZ;
constexpr u32 OFF_X3  = OFF_YT + TSZ;
constexpr u32 OFF_H   = OFF_X3 + TSZ;
constexpr u32 WS_BYTES = OFF_H + 64 * 112 * 1024;
static_assert(OFF_ST + 64 * 24 * 512 <= OFF_KVG, "ws map");
#define STAMP(i) do {} while (0)
#define STAMP2(i) do {} while (0)
#define STAMP4(i) do {} while (0)
#define STAMP3(i) do {} while (0)
constexpr int RSRC_W3 = 0x00020000;
constexpr int AUX_SC1 = 16;
constexpr unsigned SPIN_CAP = 1u << 14;
constexpr u32 SGN = 0x80008000u;

template <int I, int N, class Fn> DI void sfor(Fn&& fn) { if constexpr (I < N) { fn(std::integral_constant<int, I>{}); sfor<I + 1, N>(fn); } }

DI float bflo(u32 x) { return __uint_as_float(x << 16); }
DI float bfhi(u32 x) { return __uint_as_float(x & 0xffff0000u); }
DI u32 pk(float a, float b) { f32x2 v = {a, b}; return __builtin_bit_cast(u32, __builtin_convertvector(v, bf16x2)); }
DI float rbf(float a) { return bflo(pk(a, a)); }
DI __amdgpu_buffer_rsrc_t mkrs(const void* p, u32 bytes) { return __builtin_amdgcn_make_buffer_rsrc((void*)p, 0, (int)bytes, RSRC_W3); }
DI f32x4 mfma16(v4u a, v4u b, f32x4 c) { return __builtin_amdgcn_mfma_f32_16x16x32_bf16(__builtin_bit_cast(bf16x8, a), __builtin_bit_cast(bf16x8, b), c, 0, 0, 0); }
DI f32x16 mfma32(v4u a, v4u b, f32x16 c) { return __builtin_amdgcn_mfma_f32_32x32x16_bf16(__builtin_bit_cast(bf16x8, a), __builtin_bit_cast(bf16x8, b), c, 0, 0, 0); }
DI f32x16 zero16() { f32x16 z; for (int i = 0; i < 16; ++i) z[i] = 0.f; return z; }
DI v2u trd(LAS char* p) { return __builtin_bit_cast(v2u, __builtin_amdgcn_ds_read_tr16_b64_v4i16((LAS s16x4*)p)); }
DI v4u trd2(LAS char* p, int second) { v2u a = trd(p), b = trd(p + second); return v4u{a.x, a.y, b.x, b.y}; }
DI float dot2(u32 a, u32 b, float c) { return __builtin_amdgcn_fdot2_f32_bf16(__builtin_bit_cast(bf16x2, a), __builtin_bit_cast(bf16x2, b), c, false); }
DI v4u ld128(__amdgpu_buffer_rsrc_t rs, u32 vo, u32 so) { return __builtin_amdgcn_raw_buffer_load_b128(rs, vo, so, AUX_SC1); }
template <bool SC1, int OFF = 0> DI v4u ld128_asm(__amdgpu_buffer_rsrc_t rs, u32 vo, u32 so) {
    v4u r;
    if constexpr (SC1) asm volatile("buffer_load_dwordx4 %0, %1, %2, %3 offen offset:%4 sc1" : "=v"(r) : "v"(vo), "s"(rs), "s"(so), "i"(OFF) : "memory");
    else asm volatile("buffer_load_dwordx4 %0, %1, %2, %3 offen offset:%4" : "=v"(r) : "v"(vo), "s"(rs), "s"(so), "i"(OFF) : "memory");
    return r;
}
template <int OFF = 0> DI void st128(v4u v, __amdgpu_buffer_rsrc_t rs, u32 vo, u32 so) {
    asm volatile("buffer_store_dwordx4 %0, %1, %2, %3 offen offset:%4 sc1\n\ts_nop 1" :: "v"(v), "v"(vo), "s"(rs), "s"(so), "i"(OFF) : "memory");
}
DI int lane_id() { int l; asm volatile("v_mbcnt_lo_u32_b32 %0, -1, 0\n\tv_mbcnt_hi_u32_b32 %0, -1, %0" : "=v"(l)); return l; }
DI float hsum(float a) {
    const u32 v = __float_as_uint(a);
    auto t = __builtin_amdgcn_permlane32_swap(v, v, false, false);
    return __uint_as_float(t[0]) + __uint_as_float(t[1]);
}
DI float ln_sd(float var) { return rbf(__builtin_amdgcn_sqrtf(rbf(rbf(var) + EPS))); }
DI void ln_scalars(float s1, float s2, float& m, float& r) {
    m = s1 * (1.0f / D);
    r = __builtin_amdgcn_rsqf(fmaxf(__builtin_fmaf(-m, m, s2 * (1.0f / D)), 0.f) + EPS);
}
DI u32 xor_or(u32 a, u32 b, u32 c) { return __builtin_amdgcn_bitop3_b32(a, b, c, 0xBE); }
DI u32 tag_bad(const v4u& g, u32 tA, u32 tB, u32 acc) { return xor_or(g.w, tB, xor_or(g.y, tA, acc)); }
template <bool SC1> DI void dma16(u32 ldsaddr, u32 voff, __amdgpu_buffer_rsrc_t rs, u32 soff) {
    if constexpr (SC1) asm volatile("s_mov_b32 m0, %0\n\ts_nop 0\n\tbuffer_load_dwordx4 %1, %2, %3 offen sc1 lds" :: "s"(ldsaddr), "v"(voff), "s"(rs), "s"(soff) : "memory", "m0");
    else asm volatile("s_mov_b32 m0, %0\n\ts_nop 0\n\tbuffer_load_dwordx4 %1, %2, %3 offen lds" :: "s"(ldsaddr), "v"(voff), "s"(rs), "s"(soff) : "memory", "m0");
}
template <int LI> DI void dma_pair(u32 ldsbase, u32 vo0, u32 vo1, __amdgpu_buffer_rsrc_t rs, u32 soff) {
    asm volatile("s_add_u32 m0, %0, %5\n\ts_nop 0\n\tbuffer_load_dwordx4 %1, %3, %4 offen lds\n\tbuffer_load_dwordx4 %2, %3, %4 offen offset:1024 lds"
                 :: "s"(ldsbase), "v"(vo0), "v"(vo1), "s"(rs), "s"(soff), "i"(LI) : "memory", "m0", "scc");
}
DI void touch(u32 junk, u32 voff, __amdgpu_buffer_rsrc_t rs, u32 soff) {
    asm volatile("s_mov_b32 m0, %0\n\ts_nop 0\n\tbuffer_load_dword %1, %2, %3 offen lds" :: "s"(junk), "v"(voff), "s"(rs), "s"(soff) : "memory", "m0");
}
#define WAIT_VM(n) asm volatile("s_waitcnt vmcnt(%0)" :: "i"(n) : "memory")
#define WAIT_LGKM(n) asm volatile("s_waitcnt lgkmcnt(%0)" :: "i"(n) : "memory")
DI void bar() { asm volatile("s_waitcnt lgkmcnt(0)\n\ts_barrier" ::: "memory"); }
DI int wrap7(int x) { return x >= 7 ? x - 7 : x; }
template <int N, int K> DI void ilv() { if constexpr (N > 0) { __builtin_amdgcn_sched_group_barrier(0x8, 1, 0); __builtin_amdgcn_sched_group_barrier(0x366, K, 0); ilv<N - 1, K>(); } }

constexpr int L_X = 0;
constexpr int L_W = 57344;
constexpr int L_M = L_W + 98304;
constexpr int L_SP = L_M;
constexpr int L_CS = L_M + 2048;
constexpr int L_S2 = L_M + 3072;
constexpr int L_JK = L_M + 4096;
constexpr int L_TOT = L_M + 8192;
constexpr int T_K = 0, T_V = 9216, T_X = 17408, T_S = L_JK;
constexpr int CV_B = 25600;
constexpr int CV_HE = 0, CV_HO = 4160, CV_KV = CV_HO + 4160, CV_CH = CV_KV + 4608;
constexpr int CV_ST = 16384;
static_assert(CV_B + 2 * CV_CH <= L_W, "lds map");

struct Ctx {
    LAS char* L;
    __amdgpu_buffer_rsrc_t rws, rw1, rw2;
    GAS u32* tmo;
    int w, j, grp, kb0, gi;
    u32 lbase, tagA, tagB;
};
DI Ctx fresh(const Ctx& c0) { Ctx c = c0; asm volatile("" : "+s"(c.w), "+s"(c.j), "+s"(c.grp), "+s"(c.kb0), "+s"(c.gi)); return c; }
DI bool spin_fail(const Ctx& c, unsigned& spins, u32 code) {
    if (++spins > SPIN_CAP) { __hip_atomic_store(c.tmo, code, RLX_AGENT); return true; }
    __builtin_amdgcn_s_sleep(1);
    return false;
}
DI u32 xtr_of(int lane) { return (u32)(((lane >> 5) * 4 + ((lane >> 2) & 3)) * 64 + ((lane >> 4) & 1) * 32 + (lane & 3) * 8); }
DI u32 wl1_of(const Ctx& c, int lane) {
    const int rr = (lane >> 5) * 4 + ((lane >> 2) & 3);
    return (u32)(L_W + c.w * 24576 + rr * 128 + ((rr >> 1) & 1) * 64 + ((lane >> 4) & 1) * 32 + (lane & 3) * 8);
}
DI u32 wl2_of(const Ctx& c, int lane) { return wl1_of(c, lane) ^ (u32)((c.w >> 1) * 64 + (c.w & 1) * 32); }
DI int member(const Ctx& c, int m) {
    if (m == 0) return c.j;
    int t = m - 1 + 2 * c.gi; t = t >= 6 ? t - 6 : t; t = t >= 6 ? t - 6 : t;
    return wrap7(c.j + 1 + t);
}
typedef v4u W1T[28][2];
typedef v4u W2T[16][2];
typedef f32x16 AccT[2];

DI constexpr int k_mem(int f) { return f < 4 ? 0 : 1 + (f - 4) % 6; }
DI constexpr int k_sub(int f) { return f < 4 ? f : (f - 4) / 6; }
DI constexpr int w2_step_of(int n) { return n < 40 ? 2 * (n - 28) : (n < 44 ? n - 16 : 2 * (n - 44) + 1); }
template <int N> DI u32 w_soff(const Ctx& c) {
    if constexpr (N < 28) return (N % 7 + 7 * ((N / 7 + c.kb0) & 3)) * 16 * (F * 2) + (256 * c.j + 64 * c.w) * 2;
    else return (256 * member(c, k_mem(w2_step_of(N))) + 64 * c.w + 16 * k_sub(w2_step_of(N))) * (D * 2) + 128 * c.j;
}
template <int N> DI u32 w_voff(int lane) { return (lane >> 3) * ((N < 28 ? F : D) * 2) + ((lane & 7) ^ (((lane >> 4) & 1) << 2)) * 16; }
struct WSt { u32 ring, vo0, vo1; };
template <int N> DI WSt w_st(const Ctx& c, int lane) { WSt t; t.ring = c.lbase + L_W + c.w * 24576; t.vo0 = w_voff<N>(lane); t.vo1 = t.vo0 + 8 * ((N < 28 ? F : D) * 2) - 1024; return t; }
template <int N> DI void w_dma(const Ctx& c, const WSt& t, u32 so) { dma_pair<(N % 12) * 2048>(t.ring, t.vo0, t.vo1, N < 28 ? c.rw1 : c.rw2, so); }
DI v4u w_frag(const Ctx& c, u32 wl, int slot, int t) { return trd2(c.L + (wl ^ (u32)(t * 64)) + slot * 2048, 1024); }
template <int N> DI void w_read1(const Ctx& c, u32 wl, W1T& W1, W2T& W2) {
#pragma unroll
    for (int t = 0; t < 2; ++t) {
        if constexpr (N < 28) W1[N][t] = w_frag(c, wl, N % 12, t);
        else W2[N - 28][t] = w_frag(c, wl, N % 12, t);
    }
}
template <int N> DI void w_neg(W2T& W2) {
#pragma unroll
    for (int t = 0; t < 2; ++t) { LV(W2[N - 28][t]); W2[N - 28][t].z ^= SGN; W2[N - 28][t].w ^= SGN; asm volatile("" : "+v"(W2[N - 28][t])); }
}
template <int N0, int N1> DI void w_read(const Ctx& c0, W1T& W1, W2T& W2) {
    const Ctx c = fresh(c0);
    const u32 wl = wl1_of(c, lane_id());
    sfor<N0, N1>([&](auto NC) { w_read1<decltype(NC)::value>(c, wl, W1, W2); });
}

constexpr int PS0 = 0, PS1 = 33;
template <int N0, int N1> DI void pin_a(W1T& W1) {
    if constexpr (N0 < N1) { asm volatile("" : "+a"(W1[N0][0]), "+a"(W1[N0][1])); pin_a<N0 + 1, N1>(W1); }
}
struct G1Out { AccT acc; float mb, r; };
DI constexpr bool reg_start(unsigned long long end, int f) { return f == 0 || ((end >> (f - 1)) & 1ull); }
DI constexpr int reg_len(unsigned long long end, int f) { int n = 1; while (!reg_start(end, f - n + 1)) ++n; return n; }
struct Ad {
    u32 l16;
    u32 ca;
    u32 hst;
    u32 x3o;
    u32 sto;
    u32 so[7];
};
constexpr u32 ST_HG = 3 * 4096;
DI u32 tok32(const Ad& a) { return a.l16 & 496u; }

template <bool TR, int HALF, int S0, unsigned END, class Side>
DI void g1_main(const Ctx& c, Ad& a, const W1T& W1, AccT& acc, Side&& side) {
    const u32 lp = TR ? xtr_of(a.l16 >> 4) : a.l16;
    int kb = c.kb0; asm volatile("" : "+s"(kb));
    u32 la = 0;
    auto ldb = [&](int f) {
        if (f % 7 == 0) la = lp + ((f / 7 + kb) & 3) * 7168;
        LAS char* p = c.L + la + (L_X + HALF * 28672 + (f % 7) * 1024);
        if constexpr (TR) return trd2(p, 512);
        else return *(LAS v4u*)p;
    };
    v4u B[28];
    B[0] = ldb(0); B[1] = ldb(1); B[2] = ldb(2);
    SB();
    sfor<0, 28>([&](auto FC) {
        constexpr int f = decltype(FC)::value;
        if constexpr (f % 4 == 0) STAMP2(S0 + f / 4);
        if constexpr (reg_start(END, f)) asm volatile("" : "+v"(a.l16), "+v"(a.ca));
        acc[0] = mfma32(W1[f][0], B[f], f == 0 ? zero16() : acc[0]);
        acc[1] = mfma32(W1[f][1], B[f], f == 0 ? zero16() : acc[1]);
        if constexpr (f + 3 < 28) B[f + 3] = ldb(f + 3);
        side(FC);
        if constexpr ((END >> f) & 1u) { ilv<2 * reg_len(END, f), 5>(); SB(); }
    });
}
struct St1 { u32 ad; v4u fr[2]; float s1, s2; };
template <bool TR, int HALF, int K> DI void st1_read(const Ctx& c, St1& s) {
    if constexpr (TR) s.fr[K & 1] = trd2(c.L + s.ad + (L_X + HALF * 28672 + K * 1024), 512);
    else s.fr[K & 1] = *(LAS v4u*)(c.L + s.ad + (L_X + HALF * 28672 + K * 1024));
}
template <int K, int E0, int E1> DI void st1_acc(St1& s) {
#pragma unroll
    for (int e = E0; e < E1; ++e) { if (e == E0) LV(s.fr[K & 1]); const u32 x = s.fr[K & 1][e >> 1]; if (e & 1) s.s2 = dot2(x, x, s.s2); else s.s1 = dot2(x, 0x3f803f80u, s.s1); }
}
DI void part_put(const Ctx& c, const Ad& a, int off, float s1, float s2) { *(LAS f32x2*)(c.L + off + tok32(a) * 2 + c.w * 8) = f32x2{hsum(s1), hsum(s2)}; }
DI f32x2 part_get(const Ctx& c, const Ad& a, int off) {
    const f32x4 u = *(LAS f32x4*)(c.L + off + tok32(a) * 2), v = *(LAS f32x4*)(c.L + off + tok32(a) * 2 + 16);
    return f32x2{(u[0] + u[2]) + (v[0] + v[2]), (u[1] + u[3]) + (v[1] + v[3])};
}
DI void frag_sums(const v4u& p, float& s1, float& s2) {
    s1 = 0.f; s2 = 0.f;
#pragma unroll
    for (int e = 0; e < 4; ++e) { s1 = dot2(p[e], 0x3f803f80u, s1); s2 = dot2(p[e], p[e], s2); }
}
template <int HALF, int KIND> DI void stat_pub(const Ctx& c, const Ad& a, int off, int who) {
    if (c.w == who) {
        f32x2 t = part_get(c, a, off);
        const bool z = c.j == 6 && a.l16 >= 512u;
        st128(v4u{z ? 0u : __float_as_uint(t[0]), c.tagA, z ? 0u : __float_as_uint(t[1]), c.tagB}, c.rws, z ? a.l16 : tok32(a), a.sto + HALF * ST_HG + KIND * 4096 + c.j * 512);
    }
}
template <bool DO> DI void stat_pub_if(const Ctx& c, const Ad& a) { if constexpr (DO) stat_pub<0, 0>(c, a, L_S2, 0); }
struct Gath { v4u g[4]; };
template <int HALF, int KIND, int I> DI void gath_ld(const Ctx& c, const Ad& a, Gath& g) {
    g.g[I] = ld128(c.rws, a.l16 + I * 1024, a.sto + HALF * ST_HG + KIND * 4096);
}
DI bool gath_bad(const Ctx& c, const Gath& g) {
    u32 d = 0;
#pragma unroll
    for (int i = 0; i < 4; ++i) d = tag_bad(g.g[i], c.tagA, c.tagB, d);
    return ANY(d != 0u);
}
template <int HALF, int KIND> DI void gath_wait(const Ctx& c, const Ad& a, Gath& g) {
    if (gath_bad(c, g)) {
        unsigned spins = 0;
        NOUNROLL do { MEMBAR(); sfor<0, 4>([&](auto IC) { gath_ld<HALF, KIND, decltype(IC)::value>(c, a, g); }); } while (gath_bad(c, g) && !spin_fail(c, spins, 2u));
    }
}
DI void gath_ln(const Gath& g, float& m, float& r) {
    const float t1 = (__uint_as_float(g.g[0].x) + __uint_as_float(g.g[1].x)) + (__uint_as_float(g.g[2].x) + __uint_as_float(g.g[3].x));
    const float t2 = (__uint_as_float(g.g[0].z) + __uint_as_float(g.g[1].z)) + (__uint_as_float(g.g[2].z) + __uint_as_float(g.g[3].z));
    ln_scalars(hsum(t1), hsum(t2), m, r);
}
DI f32x4 epi_cs(const Ctx& c, const Ad& a, int E) { return *(LAS f32x4*)(c.L + a.ca + E * 32); }
template <int HALF, int MLP, int E>
DI void g1_epi(const Ctx& c, const Ad& a, G1Out& o, f32x4& cs, v4u& fr) {
    constexpr int t = E >> 2, r0 = 4 * (E & 3), g = E & 1;
    constexpr bool neg = (g ^ MLP) != 0;
    float h[4];
    LV(cs); LA(o.acc[t]);
#pragma unroll
    for (int e = 0; e < 4; ++e) h[e] = __builtin_fmaf(-o.mb, cs[e], o.acc[t][r0 + e]);
#pragma unroll
    for (int e = 0; e < 2; ++e) {
        const u32 p = __builtin_bit_cast(u32, __builtin_elementwise_max(__builtin_bit_cast(s16x2, pk(h[2 * e], h[2 * e + 1])), s16x2{0, 0}));
        fr[2 * g + e] = neg ? (p | SGN) : p;
    }
    if (g == 0) LV(fr);
}
template <int HALF, int K> DI void h_store(const Ctx& c, const Ad& a, const v4u& fr) { st128<K * 1024>(fr, c.rws, a.l16, a.hst + HALF * 114688); }

#define NW0_ 0
#define NT0_ 0
constexpr int NT0 = NT0_;
#define NW1_ 2
constexpr int NW1 = NW1_;
#define KP_ 4
constexpr int KP = KP_;
#define TLBW_ 1
constexpr int TLBW = TLBW_;
#define SLP0 0
#define HT1 0
#define HT2 13
#define CPH 2
#define SLP1 16
constexpr int NW0 = NW0_;
#define EPE 8
constexpr int HR = 10, GL = 14, GC = 20;
template <int MLP> DI bool h_bad(const v4u& a, const v4u& b) {
    const u32 lo = MLP == 0 ? (a.x | a.y | b.x | b.y) : ~(a.x & a.y & b.x & b.y);
    const u32 hi = MLP == 0 ? ~(a.z & a.w & b.z & b.w) : (a.z | a.w | b.z | b.w);
    return ((lo | hi) & SGN) != 0u;
}
DI u32 h_base(const Ctx& c, int half) { return OFF_H + ((c.grp * 2 + half) * 112 + 4 * c.w) * 1024; }
template <bool ASM> DI v2u gate_ld(const Ctx& c, const Ad& a, int half) {
    const u32 vo = min(a.l16, 96u) * 1024, so = a.hst - c.j * 16384 + half * 114688 + 4;
    if constexpr (ASM) { v2u r; asm volatile("buffer_load_dwordx2 %0, %1, %2, %3 offen sc1" : "=v"(r) : "v"(vo), "s"(c.rws), "s"(so) : "memory"); return r; }
    else return __builtin_amdgcn_raw_buffer_load_b64(c.rws, vo, so, AUX_SC1);
}
template <int MLP> DI bool gate_bad(v2u x) { return ((MLP == 0 ? (x.x | ~x.y) : (~x.x | x.y)) & SGN) != 0u; }
template <int MLP> DI void gate_wait(const Ctx& c, const Ad& a, int half, v2u x) {
    if (ANY(gate_bad<MLP>(x))) {
        unsigned spins = 0;
        NOUNROLL do { MEMBAR(); x = gate_ld<false>(c, a, half); } while (ANY(gate_bad<MLP>(x)) && !spin_fail(c, spins, 1u));
    }
}
template <int N> DI v4u h_next(const Ctx& c, Ad& a) {
    constexpr int f = N >= 28 ? N - 28 : N;
    if constexpr (N >= 32 && N < 38) a.so[k_mem(f)] += 114688u;
    v4u r = __builtin_amdgcn_raw_buffer_load_b128(c.rws, a.l16 + k_sub(f) * 1024, a.so[k_mem(f)], AUX_SC1);
    if constexpr (N >= 50) a.so[k_mem(f)] -= 114688u;
    return r;
}
template <int MLP, int F_> DI void h_check(const Ctx& c0, v4u& a, v4u& b) {
    if (__builtin_expect(ANY(h_bad<MLP>(a, b)), 0)) {
        const Ctx c = fresh(c0);
        constexpr int half = F_ >= 28, f = F_ - 28 * half;
        const u32 vo = lane_id() * 16 + k_sub(f) * 1024, s0 = h_base(c, half) + member(c, k_mem(f)) * 16384, s1 = h_base(c, half) + member(c, k_mem(f + 1)) * 16384;
        unsigned spins = 0;
        NOUNROLL do {
            MEMBAR();
            a = ld128(c.rws, vo, s0); b = ld128(c.rws, vo, s1);
        } while (ANY(h_bad<MLP>(a, b)) && !spin_fail(c, spins, 3u));
    }
}
template <int HALF, int P> DI void red_write(const Ctx& c, const Ad& a, const AccT& acc) {
    LAS char* dst = c.L + (a.l16 + (P ^ c.w) * 6144) + (L_X + (1 - HALF) * 28672 + (P - 1) * 2048);
    const f32x16& x = acc[P >> 1];
    constexpr int r0 = (P & 1) * 8;
    *(LAS f32x4*)dst = f32x4{x[r0], x[r0 + 1], x[r0 + 2], x[r0 + 3]};
    *(LAS f32x4*)(dst + 1024) = f32x4{x[r0 + 4], x[r0 + 5], x[r0 + 6], x[r0 + 7]};
}
template <int HALF> DI void red_read(const Ctx& c, const Ad& a, f32x4 (&t)[6]) {
    LAS char* src = c.L + (a.l16 + c.w * 6144) + (L_X + (1 - HALF) * 28672);
#pragma unroll
    for (int i = 0; i < 6; ++i) t[i] = *(LAS f32x4*)(src + i * 1024);
}
template <int K> DI void post_half(const f32x4& o, float r, v4u& xres) {
#pragma unroll
    for (int e = 0; e < 2; ++e) xres[2 * K + e] = pk(__builtin_fmaf(o[2 * e], r, bflo(xres[2 * K + e])), __builtin_fmaf(o[2 * e + 1], r, bfhi(xres[2 * K + e])));
}
template <int HALF> DI void out_store(const Ctx& c, const Ad& a, const v4u& x, __amdgpu_buffer_rsrc_t rout) {
    auto r0 = __builtin_amdgcn_permlane32_swap(x.x, x.z, false, false);
    auto r1 = __builtin_amdgcn_permlane32_swap(x.y, x.w, false, false);
    const v4u v = {r0[0], r1[0], r0[1], r1[1]};
    const u32 vo = tok32(a) * (D / 8) + (a.l16 >> 9) * 16, so = (u32)((c.grp * 64 + HALF * 32) * D + 64 * c.j + 16 * c.w) * 2;
    asm volatile("buffer_store_dwordx4 %0, %1, %2, %3 offen\n\ts_nop 1" :: "v"(v), "v"(vo), "s"(rout), "s"(so) : "memory");
}
template <int HALF, int MLP> DI v4u xres_ld(const Ctx& c, const Ad& a) {
    if constexpr (MLP == 0) return trd2(c.L + (xtr_of(a.l16 >> 4) + (4 * c.j + c.w) * 1024) + (L_X + HALF * 28672), 512);
    else return *(LAS v4u*)(c.L + (a.l16 + (4 * c.j + c.w) * 1024) + (L_X + HALF * 28672));
}
template <int HALF> DI v4u y_ld(const Ctx& c, const Ad& a) { return trd2(c.L + (xtr_of(a.l16 >> 4) + c.w * 1024) + (L_X + HALF * 28672 + 24576), 512); }
DI void x3_make(v4u& x, const v4u& yf, float m, float r) {
    const float n = -m * r;
#pragma unroll
    for (int e = 0; e < 4; ++e) {
        const float p = bflo(x[e]), q = bfhi(x[e]);
        x[e] = pk(__builtin_fmaf(__builtin_fmaf(p, r, n), bflo(yf[e]), p), __builtin_fmaf(__builtin_fmaf(q, r, n), bfhi(yf[e]), q));
    }
}
template <int HALF> DI void x3_store(const Ctx& c, const Ad& a, const v4u& x) { st128(x, c.rws, a.l16, a.x3o + HALF * 28672); }
template <int HALF, int I> DI v4u x3_ld1(const Ctx& c, const Ad& a) {
    return ld128(c.rws, a.l16 + (I & 3) * 1024, a.x3o + (6 * c.w - 4 * c.j + (I & 4)) * 1024 + HALF * 28672);
}
DI u32 max3u(u32 a, u32 b, u32 c) { u32 r; asm("v_max3_u32 %0, %1, %2, %3" : "=v"(r) : "v"(a), "v"(b), "v"(c)); return r; }
DI bool x3_bad(const v4u (&t)[7]) {
    u32 mx = 0;
#pragma unroll
    for (int i = 0; i < 7; ++i) mx = max3u(max3u(mx, t[i].x, t[i].y), t[i].z, t[i].w);
    return ANY(mx == ~0u);
}
template <bool EQ> DI void gate_spin(const Ctx& c, u32 vo, u32 so, u32 word, u32 code) {
    unsigned spins = 0;
    NOUNROLL for (;;) {
        MEMBAR();
        const u32 x = __builtin_amdgcn_raw_buffer_load_b32(c.rws, vo, so, AUX_SC1);
        if (!ANY(EQ ? x == word : x != word)) break;
        if (spin_fail(c, spins, code)) break;
    }
}
template <int HALF> DI void x3_wait(const Ctx& c, const Ad& a, v4u (&t)[7]) {
    if (x3_bad(t)) {
        unsigned spins = 0;
        NOUNROLL do { MEMBAR(); sfor<0, 7>([&](auto IC) { t[decltype(IC)::value] = x3_ld1<HALF, decltype(IC)::value>(c, a); }); } while (x3_bad(t) && !spin_fail(c, spins, 4u));
    }
}
template <int HALF, int I> DI void x3_put1(const Ctx& c, const Ad& a, const v4u& t) { *(LAS v4u*)(c.L + (a.l16 + c.w * 7168) + (L_X + HALF * 28672 + I * 1024)) = t; }

template <int MLP>
DI void g2_pair(const Ctx& c, Ad& a, const W2T& W2, v4u (&hb)[56], G1Out& gb, f32x4 (&csq)[3], float r0, v4u (&xres)[2], __amdgpu_buffer_rsrc_t out) {
    const u32 wl0 = wl2_of(c, a.l16 >> 4), wl1 = wl0 ^ 64u;
    AccT acc[2];
    v2u gate = {0, 0};
    v4u wl[2], fr, yf;
    f32x4 o[2], u[2];
    float s1, s2;
    Gath gs;
    const float ra_ = MLP ? -r0 : r0, rb_ = MLP ? -gb.r : gb.r;
    auto wres = [&](int slot, int k) { return trd2(c.L + (k ? wl1 : wl0) + slot * 2048, 1024); };
    auto fetch = [&](auto NC) {
        constexpr int N = decltype(NC)::value;
        if constexpr (N < 56) {
            if constexpr (N >= 28 && N < 32) hb[N] = *(LAS v4u*)(c.L + (a.l16 + c.w * 4096) + (L_X + (N - 28) * 1024));
            else hb[N] = h_next<N>(c, a);
        }
    };
    auto rd = [&](int i) { return *(LAS f32x4*)(c.L + (a.l16 + c.w * 6144) + (L_X + 28672 + i * 1024)); };
    auto own = [&](int k) { return f32x4{acc[0][0][4 * k], acc[0][0][4 * k + 1], acc[0][0][4 * k + 2], acc[0][0][4 * k + 3]}; };
    wl[0] = wres(8, 0); wl[1] = wres(8, 1);
    SB();
    sfor<0, 28>([&](auto PC) {
        constexpr int F_ = 2 * decltype(PC)::value, H = F_ >= 28, f = F_ - 28 * H;
        constexpr bool res = f < 24;
        constexpr int r0_ = f < 24 ? f >> 1 : f - 12;
        if constexpr (F_ == 24 || F_ == 30 || (MLP == 0 && F_ == 36)) { bar(); SB(); stat_pub_if<MLP == 0 && F_ == 36>(c, a); }
        if constexpr (F_ % 4 == 0) STAMP2((MLP ? PS1 : PS0) + 15 + F_ / 4);
        asm volatile("" : "+v"(a.l16), "+v"(a.ca));
        acc[H][0] = mfma32(W2[r0_][0], hb[F_], f == 0 ? zero16() : acc[H][0]);
        acc[H][1] = mfma32(W2[r0_][1], hb[F_], f == 0 ? zero16() : acc[H][1]);
        fetch(std::integral_constant<int, F_ + (F_ < 8 ? HR - 2 : HR)>{});
        fetch(std::integral_constant<int, F_ + 1 + (F_ < 8 ? HR - 2 : HR)>{});
        if constexpr (F_ == 6) { fetch(std::integral_constant<int, F_ + HR>{}); fetch(std::integral_constant<int, F_ + HR + 1>{}); }
        if constexpr (F_ < 6) csq[(F_ + 2) % 3] = epi_cs(c, a, F_ + 2);
        if constexpr (F_ < 8) g1_epi<1, MLP, F_>(c, a, gb, csq[F_ % 3], fr);
        if constexpr (F_ < 6) csq[F_ % 3] = epi_cs(c, a, F_ + 3);
        if constexpr (F_ < 8) g1_epi<1, MLP, F_ + 1>(c, a, gb, csq[(F_ + 1) % 3], fr);
        if constexpr (F_ == 32) { LA(acc[0][0]); LV(o[0]); LV(u[0]); LV(u[1]); o[0] = (o[0] + own(0)) + (u[0] + u[1]); post_half<0>(o[0], ra_, xres[0]); LV(xres[0]); }
        if constexpr (F_ == 34) { LA(acc[0][0]); LV(o[0]); LV(u[0]); LV(u[1]); o[0] = (o[0] + own(1)) + (u[0] + u[1]); post_half<1>(o[0], ra_, xres[0]); LV(xres[0]); }
        acc[H][0] = mfma32(res ? wl[0] : W2[r0_ + 1][0], hb[F_ + 1], acc[H][0]);
        acc[H][1] = mfma32((res || f == 26) ? wl[1] : W2[r0_ + 1][1], hb[F_ + 1], acc[H][1]);
        if constexpr (f < 22) { wl[0] = wres((9 + (f >> 1)) % 12, 0); wl[1] = wres((9 + (f >> 1)) % 12, 1); }
        if constexpr (f == 24) wl[1] = *(LAS v4u*)(c.L + (a.l16 + c.w * 1024) + L_JK);
        if constexpr (F_ == 26) { wl[0] = wres(8, 0); wl[1] = wres(8, 1); }
        if constexpr (F_ == 22) xres[1] = xres_ld<1, MLP>(c, a);
        if constexpr (F_ == 30) { u[0] = rd(0); u[1] = rd(2); o[0] = rd(4); }
        if constexpr (F_ == 32) { u[0] = rd(1); u[1] = rd(3); o[0] = rd(5); }
        if constexpr (MLP == 0 && F_ == 54) yf = y_ld<0>(c, a);
        if constexpr (F_ == GL) gate = gate_ld<false>(c, a, 1);
        if constexpr (F_ < 8) { *(LAS v4u*)(c.L + (a.l16 + c.w * 4096) + (L_X + (F_ / 2) * 1024)) = fr; h_store<1, F_ / 2>(c, a, fr); }
        if constexpr (MLP == 0 && (F_ == 24 || F_ == 26))
            dma16<true>(c.lbase + L_X + (F_ - 24) / 2 * 28672 + 24576 + c.w * 1024, a.l16, c.rws, OFF_YT + (c.grp * 2 + (F_ - 24) / 2) * 28672 + c.j * 4096 + c.w * 1024);
        if constexpr (F_ == 28) { red_write<0, 1>(c, a, acc[0]); red_write<0, 2>(c, a, acc[0]); red_write<0, 3>(c, a, acc[0]); }
        if constexpr (MLP == 0 && F_ == 34) { frag_sums(xres[0], s1, s2); part_put(c, a, L_S2, s1, s2); }
        if constexpr (MLP == 1 && F_ == 36) out_store<0>(c, a, xres[0], out);
        ilv<4, 5>();
        if constexpr (F_ + 2 < 56 && f + 2 != 28 && f + 2 >= 4) h_check<MLP, F_ + 2>(c, hb[F_ + 2], hb[F_ + 3]);
        if constexpr (F_ == GC) gate_wait<MLP>(c, a, 1, gate);
        SB();
    });
    static_assert(GC + 2 + HR <= 32 && (GC & 1) == 0 && (HR & 1) == 0, "half B's first foreign fragment is requested behind the gate");
    STAMP2((MLP ? PS1 : PS0) + 29);
    if constexpr (MLP == 0) STAMP4(0);
    red_write<1, 1>(c, a, acc[1]); red_write<1, 2>(c, a, acc[1]); red_write<1, 3>(c, a, acc[1]);
    if constexpr (MLP == 0) sfor<0, 4>([&](auto IC) { gath_ld<0, 0, decltype(IC)::value>(c, a, gs); });
    bar();
    if constexpr (MLP == 0) STAMP4(1);
    f32x4 t[6];
    red_read<1>(c, a, t);
    o[0] = (f32x4{acc[1][0][0], acc[1][0][1], acc[1][0][2], acc[1][0][3]} + t[0]) + (t[2] + t[4]);
    o[1] = (f32x4{acc[1][0][4], acc[1][0][5], acc[1][0][6], acc[1][0][7]} + t[1]) + (t[3] + t[5]);
    post_half<0>(o[0], rb_, xres[1]); post_half<1>(o[1], rb_, xres[1]);
    if constexpr (MLP == 0) {
        frag_sums(xres[1], s1, s2); part_put(c, a, L_S2, s1, s2);
        STAMP4(2);
        bar();
        stat_pub<1, 0>(c, a, L_S2, 1);
        STAMP4(3);
        gath_wait<0, 0>(c, a, gs);
        gath_ln(gs, s1, s2);
        x3_make(xres[0], yf, s1, s2);
        x3_store<0>(c, a, xres[0]);
        STAMP4(4);
        yf = y_ld<1>(c, a);
        __builtin_amdgcn_s_sleep(SLP1);
        v4u x[7];
        unsigned spins = 0;
        NOUNROLL do {
            MEMBAR();
            sfor<0, 4>([&](auto IC) { gath_ld<1, 0, decltype(IC)::value>(c, a, gs); });
            sfor<0, 7>([&](auto IC) { x[decltype(IC)::value] = x3_ld1<0, decltype(IC)::value>(c, a); });
        } while ((gath_bad(c, gs) || x3_bad(x)) && !spin_fail(c, spins, 4u));
        STAMP4(5);
        gath_ln(gs, s1, s2);
        x3_make(xres[1], yf, s1, s2);
        x3_store<1>(c, a, xres[1]);
        sfor<0, 7>([&](auto IC) { x3_put1<0, decltype(IC)::value>(c, a, x[decltype(IC)::value]); });
    } else out_store<1>(c, a, xres[1], out);
}

__global__ void __launch_bounds__(256, 1) fused_kernel(const u16* __restrict__ x0, const u16* __restrict__ kin, const u16* __restrict__ vin,
                                                       const u16* __restrict__ sk, const u16* __restrict__ hk, const u16* __restrict__ Dv,
                                                       const u16* __restrict__ w1, const u16* __restrict__ w2,
                                                       unsigned char* __restrict__ ws, u16* __restrict__ out) {
    extern __shared__ __attribute__((aligned(16))) char smem[];
    Ctx c;
    c.L = (LAS char*)smem;
    c.lbase = (u32)(uintptr_t)c.L;
    const int b = blockIdx.x, tid = threadIdx.x;
    c.w = __builtin_amdgcn_readfirstlane(tid >> 6);
    c.rws = mkrs(ws, WS_BYTES);
    c.tmo = (GAS u32*)(ws + 256);
    STAMP(0);
    STAMP3(0);

    __amdgpu_buffer_rsrc_t rcode;
    {
        unsigned long long pc; u32 len;
        asm volatile("s_getpc_b64 %0\n.Lfk_begin:\n\ts_mov_b32 %1, .Lfk_end-.Lfk_begin" : "=s"(pc), "=s"(len));
        rcode = mkrs((const void*)pc, len);
    }
    if (b < 32) {
        if (c.w < CPH) touch(c.lbase + L_JK, (tid & 63) * 128, rcode, (((b >> 3) * 2 + c.w) & 7) * 8192);
        const v2u ep = __builtin_amdgcn_raw_buffer_load_b64(c.rws, 0, OFF_EP, AUX_SC1);
        v4u xr[2][7];
#pragma unroll
        for (int h = 0; h < 2; ++h)
#pragma unroll
            for (int i = 0; i < 7; ++i) xr[h][i] = *(const v4u*)(x0 + (b * 64 + 2 * (tid >> 3) + h) * D + (i * 8 + (tid & 7)) * 8);
        MEMBAR();
        u32 res[2];
#pragma unroll
        for (int h = 0; h < 2; ++h) {
            float s1 = 0.f, s2 = 0.f;
#pragma unroll
            for (int i = 0; i < 7; ++i)
#pragma unroll
                for (int e = 0; e < 4; ++e) { s1 = dot2(xr[h][i][e], 0x3f803f80u, s1); s2 = dot2(xr[h][i][e], xr[h][i][e], s2); }
#pragma unroll
            for (int o = 1; o < 8; o <<= 1) { s1 += __shfl_xor(s1, o); s2 += __shfl_xor(s2, o); }
            const float m = s1 * (1.0f / D);
            res[h] = pk(m, ln_sd(fmaxf(__builtin_fmaf(-m, m, s2 * (1.0f / D)), 0.f)));
        }
        if ((tid & 7) == 0) st128(v4u{res[0], ep.x + 1u, res[1], ep.y + 1u}, c.rws, (u32)(b * 32 + (tid >> 3)) * 16, OFF_ST0);
        const int rg = tid >> 3, c8 = min(tid & 7, 6);
        v4u wr[14];
#pragma unroll
        for (int i = 0; i < 14; ++i) wr[i] = *(const v4u*)(w1 + (rg * 14 + i) * F + b * 56 + c8 * 8);
        float cs[8] = {0, 0, 0, 0, 0, 0, 0, 0};
#pragma unroll
        for (int i = 0; i < 14; ++i)
#pragma unroll
            for (int e = 0; e < 4; ++e) { cs[2 * e] += bflo(wr[i][e]); cs[2 * e + 1] += bfhi(wr[i][e]); }
        LAS float* sf = (LAS float*)c.L;
#pragma unroll
        for (int e = 0; e < 8; ++e) sf[rg * 65 + (tid & 7) * 8 + e] = cs[e];
        bar();
        if (tid < 56) {
            float t = 0.f;
#pragma unroll
            for (int i = 0; i < 32; ++i) t += sf[i * 65 + tid];
            const float t2 = __shfl_down(t, 1);
            if ((tid & 1) == 0) st128(v4u{__float_as_uint(t), ep.x + 1u, __float_as_uint(t2), ep.y + 1u}, c.rws, (u32)(b * 28 + (tid >> 1)) * 16, OFF_CSG);
        }
        WAIT_VM(0);
        bar();
        {
            const __amdgpu_buffer_rsrc_t rw1 = mkrs(w1, D * F * 2), rw2 = mkrs(w2, D * F * 2);
#pragma unroll
            for (int i = 0; i < HT1; ++i) touch(c.lbase + L_JK, (tid & 63) * 128, rw1, (u32)(112 + i * 16 + (b >> 3) * 4 + c.w) * 8192);
#pragma unroll
            for (int i = 0; i < HT2; ++i) touch(c.lbase + L_JK, (tid & 63) * 128, rw2, (u32)(i * 16 + (b >> 3) * 4 + c.w) * 8192);
        }
        WAIT_VM(0);
        return;
    }
    const int wb = b - 32;
    { const int xcd = wb & 7, idx = wb >> 3; c.grp = xcd * 4 + idx / 7; c.j = idx % 7; }
    c.gi = c.grp & 3;
    const int w = c.w;
    c.rw1 = mkrs(w1, D * F * 2); c.rw2 = mkrs(w2, D * F * 2);
    c.kb0 = (w + c.gi + 2) & 3;
    const int cp = (wb & 7) * 28 + (wb >> 3);
    W1T W1; W2T W2;

    u32 dval;
    {
        const int g = wb % 7, rb = wb / 7, lane = tid & 63;
        const int rg = (lane & 7) * 2 + ((lane >> 4) & 1), cl = ((lane >> 3) & 1) + 2 * (lane >> 5), cq = 4 * w + cl;
        const __amdgpu_buffer_rsrc_t rk = mkrs(kin, TSZ), rv = mkrs(vin, TSZ), rx = mkrs(x0, TSZ), rh = mkrs(hk, TSZ), rs_ = mkrs(sk, D * 8), rd = mkrs(Dv, D * 2);
        STAMP3(1);
        const u32 tb = (u32)((rb * 64) * 896 + g * 128);
        const u32 tv0 = (u32)((lane >> 3) * 896) + (((lane & 7) ^ ((lane >> 5) + 2 * w)) & 7) * 16;
        dma16<false>(c.lbase + T_X + w * 1024, tv0, rx, tb + w * 7168);
        dma16<false>(c.lbase + T_X + (w + 4) * 1024, tv0, rx, tb + (w + 4) * 7168);
        dma16<false>(c.lbase + T_K + w * 1024, tv0 + tb + (w - 1) * 7168, rk, 0u);
        dma16<false>(c.lbase + T_K + (w + 4) * 1024, tv0, rk, tb + (w + 3) * 7168);
        dma16<false>(c.lbase + T_V + w * 1024, tv0, rv, tb + w * 7168);
        dma16<false>(c.lbase + T_V + (w + 4) * 1024, tv0, rv, tb + (w + 4) * 7168);
        if (w == 0) dma16<false>(c.lbase + T_K + 8192, tv0, rk, tb + 7 * 7168);
        else dma16<false>(c.lbase + T_S + (w - 1) * 1024, (u32)lane * 16, rs_, g * 512);
        v4u ep4 = ld128_asm<true>(c.rws, 0u, OFF_EP);
        v4u he[2];
        he[0] = ld128_asm<false>(rh, tid * 16, cp * 8192); he[1] = ld128_asm<false>(rh, tid * 16, cp * 8192 + 4096);
        v4u dv4 = ld128_asm<false>(rd, 0u, cp * 4);
        {
            const WSt wt = w_st<0>(c, lane);
            sfor<0, NW0>([&](auto NC) { w_dma<decltype(NC)::value>(c, wt, w_soff<decltype(NC)::value>(c)); });
        }
        if constexpr (TLBW) {
            if (w == 0) touch(c.lbase + L_JK + 3072, (u32)lane * 524288u, c.rws, 4096u);
            else if (w < 3) touch(c.lbase + L_JK + 3072, (u32)lane * 262144u, w == 1 ? c.rw1 : c.rw2, 0u);
            else touch(c.lbase + L_JK + 3072, (u32)lane * 128u, rcode, (u32)((wb ^ (wb >> 3)) & 7) * 8192);
        }
        if constexpr (NT0) touch(c.lbase + L_JK + 3072, (u32)lane * 128, c.rw1, (u32)((wb >> 3) * 4 + w) * 8192);
        STAMP3(2);
        asm volatile("s_waitcnt vmcnt(%1)" : "+v"(ep4) : "i"(3 + NT0 + TLBW + 2 * NW0) : "memory");
        c.tagA = __builtin_amdgcn_readfirstlane(ep4.x) + 1u; c.tagB = __builtin_amdgcn_readfirstlane(ep4.y) + 1u;
        bar();
        STAMP(1); STAMP3(3);
        const u32 a1 = (u32)(rg * 512 + (((cq >> 1) ^ rg) & 7) * 16 + (cq & 1) * 8);
        const u32 ak1 = (u32)(rg * 512 + 1024 + (((cq >> 1) ^ (rg + 2)) & 7) * 16 + (cq & 1) * 8);
        const u32 ak0 = (u32)(rg * 512 + 512 + (((cq >> 1) ^ (rg + 1)) & 7) * 16 + (cq & 1) * 8);
        v2u kr[7], vr[4], xr[4];
#pragma unroll
        for (int i = 0; i < 4; ++i) xr[i] = *(LAS v2u*)(c.L + T_X + a1 + i * 128);
        const v4u w01 = *(LAS v4u*)(c.L + T_S + cq * 32), w23 = *(LAS v4u*)(c.L + T_S + cq * 32 + 16);
#pragma unroll
        for (int i = 0; i < 3; ++i) kr[i] = *(LAS v2u*)(c.L + T_K + ak0 + (i + 1) * 128);
#pragma unroll
        for (int i = 0; i < 4; ++i) { kr[3 + i] = *(LAS v2u*)(c.L + T_K + ak1 + i * 128); vr[i] = *(LAS v2u*)(c.L + T_V + a1 + i * 128); }
#pragma unroll
        for (int h = 0; h < 2; ++h) {
            auto r0 = __builtin_amdgcn_permlane16_swap(__builtin_amdgcn_perm(xr[1][h], xr[0][h], 0x05040100u), __builtin_amdgcn_perm(xr[1][h], xr[0][h], 0x07060302u), false, false);
            auto r1 = __builtin_amdgcn_permlane16_swap(__builtin_amdgcn_perm(xr[3][h], xr[2][h], 0x05040100u), __builtin_amdgcn_perm(xr[3][h], xr[2][h], 0x07060302u), false, false);
            st128(v4u{r0[0], r1[0], r0[1], r1[1]}, c.rws, (u32)((cl * 4 + ((lane >> 4) & 1)) * 4096 + (lane & 7) * 16), OFF_X0G + (g * 64 + 16 * w + 2 * h) * 4096 + rb * 128);
        }
        STAMP3(27);
        v4u kg[4];
#pragma unroll
        for (int e = 0; e < 4; ++e) {
            const v4u& wq = e < 2 ? w01 : w23;
            const float w0 = bflo(wq[(e & 1) * 2]), w1_ = bfhi(wq[(e & 1) * 2]), w2_ = bflo(wq[(e & 1) * 2 + 1]), w3 = bfhi(wq[(e & 1) * 2 + 1]);
            float kk[7], o[4];
#pragma unroll
            for (int i = 0; i < 7; ++i) kk[i] = (e & 1) ? bfhi(kr[i][e >> 1]) : bflo(kr[i][e >> 1]);
#pragma unroll
            for (int i = 0; i < 4; i += 2) {
                u32 a_ = pk(kk[i] * w3, kk[i + 1] * w3), b_ = pk(kk[i + 1] * w2_, kk[i + 2] * w2_);
                a_ = pk(bflo(a_) + bflo(b_), bfhi(a_) + bfhi(b_)); b_ = pk(kk[i + 2] * w1_, kk[i + 3] * w1_);
                a_ = pk(bflo(a_) + bflo(b_), bfhi(a_) + bfhi(b_)); b_ = pk(kk[i + 3] * w0, kk[i + 4] * w0);
                a_ = pk(bflo(a_) + bflo(b_), bfhi(a_) + bfhi(b_));
                o[i] = bflo(a_) * ((e & 1) ? bfhi(vr[i][e >> 1]) : bflo(vr[i][e >> 1]));
                o[i + 1] = bfhi(a_) * ((e & 1) ? bfhi(vr[i + 1][e >> 1]) : bflo(vr[i + 1][e >> 1]));
            }
            kg[e] = v4u{pk(o[3], o[2]), c.tagA, pk(o[1], o[0]), c.tagB};
        }
        STAMP3(28);
        asm volatile("s_waitcnt vmcnt(0)\n\ts_barrier" : "+v"(he[0]), "+v"(he[1]), "+v"(dv4), "+v"(kg[0]), "+v"(kg[1]), "+v"(kg[2]), "+v"(kg[3]) :: "memory");
        STAMP3(29);
#pragma unroll
        for (int e = 0; e < 4; ++e) st128(kg[e], c.rws, (u32)(cl * 4 * 8192 + (15 - rg) * 16), OFF_KVG + ((g * 64 + 16 * w + e) * 512 + 496 - 16 * rb) * 16);
        {
            const WSt wt = w_st<0>(c, lane);
            sfor<NW0, NW1>([&](auto NC) { w_dma<decltype(NC)::value>(c, wt, w_soff<decltype(NC)::value>(c)); });
        }
        STAMP(2); STAMP3(4);
        dval = (w >> 1) ? dv4.x >> 16 : dv4.x & 0xffffu;
#pragma unroll
        for (int cc = 0; cc < 2; ++cc) {
            LAS char* cb = c.L + CV_B + cc * CV_CH;
            *(LAS v4u*)(cb + CV_HE + 32 + tid * 16) = he[cc];
            if (tid < 2) { *(LAS v4u*)(cb + CV_HE + tid * 16) = v4u{0, 0, 0, 0}; *(LAS v4u*)(cb + CV_HO + tid * 16) = v4u{0, 0, 0, 0}; }
            if (tid < 32) *(LAS v4u*)(cb + CV_KV + 4096 + tid * 16) = v4u{0, 0, 0, 0};
        }
        bar();
#pragma unroll
        for (int cc = 0; cc < 2; ++cc) {
            const v4u e = he[cc];
            const u32 pv = *(LAS u32*)(c.L + CV_B + cc * CV_CH + CV_HE + 28 + tid * 16);
            *(LAS v4u*)(c.L + CV_B + cc * CV_CH + CV_HO + 32 + tid * 16) = v4u{__builtin_amdgcn_alignbit(e.x, pv, 16), __builtin_amdgcn_alignbit(e.y, e.x, 16),
                                                                            __builtin_amdgcn_alignbit(e.z, e.y, 16), __builtin_amdgcn_alignbit(e.w, e.z, 16)};
        }
        bar();
        STAMP3(5);
        SB();
    }
    {
        const int lane = lane_id(), i16 = lane & 15, q = lane >> 4, cc = w >> 1, p = w & 1, ch = 2 * cp + cc;
        LAS char* cb = c.L + CV_B + cc * CV_CH;
        const int o0 = i16 + 1 + 32 * p + 8 * q;
        LAS char* ab = cb + ((o0 & 1) ? CV_HO + 4 : CV_HE) + (o0 >> 1) * 4;
        v4u A[32];
        v4u AD;
        {
            const int jj = p ? -1 : 15 - i16 - 8 * q;
            const u32 dv = dval << ((jj & 1) * 16);
#pragma unroll
            for (int d = 0; d < 4; ++d) AD[d] = (jj >= 0 && (jj >> 1) == d) ? dv : 0u;
        }
        const Ctx cs_ = fresh(c);
        const u32 wl = wl1_of(cs_, lane);
        const WSt wt = w_st<0>(cs_, lane);
        v4u sg[4], g[4];
        SB();
        sfor<0, 8>([&](auto NC) {
            constexpr int n = decltype(NC)::value;
#pragma unroll
            for (int t = n * 4; t < n * 4 + 4; ++t) { LAS u32* pp = (LAS u32*)(ab + t * 128); A[t] = v4u{pp[0], pp[1], pp[2], pp[3]}; }
            SB();
        });
        STAMP(3); STAMP3(6);
        {
            __builtin_amdgcn_s_sleep(SLP0);
            unsigned spins = 0;
            auto bad4 = [&](const v4u (&x)[4]) { u32 d = 0; for (int t = 0; t < 4; ++t) d = tag_bad(x[t], c.tagA, c.tagB, d); return ANY(d != 0u); };
            NOUNROLL do {
                MEMBAR();
#pragma unroll
                for (int i = 0; i < 4; ++i) g[i] = ld128(c.rws, tid * 16, OFF_KVG + cp * 16384 + i * 4096);
            } while (bad4(g) && !spin_fail(c, spins, 5u));
            STAMP3(30);
#pragma unroll
            for (int i = 0; i < 4; ++i) *(LAS v2u*)(c.L + CV_B + (i >> 1) * CV_CH + CV_KV + (tid + 256 * (i & 1)) * 8) = v2u{g[i].x, g[i].z};
        }
        bar();
        STAMP(4); STAMP3(7);
        LAS char* bb = cb + CV_KV + (2032 - 16 * i16 + 32 * p + 8 * q) * 2;
        f32x4 acc[8];
        float xs[4][4], qs[4][4];
        v4u B[32];
        v2u xg[4];
        B[0] = *(LAS v4u*)(bb + 3 * 128); B[1] = *(LAS v4u*)(bb + 2 * 128); B[2] = *(LAS v4u*)(bb + 128);
        const u32 s4 = (u32)(256 * p + 4 * i16 + q), xo = OFF_X0G + ch * 4096;
        SB();
        sfor<0, 32>([&](auto OC) {
            constexpr int oi = decltype(OC)::value, om = oi - 3;
            constexpr int nm = 8 - (om > 0 ? (om + 3) / 4 : 0) + ((om >= 0 && (om & 3) == 0) ? 1 : 0);
            if constexpr (oi % 4 == 0) STAMP3(8 + oi / 4);
            if constexpr (oi == 9) { bar(); SB(); }
            if constexpr (oi + 3 < 32) B[oi + 3] = *(LAS v4u*)(bb - (om + 3) * 128);
#pragma unroll
            for (int cb_ = 7; cb_ >= 0; --cb_)
                if (4 * cb_ - om >= 0) acc[cb_] = mfma16(A[4 * cb_ - om], B[oi], oi == 0 ? f32x4{0, 0, 0, 0} : acc[cb_]);
            if constexpr (om >= 0 && (om & 3) == 0) acc[om >> 2] = mfma16(AD, B[oi], acc[om >> 2]);
            if constexpr (oi < 2) { sg[2 * oi] = ld128_asm<true>(c.rws, tid * 16, OFF_ST0 + oi * 8192); sg[2 * oi + 1] = ld128_asm<true>(c.rws, tid * 16, OFF_ST0 + oi * 8192 + 4096); }
            if constexpr (oi == 2 || oi == 3) {
                asm volatile("buffer_load_dwordx2 %0, %2, %3, %4 offen offset:%5 sc1\n\tbuffer_load_dwordx2 %1, %2, %3, %4 offen offset:%6 sc1"
                             : "=&v"(xg[2 * oi - 4]), "=&v"(xg[2 * oi - 3]) : "v"(s4 * 8), "s"(c.rws), "s"(xo), "i"((oi - 2) * 1024), "i"((oi - 2) * 1024 + 512) : "memory");
            }
            if constexpr (oi < NW1) w_read1<oi>(cs_, wl, W1, W2);
            if constexpr (oi >= 4 && (oi & 1) == 0 && NW1 + (oi - 4) / 2 < 12) w_dma<NW1 + (oi - 4) / 2>(cs_, wt, w_soff<NW1 + (oi - 4) / 2>(cs_));
            if constexpr (oi == 7) {
                asm volatile("s_waitcnt vmcnt(4)" : "+v"(sg[0]), "+v"(sg[1]), "+v"(sg[2]), "+v"(sg[3]), "+v"(xg[0]), "+v"(xg[1]), "+v"(xg[2]), "+v"(xg[3]) :: "memory");
                auto bad = [&]() { u32 d = 0; for (int t = 0; t < 4; ++t) d = tag_bad(sg[t], c.tagA, c.tagB, d); return ANY(d != 0u); };
                if (bad()) {
                    unsigned spins = 0;
                    NOUNROLL do {
                        MEMBAR();
#pragma unroll
                        for (int t = 0; t < 4; ++t) sg[t] = ld128(c.rws, tid * 16, OFF_ST0 + t * 4096);
                    } while (bad() && !spin_fail(c, spins, 6u));
                }
#pragma unroll
                for (int t = 0; t < 4; ++t) *(LAS v2u*)(c.L + CV_ST + (tid + 256 * t) * 8) = v2u{sg[t].x, sg[t].z};
            }
            if constexpr (oi >= 9 && oi < 13) sg[oi - 9] = *(LAS v4u*)(c.L + CV_ST + s4 * 16 + (oi - 9) * 1024);
            if constexpr (oi >= 11 && oi < 19) {
                constexpr int t = (oi - 11) >> 1, hf = (oi - 11) & 1;
                LV(xg[t]); LV(sg[t]);
                const u32 xw = xg[t][hf];
                const u32 sa = sg[t][2 * hf], sb = sg[t][2 * hf + 1];
                xs[t][2 * hf] = bflo(xw); xs[t][2 * hf + 1] = bfhi(xw);
                qs[t][2 * hf] = (xs[t][2 * hf] - bflo(sa)) * __builtin_amdgcn_rcpf(bfhi(sa));
                qs[t][2 * hf + 1] = (xs[t][2 * hf + 1] - bflo(sb)) * __builtin_amdgcn_rcpf(bfhi(sb));
                asm volatile("" : "+v"(qs[t][2 * hf]), "+v"(qs[t][2 * hf + 1]));
            }
            if constexpr (oi == 23 || oi == 25) {
                const v4u ff = {~0u, ~0u, ~0u, ~0u};
                st128(ff, c.rws, lane * 16, OFF_X3 + (cs_.grp * 56 + (oi - 23) * 14 + 4 * cs_.j + cs_.w) * 1024);
            }
            ilv<nm, 2>();
            SB();
        });
        STAMP(5); STAMP3(16);
        LAS char* ex = c.L;
        f32x4 fin[4];
        if (p == 0) {
#pragma unroll
            for (int t = 0; t < 4; ++t) { *(LAS f32x4*)(ex + w * 4096 + t * 1024 + lane * 16) = acc[4 + t]; fin[t] = acc[t]; }
        } else {
#pragma unroll
            for (int t = 0; t < 4; ++t) { *(LAS f32x4*)(ex + w * 4096 + t * 1024 + lane * 16) = acc[t]; fin[t] = acc[4 + t]; }
        }
        bar();
        STAMP3(17);
        const u32 toff = (u32)((32 * p + 8 * (q & 1) + (i16 >> 1)) * 28672 + ch * 64 + (i16 & 1) * 32 + (q >> 1) * 16);
        v2u y[4], x1[4];
#pragma unroll
        for (int t = 0; t < 4; ++t) {
            fin[t] += *(LAS f32x4*)(ex + (w ^ 1) * 4096 + t * 1024 + lane * 16);
            y[t] = v2u{pk(fin[t][0], fin[t][1]), pk(fin[t][2], fin[t][3])};
            x1[t] = v2u{pk(__builtin_fmaf(qs[t][0], bflo(y[t].x), xs[t][0]), __builtin_fmaf(qs[t][1], bfhi(y[t].x), xs[t][1])),
                        pk(__builtin_fmaf(qs[t][2], bflo(y[t].y), xs[t][2]), __builtin_fmaf(qs[t][3], bfhi(y[t].y), xs[t][3]))};
        }
        auto merge = [&](const v2u& a, const v2u& b_) {
            auto r0 = __builtin_amdgcn_permlane16_swap(a.x, b_.x, false, false);
            auto r1 = __builtin_amdgcn_permlane16_swap(a.y, b_.y, false, false);
            return v4u{r0[0], r1[0], r0[1], r1[1]};
        };
        st128(merge(x1[0], x1[1]), c.rws, toff, OFF_X1);
        st128(merge(x1[2], x1[3]), c.rws, toff, OFF_X1 + 16 * 28672);
        st128(merge(y[0], y[1]), c.rws, toff, OFF_YT);
        st128(merge(y[2], y[3]), c.rws, toff, OFF_YT + 16 * 28672);
        STAMP(6); STAMP3(18);
        sfor<12, 12 + NW1>([&](auto NC) { w_dma<decltype(NC)::value>(cs_, wt, w_soff<decltype(NC)::value>(cs_)); });
        WAIT_VM(6 + 2 * NW1);
        sfor<NW1, 12>([&](auto NC) { w_read1<decltype(NC)::value>(cs_, wl, W1, W2); });
        WAIT_LGKM(0);
        sfor<12 + NW1, 20>([&](auto NC) { w_dma<decltype(NC)::value>(cs_, wt, w_soff<decltype(NC)::value>(cs_)); });
        STAMP3(19);
        WAIT_VM(16);
        bar();
        if (tid < 8) __builtin_amdgcn_raw_buffer_store_b64(v2u{c.tagA, c.tagB}, c.rws, (u32)(tid * 2048 + wb * 8), OFF_FL, AUX_SC1);
        MEMBAR();
        STAMP(7);
        sfor<20, 24>([&](auto NC) { w_dma<decltype(NC)::value>(cs_, wt, w_soff<decltype(NC)::value>(cs_)); });
        WAIT_VM(12);
        sfor<12, 18>([&](auto NC) { w_read1<decltype(NC)::value>(cs_, wl, W1, W2); });
        WAIT_LGKM(0);
        sfor<24, 28>([&](auto NC) { w_dma<decltype(NC)::value>(cs_, wt, w_soff<decltype(NC)::value>(cs_)); });
        STAMP3(21);
    }
    Ad ad;
    ad.l16 = lane_id() * 16;
    ad.hst = OFF_H + (c.grp * 224 + 16 * c.j + 4 * w) * 1024;
    ad.x3o = OFF_X3 + (c.grp * 56 + 4 * c.j + w) * 1024;
    ad.sto = OFF_ST + c.grp * 2 * ST_HG;
    asm volatile("" : "+s"(ad.hst), "+s"(ad.x3o), "+s"(ad.sto));
    const u32 swl = wl2_of(c, ad.l16 >> 4);
    const WSt w2t = w_st<28>(c, ad.l16 >> 4);
    u32 wso[7];
#pragma unroll
    for (int m = 0; m < 7; ++m) {
        const int mm = member(c, m);
        wso[m] = (256 * mm + 64 * w) * (D * 2) + 128 * c.j; ad.so[m] = ad.hst + (mm - c.j) * 16384;
        asm volatile("" : "+s"(wso[m]), "+s"(ad.so[m]));
    }
    auto w2_dma = [&](auto NC) { constexpr int n = decltype(NC)::value; w_dma<n>(c, w2t, wso[k_mem(w2_step_of(n))] + k_sub(w2_step_of(n)) * 16 * (D * 2)); };
    {
        v4u cg = ld128_asm<true>(c.rws, tok32(ad), OFF_CSG + c.j * 2048 + w * 512);
        sfor<28, 30>(w2_dma);
        WAIT_VM(12);
        w_read<18, 24>(c, W1, W2);
        STAMP(8); STAMP3(22);
        if (w == 0) {
            unsigned spins = 0;
            NOUNROLL for (;;) {
                MEMBAR();
                const v4u f0 = ld128(c.rws, ad.l16, OFF_FL + (wb & 7) * 2048);
                const v4u f1 = ld128(c.rws, 1024 + min(ad.l16, 752u), OFF_FL + (wb & 7) * 2048);
                const u32 d = xor_or(f0.x, c.tagA, xor_or(f0.z, c.tagA, xor_or(f1.x, c.tagA, xor_or(f1.z, c.tagA, tag_bad(f0, c.tagB, c.tagB, tag_bad(f1, c.tagB, c.tagB, 0u))))));
                if (!ANY(d != 0u)) break;
                if (spin_fail(c, spins, 7u)) break;
            }
            if (wb == 0 && ad.l16 == 0) __builtin_amdgcn_raw_buffer_store_b64(v2u{c.tagA, c.tagB}, c.rws, 0, OFF_EP, AUX_SC1);
        }
        bar();
        STAMP(9); STAMP3(23);
        {
            const u32 xb = c.lbase + L_X + w * 7168, xs_ = OFF_X1 + c.grp * 57344 + w * 7168;
            sfor<0, 14>([&](auto IC) {
                constexpr int i = decltype(IC)::value, o = (i / 7) * 28672 + (i % 7) * 1024;
                dma16<true>(xb + o, ad.l16, c.rws, xs_ + o);
            });
        }
        STAMP3(24);
        WAIT_VM(14);
        w_read<24, 28>(c, W1, W2);
        w_read1<28>(c, swl, W1, W2); w_read1<29>(c, swl, W1, W2);
        w_neg<28>(W2); w_neg<29>(W2);
        LV(cg);
        if (ANY(tag_bad(cg, c.tagA, c.tagB, 0u) != 0u)) {
            unsigned spins = 0;
            NOUNROLL do { MEMBAR(); cg = ld128(c.rws, tok32(ad), OFF_CSG + c.j * 2048 + w * 512); } while (ANY(tag_bad(cg, c.tagA, c.tagB, 0u) != 0u) && !spin_fail(c, spins, 8u));
        }
        *(LAS v2u*)(c.L + L_CS + w * 256 + (tok32(ad) >> 1)) = v2u{cg.x, cg.z};
        WAIT_VM(7);
        bar();
        STAMP(10); STAMP3(25);
    }
    STAMP3(26);
    pin_a<0, 25>(W1);
    v4u hb[56], xres[2];
    G1Out ga, gb;
    v2u gate;
    f32x4 csq[3];
    St1 st;
    ad.ca = L_CS + (w * 64 + (ad.l16 >> 9) * 4) * 4;
    using M0 = std::integral_constant<int, 0>; using M1 = std::integral_constant<int, 1>;
    {
        st.ad = xtr_of(ad.l16 >> 4) + w * 7168;
        auto stats = [&](auto MC, auto HC, auto FC, G1Out& o) {
            constexpr int H = decltype(HC)::value, f = decltype(FC)::value;
            constexpr bool TR = decltype(MC)::value == 0;
            if constexpr (f == 0) { st.s1 = 0.f; st.s2 = 0.f; }
            if constexpr (f >= 2 && f < 23) { constexpr int ph = (f - 2) % 3, k = (f - 2) / 3; st1_acc<k, ph * 3, ph == 2 ? 8 : ph * 3 + 3>(st); }
            if constexpr (f % 3 == 0 && f < 21) st1_read<TR, H, f / 3>(c, st);
            if constexpr (f == 23) part_put(c, ad, L_SP + H * 1024, st.s1, st.s2);
            if constexpr (f == 24) { SB(); asm volatile("s_waitcnt lgkmcnt(%0)\n\ts_barrier" :: "i"(TR ? 2 : 1) : "memory"); }
            if constexpr (f == 25) { const f32x2 t = part_get(c, ad, L_SP + H * 1024); ln_scalars(t[0], t[1], o.mb, o.r); asm volatile("" : "+v"(o.mb), "+v"(o.r)); }
        };
        auto stage = [&](auto GC_, auto XC) {
            constexpr int g = decltype(GC_)::value, extra = decltype(XC)::value;
            if constexpr (g >= 15 && g < 43 && (g & 1)) w_neg<30 + (g - 15) / 2>(W2);
            if constexpr (g == 41) *(LAS v4u*)(c.L + (ad.l16 + w * 1024) + L_JK) = W2[15][1];
            if constexpr (g >= 13 && g < 41 && (g & 1)) { WAIT_VM(12 + extra); w_read1<30 + (g - 13) / 2>(c, swl, W1, W2); }
            if constexpr (g < 52 && (g & 1) == 0) w2_dma(std::integral_constant<int, 30 + g / 2>{});
        };
        constexpr unsigned E2 = 0xAAAAAAAu;
        g1_main<true, 0, PS0, 0x9AAAAAAu>(c, ad, W1, ga.acc, [&](auto FC) {
            constexpr int f = decltype(FC)::value;
            stage(FC, M0{});
            stats(M0{}, M0{}, FC, ga);
            if constexpr (f >= 26) csq[f - 26] = epi_cs(c, ad, f - 26);
        });
        STAMP(11);
        auto side_b = [&](auto MC, auto FC) {
            constexpr int f = decltype(FC)::value, M = decltype(MC)::value;
            if constexpr (f < 6) csq[(f + 2) % 3] = epi_cs(c, ad, f + 2);
            if constexpr (f < 8) g1_epi<0, M, f>(c, ad, ga, csq[f % 3], hb[f >> 1]);
            if constexpr (f < 8 && (f & 1)) h_store<0, f / 2>(c, ad, hb[f >> 1]);
            if constexpr (f == 18) gate = gate_ld<true>(c, ad, 0);
            if constexpr (f == 19) xres[0] = xres_ld<0, M>(c, ad);
            if constexpr (f == 23) {
                SB();
                asm volatile("s_waitcnt vmcnt(%1)" : "+v"(gate) : "i"(M == 0 ? 4 : 0) : "memory");
                gate_wait<M>(c, ad, 0, gate);
            }
            if constexpr (f >= 24) hb[f - 20] = h_next<f - 20>(c, ad);
            if constexpr (f >= 26) csq[f - 26] = epi_cs(c, ad, f - 26);
        };
        const u32 ra = L_W + w * 24576 + 1024 + ad.l16;
        v4u rm;
        g1_main<true, 1, PS0 + 7, 0x9AAAAAAu>(c, ad, W1, gb.acc, [&](auto FC) {
            constexpr int f = decltype(FC)::value;
            stage(std::integral_constant<int, 28 + f>{}, std::integral_constant<int, (f < 8 ? f / 2 : 4)>{});
            if constexpr (f >= 16 && f < 28 && (f & 1) == 0) { LV(rm); *(LAS v4u*)(c.L + ra + ((8 + (f - 16) / 2) % 12) * 2048) = rm ^ SGN; }
            if constexpr (f >= 14 && f < 26 && (f & 1) == 0) { WAIT_VM(f < 24 ? 14 : 12); rm = *(LAS v4u*)(c.L + ra + ((8 + (f - 14) / 2) % 12) * 2048); }
            stats(M0{}, M1{}, FC, gb);
            side_b(M0{}, FC);
        });
        STAMP(12);
        {
            WAIT_VM(4);
            v4u t[6];
#pragma unroll
            for (int i = 0; i < 6; ++i) t[i] = *(LAS v4u*)(c.L + ra + ((14 + i) % 12) * 2048);
#pragma unroll
            for (int i = 0; i < 6; ++i) *(LAS v4u*)(c.L + ra + ((14 + i) % 12) * 2048) = t[i] ^ SGN;
            SB();
        }
        pin_a<0, 25>(W1);
        STAMP2(PS0 + 14);
        STAMP(13);
        g2_pair<0>(c, ad, W2, hb, gb, csq, ga.r, xres, c.rws);
        pin_a<0, 25>(W1);
        STAMP(14);
        v4u t[7];
        st.ad = ad.l16 + w * 7168;
        STAMP4(6);
        bar();
        STAMP4(7);
        STAMP(17);
        g1_main<false, 0, PS1, 0x9AAAAAAu>(c, ad, W1, ga.acc, [&](auto FC) {
            constexpr int f = decltype(FC)::value;
            stats(M1{}, M0{}, FC, ga);
            if constexpr (f >= 12 && f < 19) t[f - 12] = x3_ld1<1, f - 12>(c, ad);
            if constexpr (f == 24) x3_wait<1>(c, ad, t);
            if constexpr (f >= 25) { x3_put1<1, 2 * (f - 25)>(c, ad, t[2 * (f - 25)]); x3_put1<1, 2 * (f - 25) + 1>(c, ad, t[2 * (f - 25) + 1]); }
            if constexpr (f == 27) x3_put1<1, 6>(c, ad, t[6]);
            if constexpr (f >= 26) csq[f - 26] = epi_cs(c, ad, f - 26);
        });
        STAMP4(8);
        STAMP4(9);
        STAMP4(10);
        STAMP(18);
        bar();
        STAMP4(11);
        g1_main<false, 1, PS1 + 7, 0x9AAAAAAu>(c, ad, W1, gb.acc, [&](auto FC) {
            stats(M1{}, M1{}, FC, gb);
            side_b(M1{}, FC);
        });
        (void)E2;
        STAMP(19);
        STAMP2(PS1 + 14);
        STAMP(20);
        const __attribute__((address_space(4))) char* ka = (const __attribute__((address_space(4))) char*)__builtin_amdgcn_kernarg_segment_ptr();
        asm volatile("" : "+s"(ka));
        g2_pair<1>(c, ad, W2, hb, gb, csq, ga.r, xres, mkrs(*(void* const __attribute__((address_space(4)))*)(ka + 72), TSZ));
        STAMP(21);
    }
    asm volatile(".Lfk_end:");
}

extern "C" void kernel_launch(void* const* d_in, const int* in_sizes, int n_in,
                              void* d_out, int out_size, void* d_ws, size_t ws_size,
                              hipStream_t stream) {
    (void)hipFuncSetAttribute((const void*)fused_kernel, hipFuncAttributeMaxDynamicSharedMemorySize, L_TOT);
    fused_kernel<<<256, 256, L_TOT, stream>>>((const u16*)d_in[0], (const u16*)d_in[1], (const u16*)d_in[2], (const u16*)d_in[3], (const u16*)d_in[4],
                                              (const u16*)d_in[5], (const u16*)d_in[7], (const u16*)d_in[8], (unsigned char*)d_ws, (u16*)d_out);
}
```

```cpp
#include <hip/hip_runtime.h>
#include <stdint.h>
#include <type_traits>

typedef unsigned short u16;
typedef unsigned u32;
typedef u32 v4u __attribute__((ext_vector_type(4)));
typedef u32 v2u __attribute__((ext_vector_type(2)));
typedef float f32x2 __attribute__((ext_vector_type(2)));
typedef float f32x4 __attribute__((ext_vector_type(4)));
typedef float f32x16 __attribute__((ext_vector_type(16)));
typedef __bf16 bf16x8 __attribute__((ext_vector_type(8)));
typedef __bf16 bf16x2 __attribute__((ext_vector_type(2)));
typedef short s16x4 __attribute__((ext_vector_type(4)));
typedef short s16x2 __attribute__((ext_vector_type(2)));
typedef unsigned short u16x2 __attribute__((ext_vector_type(2)));
#define LAS __attribute__((address_space(3)))
#define GAS __attribute__((address_space(1)))
#define DI __device__ __forceinline__
#define RLX_AGENT __ATOMIC_RELAXED, __HIP_MEMORY_SCOPE_AGENT
#define SB() __builtin_amdgcn_sched_barrier(0)
#define NOUNROLL _Pragma("unroll 1")
#define MEMBAR() asm volatile("" ::: "memory")
template <class T> __device__ __forceinline__ void LV(T& x) { asm volatile("" : "+v"(x)); }
template <class T> __device__ __forceinline__ void LA(T& x) { asm volatile("" : "+a"(x)); }
#define ANY(x) (__builtin_amdgcn_ballot_w64(x) != 0ull)

constexpr int S = 2048, D = 448, F = 1792;
constexpr float EPS = 1e-5f;
constexpr u32 TSZ = S * D * 2;
constexpr u32 OFF_EP  = 0;
constexpr u32 OFF_FL  = 4096;
constexpr u32 OFF_ST0 = OFF_FL + 16384;
constexpr u32 OFF_CSG = OFF_ST0 + 16384;
constexpr u32 OFF_ST  = OFF_CSG + 16384;
constexpr u32 OFF_KVG = 1048576;
constexpr u32 OFF_X0G = OFF_KVG + 2 * TSZ;
constexpr u32 OFF_X1  = OFF_X0G + 2 * TSZ;
constexpr u32 OFF_YT  = OFF_X1 + TSZ;
constexpr u32 OFF_X3  = OFF_YT + TSZ;
constexpr u32 OFF_H   = OFF_X3 + TSZ;
constexpr u32 WS_BYTES = OFF_H + 64 * 112 * 1024;
static_assert(OFF_ST + 64 * 24 * 512 <= OFF_KVG, "ws map");
constexpr int RSRC_W3 = 0x00020000;
constexpr int AUX_SC1 = 16;
constexpr unsigned SPIN_CAP = 1u << 14;
constexpr u32 SGN = 0x80008000u;

template <int I, int N, class Fn> DI void sfor(Fn&& fn) { if constexpr (I < N) { fn(std::integral_constant<int, I>{}); sfor<I + 1, N>(fn); } }

DI float bflo(u32 x) { return __uint_as_float(x << 16); }
DI float bfhi(u32 x) { return __uint_as_float(x & 0xffff0000u); }
DI u32 pk(float a, float b) { f32x2 v = {a, b}; return __builtin_bit_cast(u32, __builtin_convertvector(v, bf16x2)); }
DI float rbf(float a) { return bflo(pk(a, a)); }
DI __amdgpu_buffer_rsrc_t mkrs(const void* p, u32 bytes) { return __builtin_amdgcn_make_buffer_rsrc((void*)p, 0, (int)bytes, RSRC_W3); }
DI f32x4 mfma16(v4u a, v4u b, f32x4 c) { return __builtin_amdgcn_mfma_f32_16x16x32_bf16(__builtin_bit_cast(bf16x8, a), __builtin_bit_cast(bf16x8, b), c, 0, 0, 0); }
DI f32x16 mfma32(v4u a, v4u b, f32x16 c) { return __builtin_amdgcn_mfma_f32_32x32x16_bf16(__builtin_bit_cast(bf16x8, a), __builtin_bit_cast(bf16x8, b), c, 0, 0, 0); }
DI f32x16 zero16() { f32x16 z; for (int i = 0; i < 16; ++i) z[i] = 0.f; return z; }
DI v2u trd(LAS char* p) { return __builtin_bit_cast(v2u, __builtin_amdgcn_ds_read_tr16_b64_v4i16((LAS s16x4*)p)); }
DI v4u trd2(LAS char* p, int second) { v2u a = trd(p), b = trd(p + second); return v4u{a.x, a.y, b.x, b.y}; }
DI float dot2(u32 a, u32 b, float c) { return __builtin_amdgcn_fdot2_f32_bf16(__builtin_bit_cast(bf16x2, a), __builtin_bit_cast(bf16x2, b), c, false); }
DI v4u ld128(__amdgpu_buffer_rsrc_t rs, u32 vo, u32 so) { return __builtin_amdgcn_raw_buffer_load_b128(rs, vo, so, AUX_SC1); }
template <bool SC1, int OFF = 0> DI v4u ld128_asm(__amdgpu_buffer_rsrc_t rs, u32 vo, u32 so) {
    v4u r;
    if constexpr (SC1) asm volatile("buffer_load_dwordx4 %0, %1, %2, %3 offen offset:%4 sc1" : "=v"(r) : "v"(vo), "s"(rs), "s"(so), "i"(OFF) : "memory");
    else asm volatile("buffer_load_dwordx4 %0, %1, %2, %3 offen offset:%4" : "=v"(r) : "v"(vo), "s"(rs), "s"(so), "i"(OFF) : "memory");
    return r;
}
template <int OFF = 0> DI void st128(v4u v, __amdgpu_buffer_rsrc_t rs, u32 vo, u32 so) {
    asm volatile("buffer_store_dwordx4 %0, %1, %2, %3 offen offset:%4 sc1\n\ts_nop 1" :: "v"(v), "v"(vo), "s"(rs), "s"(so), "i"(OFF) : "memory");
}
DI int lane_id() { int l; asm volatile("v_mbcnt_lo_u32_b32 %0, -1, 0\n\tv_mbcnt_hi_u32_b32 %0, -1, %0" : "=v"(l)); return l; }
DI float hsum(float a) {
    const u32 v = __float_as_uint(a);
    auto t = __builtin_amdgcn_permlane32_swap(v, v, false, false);
    return __uint_as_float(t[0]) + __uint_as_float(t[1]);
}
DI float ln_sd(float var) { return rbf(__builtin_amdgcn_sqrtf(rbf(rbf(var) + EPS))); }
DI void ln_scalars(float s1, float s2, float& m, float& r) {
    m = s1 * (1.0f / D);
    r = __builtin_amdgcn_rsqf(fmaxf(__builtin_fmaf(-m, m, s2 * (1.0f / D)), 0.f) + EPS);
}
DI u32 xor_or(u32 a, u32 b, u32 c) { return __builtin_amdgcn_bitop3_b32(a, b, c, 0xBE); }
DI u32 tag_bad(const v4u& g, u32 tA, u32 tB, u32 acc) { return xor_or(g.w, tB, xor_or(g.y, tA, acc)); }
template <bool SC1> DI void dma16(u32 ldsaddr, u32 voff, __amdgpu_buffer_rsrc_t rs, u32 soff) {
    if constexpr (SC1) asm volatile("s_mov_b32 m0, %0\n\ts_nop 0\n\tbuffer_load_dwordx4 %1, %2, %3 offen sc1 lds" :: "s"(ldsaddr), "v"(voff), "s"(rs), "s"(soff) : "memory", "m0");
    else asm volatile("s_mov_b32 m0, %0\n\ts_nop 0\n\tbuffer_load_dwordx4 %1, %2, %3 offen lds" :: "s"(ldsaddr), "v"(voff), "s"(rs), "s"(soff) : "memory", "m0");
}
template <int LI> DI void dma_pair(u32 ldsbase, u32 vo0, u32 vo1, __amdgpu_buffer_rsrc_t rs, u32 soff) {
    asm volatile("s_add_u32 m0, %0, %5\n\ts_nop 0\n\tbuffer_load_dwordx4 %1, %3, %4 offen lds\n\tbuffer_load_dwordx4 %2, %3, %4 offen offset:1024 lds"
                 :: "s"(ldsbase), "v"(vo0), "v"(vo1), "s"(rs), "s"(soff), "i"(LI) : "memory", "m0", "scc");
}
DI void touch(u32 junk, u32 voff, __amdgpu_buffer_rsrc_t rs, u32 soff) {
    asm volatile("s_mov_b32 m0, %0\n\ts_nop 0\n\tbuffer_load_dword %1, %2, %3 offen lds" :: "s"(junk), "v"(voff), "s"(rs), "s"(soff) : "memory", "m0");
}
#define WAIT_VM(n) asm volatile("s_waitcnt vmcnt(%0)" :: "i"(n) : "memory")
#define WAIT_LGKM(n) asm volatile("s_waitcnt lgkmcnt(%0)" :: "i"(n) : "memory")
DI void bar() { asm volatile("s_waitcnt lgkmcnt(0)\n\ts_barrier" ::: "memory"); }
DI int wrap7(int x) { return x >= 7 ? x - 7 : x; }
template <int N, int K> DI void ilv() { if constexpr (N > 0) { __builtin_amdgcn_sched_group_barrier(0x8, 1, 0); __builtin_amdgcn_sched_group_barrier(0x366, K, 0); ilv<N - 1, K>(); } }

constexpr int L_X = 0;
constexpr int L_W = 57344;
constexpr int L_M = L_W + 98304;
constexpr int L_SP = L_M;
constexpr int L_CS = L_M + 2048;
constexpr int L_S2 = L_M + 3072;
constexpr int L_JK = L_M + 4096;
constexpr int L_TOT = L_M + 8192;
constexpr int T_K = 0, T_V = 9216, T_X = 17408, T_S = L_JK;
constexpr int CV_B = 25600;
constexpr int CV_HE = 0, CV_HO = 4160, CV_KV = CV_HO + 4160, CV_CH = CV_KV + 4608;
constexpr int CV_ST = 16384;
static_assert(CV_B + 2 * CV_CH <= L_W, "lds map");

struct Ctx {
    LAS char* L;
    __amdgpu_buffer_rsrc_t rws, rw1, rw2;
    GAS u32* tmo;
    int w, j, grp, kb0, gi;
    u32 lbase, tagA, tagB;
};
DI Ctx fresh(const Ctx& c0) { Ctx c = c0; asm volatile("" : "+s"(c.w), "+s"(c.j), "+s"(c.grp), "+s"(c.kb0), "+s"(c.gi)); return c; }
DI bool spin_fail(const Ctx& c, unsigned& spins, u32 code) {
    if (++spins > SPIN_CAP) { __hip_atomic_store(c.tmo, code, RLX_AGENT); return true; }
    __builtin_amdgcn_s_sleep(1);
    return false;
}
DI u32 xtr_of(int lane) { return (u32)(((lane >> 5) * 4 + ((lane >> 2) & 3)) * 64 + ((lane >> 4) & 1) * 32 + (lane & 3) * 8); }
DI u32 wl1_of(const Ctx& c, int lane) {
    const int rr = (lane >> 5) * 4 + ((lane >> 2) & 3);
    return (u32)(L_W + c.w * 24576 + rr * 128 + ((rr >> 1) & 1) * 64 + ((lane >> 4) & 1) * 32 + (lane & 3) * 8);
}
DI u32 wl2_of(const Ctx& c, int lane) { return wl1_of(c, lane) ^ (u32)((c.w >> 1) * 64 + (c.w & 1) * 32); }
DI int member(const Ctx& c, int m) {
    if (m == 0) return c.j;
    int t = m - 1 + 2 * c.gi; t = t >= 6 ? t - 6 : t; t = t >= 6 ? t - 6 : t;
    return wrap7(c.j + 1 + t);
}
typedef v4u W1T[28][2];
typedef v4u W2T[16][2];
typedef f32x16 AccT[2];

DI constexpr int k_mem(int f) { return f < 4 ? 0 : 1 + (f - 4) % 6; }
DI constexpr int k_sub(int f) { return f < 4 ? f : (f - 4) / 6; }
DI constexpr int w2_step_of(int n) { return n < 40 ? 2 * (n - 28) : (n < 44 ? n - 16 : 2 * (n - 44) + 1); }
template <int N> DI u32 w_soff(const Ctx& c) {
    if constexpr (N < 28) return (N % 7 + 7 * ((N / 7 + c.kb0) & 3)) * 16 * (F * 2) + (256 * c.j + 64 * c.w) * 2;
    else return (256 * member(c, k_mem(w2_step_of(N))) + 64 * c.w + 16 * k_sub(w2_step_of(N))) * (D * 2) + 128 * c.j;
}
template <int N> DI u32 w_voff(int lane) { return (lane >> 3) * ((N < 28 ? F : D) * 2) + ((lane & 7) ^ (((lane >> 4) & 1) << 2)) * 16; }
struct WSt { u32 ring, vo0, vo1; };
template <int N> DI WSt w_st(const Ctx& c, int lane) { WSt t; t.ring = c.lbase + L_W + c.w * 24576; t.vo0 = w_voff<N>(lane); t.vo1 = t.vo0 + 8 * ((N < 28 ? F : D) * 2) - 1024; return t; }
template <int N> DI void w_dma(const Ctx& c, const WSt& t, u32 so) { dma_pair<(N % 12) * 2048>(t.ring, t.vo0, t.vo1, N < 28 ? c.rw1 : c.rw2, so); }
DI v4u w_frag(const Ctx& c, u32 wl, int slot, int t) { return trd2(c.L + (wl ^ (u32)(t * 64)) + slot * 2048, 1024); }
template <int N> DI void w_read1(const Ctx& c, u32 wl, W1T& W1, W2T& W2) {
#pragma unroll
    for (int t = 0; t < 2; ++t) {
        if constexpr (N < 28) W1[N][t] = w_frag(c, wl, N % 12, t);
        else W2[N - 28][t] = w_frag(c, wl, N % 12, t);
    }
}
template <int N> DI void w_neg(W2T& W2) {
#pragma unroll
    for (int t = 0; t < 2; ++t) { LV(W2[N - 28][t]); W2[N - 28][t].z ^= SGN; W2[N - 28][t].w ^= SGN; asm volatile("" : "+v"(W2[N - 28][t])); }
}
template <int N0, int N1> DI void w_read(const Ctx& c0, W1T& W1, W2T& W2) {
    const Ctx c = fresh(c0);
    const u32 wl = wl1_of(c, lane_id());
    sfor<N0, N1>([&](auto NC) { w_read1<decltype(NC)::value>(c, wl, W1, W2); });
}

template <int N0, int N1> DI void pin_a(W1T& W1) {
    if constexpr (N0 < N1) { asm volatile("" : "+a"(W1[N0][0]), "+a"(W1[N0][1])); pin_a<N0 + 1, N1>(W1); }
}
struct G1Out { AccT acc; float mb, r; };
DI constexpr bool reg_start(unsigned long long end, int f) { return f == 0 || ((end >> (f - 1)) & 1ull); }
DI constexpr int reg_len(unsigned long long end, int f) { int n = 1; while (!reg_start(end, f - n + 1)) ++n; return n; }
struct Ad {
    u32 l16;
    u32 ca;
    u32 hst;
    u32 x3o;
    u32 sto;
    u32 so[7];
};
constexpr u32 ST_HG = 3 * 4096;
DI u32 tok32(const Ad& a) { return a.l16 & 496u; }

template <bool TR, int HALF, unsigned END, class Side>
DI void g1_main(const Ctx& c, Ad& a, const W1T& W1, AccT& acc, Side&& side) {
    const u32 lp = TR ? xtr_of(a.l16 >> 4) : a.l16;
    int kb = c.kb0; asm volatile("" : "+s"(kb));
    u32 la = 0;
    auto ldb = [&](int f) {
        if (f % 7 == 0) la = lp + ((f / 7 + kb) & 3) * 7168;
        LAS char* p = c.L + la + (L_X + HALF * 28672 + (f % 7) * 1024);
        if constexpr (TR) return trd2(p, 512);
        else return *(LAS v4u*)p;
    };
    v4u B[28];
    B[0] = ldb(0); B[1] = ldb(1); B[2] = ldb(2);
    SB();
    sfor<0, 28>([&](auto FC) {
        constexpr int f = decltype(FC)::value;
        if constexpr (reg_start(END, f)) asm volatile("" : "+v"(a.l16), "+v"(a.ca));
        acc[0] = mfma32(W1[f][0], B[f], f == 0 ? zero16() : acc[0]);
        acc[1] = mfma32(W1[f][1], B[f], f == 0 ? zero16() : acc[1]);
        if constexpr (f + 3 < 28) B[f + 3] = ldb(f + 3);
        side(FC);
        if constexpr ((END >> f) & 1u) { ilv<2 * reg_len(END, f), 5>(); SB(); }
    });
}
struct St1 { u32 ad; v4u fr[2]; float s1, s2; };
template <bool TR, int HALF, int K> DI void st1_read(const Ctx& c, St1& s) {
    if constexpr (TR) s.fr[K & 1] = trd2(c.L + s.ad + (L_X + HALF * 28672 + K * 1024), 512);
    else s.fr[K & 1] = *(LAS v4u*)(c.L + s.ad + (L_X + HALF * 28672 + K * 1024));
}
template <int K, int E0, int E1> DI void st1_acc(St1& s) {
#pragma unroll
    for (int e = E0; e < E1; ++e) { if (e == E0) LV(s.fr[K & 1]); const u32 x = s.fr[K & 1][e >> 1]; if (e & 1) s.s2 = dot2(x, x, s.s2); else s.s1 = dot2(x, 0x3f803f80u, s.s1); }
}
DI void part_put(const Ctx& c, const Ad& a, int off, float s1, float s2) { *(LAS f32x2*)(c.L + off + tok32(a) * 2 + c.w * 8) = f32x2{hsum(s1), hsum(s2)}; }
DI f32x2 part_get(const Ctx& c, const Ad& a, int off) {
    const f32x4 u = *(LAS f32x4*)(c.L + off + tok32(a) * 2), v = *(LAS f32x4*)(c.L + off + tok32(a) * 2 + 16);
    return f32x2{(u[0] + u[2]) + (v[0] + v[2]), (u[1] + u[3]) + (v[1] + v[3])};
}
DI void frag_sums(const v4u& p, float& s1, float& s2) {
    s1 = 0.f; s2 = 0.f;
#pragma unroll
    for (int e = 0; e < 4; ++e) { s1 = dot2(p[e], 0x3f803f80u, s1); s2 = dot2(p[e], p[e], s2); }
}
template <int HALF, int KIND> DI void stat_pub(const Ctx& c, const Ad& a, int off, int who) {
    if (c.w == who) {
        f32x2 t = part_get(c, a, off);
        const bool z = c.j == 6 && a.l16 >= 512u;
        st128(v4u{z ? 0u : __float_as_uint(t[0]), c.tagA, z ? 0u : __float_as_uint(t[1]), c.tagB}, c.rws, z ? a.l16 : tok32(a), a.sto + HALF * ST_HG + KIND * 4096 + c.j * 512);
    }
}
template <bool DO> DI void stat_pub_if(const Ctx& c, const Ad& a) { if constexpr (DO) stat_pub<0, 0>(c, a, L_S2, 0); }
struct Gath { v4u g[4]; };
template <int HALF, int KIND, int I> DI void gath_ld(const Ctx& c, const Ad& a, Gath& g) {
    g.g[I] = ld128(c.rws, a.l16 + I * 1024, a.sto + HALF * ST_HG + KIND * 4096);
}
DI bool gath_bad(const Ctx& c, const Gath& g) {
    u32 d = 0;
#pragma unroll
    for (int i = 0; i < 4; ++i) d = tag_bad(g.g[i], c.tagA, c.tagB, d);
    return ANY(d != 0u);
}
template <int HALF, int KIND> DI void gath_wait(const Ctx& c, const Ad& a, Gath& g) {
    if (gath_bad(c, g)) {
        unsigned spins = 0;
        NOUNROLL do { MEMBAR(); sfor<0, 4>([&](auto IC) { gath_ld<HALF, KIND, decltype(IC)::value>(c, a, g); }); } while (gath_bad(c, g) && !spin_fail(c, spins, 2u));
    }
}
DI void gath_ln(const Gath& g, float& m, float& r) {
    const float t1 = (__uint_as_float(g.g[0].x) + __uint_as_float(g.g[1].x)) + (__uint_as_float(g.g[2].x) + __uint_as_float(g.g[3].x));
    const float t2 = (__uint_as_float(g.g[0].z) + __uint_as_float(g.g[1].z)) + (__uint_as_float(g.g[2].z) + __uint_as_float(g.g[3].z));
    ln_scalars(hsum(t1), hsum(t2), m, r);
}
DI f32x4 epi_cs(const Ctx& c, const Ad& a, int E) { return *(LAS f32x4*)(c.L + a.ca + E * 32); }
template <int HALF, int MLP, int E>
DI void g1_epi(const Ctx& c, const Ad& a, G1Out& o, f32x4& cs, v4u& fr) {
    constexpr int t = E >> 2, r0 = 4 * (E & 3), g = E & 1;
    constexpr bool neg = (g ^ MLP) != 0;
    float h[4];
    LV(cs); LA(o.acc[t]);
#pragma unroll
    for (int e = 0; e < 4; ++e) h[e] = __builtin_fmaf(-o.mb, cs[e], o.acc[t][r0 + e]);
#pragma unroll
    for (int e = 0; e < 2; ++e) {
        const u32 p = __builtin_bit_cast(u32, __builtin_elementwise_max(__builtin_bit_cast(s16x2, pk(h[2 * e], h[2 * e + 1])), s16x2{0, 0}));
        fr[2 * g + e] = neg ? (p | SGN) : p;
    }
    if (g == 0) LV(fr);
}
template <int HALF, int K> DI void h_store(const Ctx& c, const Ad& a, const v4u& fr) { st128<K * 1024>(fr, c.rws, a.l16, a.hst + HALF * 114688); }

constexpr int NW1 = 2;
constexpr int SLP1 = 16;
constexpr int HR = 10, GL = 14, GC = 20;
template <int MLP> DI bool h_bad(const v4u& a, const v4u& b) {
    const u32 lo = MLP == 0 ? (a.x | a.y | b.x | b.y) : ~(a.x & a.y & b.x & b.y);
    const u32 hi = MLP == 0 ? ~(a.z & a.w & b.z & b.w) : (a.z | a.w | b.z | b.w);
    return ((lo | hi) & SGN) != 0u;
}
DI u32 h_base(const Ctx& c, int half) { return OFF_H + ((c.grp * 2 + half) * 112 + 4 * c.w) * 1024; }
template <bool ASM> DI v2u gate_ld(const Ctx& c, const Ad& a, int half) {
    const u32 vo = min(a.l16, 96u) * 1024, so = a.hst - c.j * 16384 + half * 114688 + 4;
    if constexpr (ASM) { v2u r; asm volatile("buffer_load_dwordx2 %0, %1, %2, %3 offen sc1" : "=v"(r) : "v"(vo), "s"(c.rws), "s"(so) : "memory"); return r; }
    else return __builtin_amdgcn_raw_buffer_load_b64(c.rws, vo, so, AUX_SC1);
}
template <int MLP> DI bool gate_bad(v2u x) { return ((MLP == 0 ? (x.x | ~x.y) : (~x.x | x.y)) & SGN) != 0u; }
template <int MLP> DI void gate_wait(const Ctx& c, const Ad& a, int half, v2u x) {
    if (ANY(gate_bad<MLP>(x))) {
        unsigned spins = 0;
        NOUNROLL do { MEMBAR(); x = gate_ld<false>(c, a, half); } while (ANY(gate_bad<MLP>(x)) && !spin_fail(c, spins, 1u));
    }
}
template <int N> DI v4u h_next(const Ctx& c, Ad& a) {
    constexpr int f = N >= 28 ? N - 28 : N;
    if constexpr (N >= 32 && N < 38) a.so[k_mem(f)] += 114688u;
    v4u r = __builtin_amdgcn_raw_buffer_load_b128(c.rws, a.l16 + k_sub(f) * 1024, a.so[k_mem(f)], AUX_SC1);
    if constexpr (N >= 50) a.so[k_mem(f)] -= 114688u;
    return r;
}
template <int MLP, int F_> DI void h_check(const Ctx& c0, v4u& a, v4u& b) {
    if (__builtin_expect(ANY(h_bad<MLP>(a, b)), 0)) {
        const Ctx c = fresh(c0);
        constexpr int half = F_ >= 28, f = F_ - 28 * half;
        const u32 vo = lane_id() * 16 + k_sub(f) * 1024, s0 = h_base(c, half) + member(c, k_mem(f)) * 16384, s1 = h_base(c, half) + member(c, k_mem(f + 1)) * 16384;
        unsigned spins = 0;
        NOUNROLL do {
            MEMBAR();
            a = ld128(c.rws, vo, s0); b = ld128(c.rws, vo, s1);
        } while (ANY(h_bad<MLP>(a, b)) && !spin_fail(c, spins, 3u));
    }
}
template <int HALF, int P> DI void red_write(const Ctx& c, const Ad& a, const AccT& acc) {
    LAS char* dst = c.L + (a.l16 + (P ^ c.w) * 6144) + (L_X + (1 - HALF) * 28672 + (P - 1) * 2048);
    const f32x16& x = acc[P >> 1];
    constexpr int r0 = (P & 1) * 8;
    *(LAS f32x4*)dst = f32x4{x[r0], x[r0 + 1], x[r0 + 2], x[r0 + 3]};
    *(LAS f32x4*)(dst + 1024) = f32x4{x[r0 + 4], x[r0 + 5], x[r0 + 6], x[r0 + 7]};
}
template <int HALF> DI void red_read(const Ctx& c, const Ad& a, f32x4 (&t)[6]) {
    LAS char* src = c.L + (a.l16 + c.w * 6144) + (L_X + (1 - HALF) * 28672);
#pragma unroll
    for (int i = 0; i < 6; ++i) t[i] = *(LAS f32x4*)(src + i * 1024);
}
template <int K> DI void post_half(const f32x4& o, float r, v4u& xres) {
#pragma unroll
    for (int e = 0; e < 2; ++e) xres[2 * K + e] = pk(__builtin_fmaf(o[2 * e], r, bflo(xres[2 * K + e])), __builtin_fmaf(o[2 * e + 1], r, bfhi(xres[2 * K + e])));
}
template <int HALF> DI void out_store(const Ctx& c, const Ad& a, const v4u& x, __amdgpu_buffer_rsrc_t rout) {
    auto r0 = __builtin_amdgcn_permlane32_swap(x.x, x.z, false, false);
    auto r1 = __builtin_amdgcn_permlane32_swap(x.y, x.w, false, false);
    const v4u v = {r0[0], r1[0], r0[1], r1[1]};
    const u32 vo = tok32(a) * (D / 8) + (a.l16 >> 9) * 16, so = (u32)((c.grp * 64 + HALF * 32) * D + 64 * c.j + 16 * c.w) * 2;
    asm volatile("buffer_store_dwordx4 %0, %1, %2, %3 offen\n\ts_nop 1" :: "v"(v), "v"(vo), "s"(rout), "s"(so) : "memory");
}
template <int HALF, int MLP> DI v4u xres_ld(const Ctx& c, const Ad& a) {
    if constexpr (MLP == 0) return trd2(c.L + (xtr_of(a.l16 >> 4) + (4 * c.j + c.w) * 1024) + (L_X + HALF * 28672), 512);
    else return *(LAS v4u*)(c.L + (a.l16 + (4 * c.j + c.w) * 1024) + (L_X + HALF * 28672));
}
template <int HALF> DI v4u y_ld(const Ctx& c, const Ad& a) { return trd2(c.L + (xtr_of(a.l16 >> 4) + c.w * 1024) + (L_X + HALF * 28672 + 24576), 512); }
DI void x3_make(v4u& x, const v4u& yf, float m, float r) {
    const float n = -m * r;
#pragma unroll
    for (int e = 0; e < 4; ++e) {
        const float p = bflo(x[e]), q = bfhi(x[e]);
        x[e] = pk(__builtin_fmaf(__builtin_fmaf(p, r, n), bflo(yf[e]), p), __builtin_fmaf(__builtin_fmaf(q, r, n), bfhi(yf[e]), q));
    }
}
template <int HALF> DI void x3_store(const Ctx& c, const Ad& a, const v4u& x) { st128(x, c.rws, a.l16, a.x3o + HALF * 28672); }
template <int HALF, int I> DI v4u x3_ld1(const Ctx& c, const Ad& a) {
    return ld128(c.rws, a.l16 + (I & 3) * 1024, a.x3o + (6 * c.w - 4 * c.j + (I & 4)) * 1024 + HALF * 28672);
}
DI u32 max3u(u32 a, u32 b, u32 c) { u32 r; asm("v_max3_u32 %0, %1, %2, %3" : "=v"(r) : "v"(a), "v"(b), "v"(c)); return r; }
DI bool x3_bad(const v4u (&t)[7]) {
    u32 mx = 0;
#pragma unroll
    for (int i = 0; i < 7; ++i) mx = max3u(max3u(mx, t[i].x, t[i].y), t[i].z, t[i].w);
    return ANY(mx == ~0u);
}
template <int HALF> DI void x3_wait(const Ctx& c, const Ad& a, v4u (&t)[7]) {
    if (x3_bad(t)) {
        unsigned spins = 0;
        NOUNROLL do { MEMBAR(); sfor<0, 7>([&](auto IC) { t[decltype(IC)::value] = x3_ld1<HALF, decltype(IC)::value>(c, a); }); } while (x3_bad(t) && !spin_fail(c, spins, 4u));
    }
}
template <int HALF, int I> DI void x3_put1(const Ctx& c, const Ad& a, const v4u& t) { *(LAS v4u*)(c.L + (a.l16 + c.w * 7168) + (L_X + HALF * 28672 + I * 1024)) = t; }

template <int MLP>
DI void g2_pair(const Ctx& c, Ad& a, const W2T& W2, v4u (&hb)[56], G1Out& gb, f32x4 (&csq)[3], float r0, v4u (&xres)[2], __amdgpu_buffer_rsrc_t out) {
    const u32 wl0 = wl2_of(c, a.l16 >> 4), wl1 = wl0 ^ 64u;
    AccT acc[2];
    v2u gate = {0, 0};
    v4u wl[2], fr, yf;
    f32x4 o[2], u[2];
    float s1, s2;
    Gath gs;
    const float ra_ = MLP ? -r0 : r0, rb_ = MLP ? -gb.r : gb.r;
    auto wres = [&](int slot, int k) { return trd2(c.L + (k ? wl1 : wl0) + slot * 2048, 1024); };
    auto fetch = [&](auto NC) {
        constexpr int N = decltype(NC)::value;
        if constexpr (N < 56) {
            if constexpr (N >= 28 && N < 32) hb[N] = *(LAS v4u*)(c.L + (a.l16 + c.w * 4096) + (L_X + (N - 28) * 1024));
            else hb[N] = h_next<N>(c, a);
        }
    };
    auto rd = [&](int i) { return *(LAS f32x4*)(c.L + (a.l16 + c.w * 6144) + (L_X + 28672 + i * 1024)); };
    auto own = [&](int k) { return f32x4{acc[0][0][4 * k], acc[0][0][4 * k + 1], acc[0][0][4 * k + 2], acc[0][0][4 * k + 3]}; };
    wl[0] = wres(8, 0); wl[1] = wres(8, 1);
    SB();
    sfor<0, 28>([&](auto PC) {
        constexpr int F_ = 2 * decltype(PC)::value, H = F_ >= 28, f = F_ - 28 * H;
        constexpr bool res = f < 24;
        constexpr int r0_ = f < 24 ? f >> 1 : f - 12;
        if constexpr (F_ == 24 || F_ == 30 || (MLP == 0 && F_ == 36)) { bar(); SB(); stat_pub_if<MLP == 0 && F_ == 36>(c, a); }
        asm volatile("" : "+v"(a.l16), "+v"(a.ca));
        acc[H][0] = mfma32(W2[r0_][0], hb[F_], f == 0 ? zero16() : acc[H][0]);
        acc[H][1] = mfma32(W2[r0_][1], hb[F_], f == 0 ? zero16() : acc[H][1]);
        fetch(std::integral_constant<int, F_ + (F_ < 8 ? HR - 2 : HR)>{});
        fetch(std::integral_constant<int, F_ + 1 + (F_ < 8 ? HR - 2 : HR)>{});
        if constexpr (F_ == 6) { fetch(std::integral_constant<int, F_ + HR>{}); fetch(std::integral_constant<int, F_ + HR + 1>{}); }
        if constexpr (F_ < 6) csq[(F_ + 2) % 3] = epi_cs(c, a, F_ + 2);
        if constexpr (F_ < 8) g1_epi<1, MLP, F_>(c, a, gb, csq[F_ % 3], fr);
        if constexpr (F_ < 6) csq[F_ % 3] = epi_cs(c, a, F_ + 3);
        if constexpr (F_ < 8) g1_epi<1, MLP, F_ + 1>(c, a, gb, csq[(F_ + 1) % 3], fr);
        if constexpr (F_ == 32) { LA(acc[0][0]); LV(o[0]); LV(u[0]); LV(u[1]); o[0] = (o[0] + own(0)) + (u[0] + u[1]); post_half<0>(o[0], ra_, xres[0]); LV(xres[0]); }
        if constexpr (F_ == 34) { LA(acc[0][0]); LV(o[0]); LV(u[0]); LV(u[1]); o[0] = (o[0] + own(1)) + (u[0] + u[1]); post_half<1>(o[0], ra_, xres[0]); LV(xres[0]); }
        acc[H][0] = mfma32(res ? wl[0] : W2[r0_ + 1][0], hb[F_ + 1], acc[H][0]);
        acc[H][1] = mfma32((res || f == 26) ? wl[1] : W2[r0_ + 1][1], hb[F_ + 1], acc[H][1]);
        if constexpr (f < 22) { wl[0] = wres((9 + (f >> 1)) % 12, 0); wl[1] = wres((9 + (f >> 1)) % 12, 1); }
        if constexpr (f == 24) wl[1] = *(LAS v4u*)(c.L + (a.l16 + c.w * 1024) + L_JK);
        if constexpr (F_ == 26) { wl[0] = wres(8, 0); wl[1] = wres(8, 1); }
        if constexpr (F_ == 22) xres[1] = xres_ld<1, MLP>(c, a);
        if constexpr (F_ == 30) { u[0] = rd(0); u[1] = rd(2); o[0] = rd(4); }
        if constexpr (F_ == 32) { u[0] = rd(1); u[1] = rd(3); o[0] = rd(5); }
        if constexpr (MLP == 0 && F_ == 54) yf = y_ld<0>(c, a);
        if constexpr (F_ == GL) gate = gate_ld<false>(c, a, 1);
        if constexpr (F_ < 8) { *(LAS v4u*)(c.L + (a.l16 + c.w * 4096) + (L_X + (F_ / 2) * 1024)) = fr; h_store<1, F_ / 2>(c, a, fr); }
        if constexpr (MLP == 0 && (F_ == 24 || F_ == 26))
            dma16<true>(c.lbase + L_X + (F_ - 24) / 2 * 28672 + 24576 + c.w * 1024, a.l16, c.rws, OFF_YT + (c.grp * 2 + (F_ - 24) / 2) * 28672 + c.j * 4096 + c.w * 1024);
        if constexpr (F_ == 28) { red_write<0, 1>(c, a, acc[0]); red_write<0, 2>(c, a, acc[0]); red_write<0, 3>(c, a, acc[0]); }
        if constexpr (MLP == 0 && F_ == 34) { frag_sums(xres[0], s1, s2); part_put(c, a, L_S2, s1, s2); }
        if constexpr (MLP == 1 && F_ == 36) out_store<0>(c, a, xres[0], out);
        ilv<4, 5>();
        if constexpr (F_ + 2 < 56 && f + 2 != 28 && f + 2 >= 4) h_check<MLP, F_ + 2>(c, hb[F_ + 2], hb[F_ + 3]);
        if constexpr (F_ == GC) gate_wait<MLP>(c, a, 1, gate);
        SB();
    });
    static_assert(GC + 2 + HR <= 32 && (GC & 1) == 0 && (HR & 1) == 0, "half B's first foreign fragment is requested behind the gate");

    red_write<1, 1>(c, a, acc[1]); red_write<1, 2>(c, a, acc[1]); red_write<1, 3>(c, a, acc[1]);
    if constexpr (MLP == 0) sfor<0, 4>([&](auto IC) { gath_ld<0, 0, decltype(IC)::value>(c, a, gs); });
    bar();
    f32x4 t[6];
    red_read<1>(c, a, t);
    o[0] = (f32x4{acc[1][0][0], acc[1][0][1], acc[1][0][2], acc[1][0][3]} + t[0]) + (t[2] + t[4]);
    o[1] = (f32x4{acc[1][0][4], acc[1][0][5], acc[1][0][6], acc[1][0][7]} + t[1]) + (t[3] + t[5]);
    post_half<0>(o[0], rb_, xres[1]); post_half<1>(o[1], rb_, xres[1]);
    if constexpr (MLP == 0) {
        frag_sums(xres[1], s1, s2); part_put(c, a, L_S2, s1, s2);

        bar();
        stat_pub<1, 0>(c, a, L_S2, 1);

        gath_wait<0, 0>(c, a, gs);
        gath_ln(gs, s1, s2);
        x3_make(xres[0], yf, s1, s2);
        x3_store<0>(c, a, xres[0]);

        yf = y_ld<1>(c, a);
        __builtin_amdgcn_s_sleep(SLP1);
        v4u x[7];
        unsigned spins = 0;
        NOUNROLL do {
            MEMBAR();
            sfor<0, 4>([&](auto IC) { gath_ld<1, 0, decltype(IC)::value>(c, a, gs); });
            sfor<0, 7>([&](auto IC) { x[decltype(IC)::value] = x3_ld1<0, decltype(IC)::value>(c, a); });
        } while ((gath_bad(c, gs) || x3_bad(x)) && !spin_fail(c, spins, 4u));

        gath_ln(gs, s1, s2);
        x3_make(xres[1], yf, s1, s2);
        x3_store<1>(c, a, xres[1]);
        sfor<0, 7>([&](auto IC) { x3_put1<0, decltype(IC)::value>(c, a, x[decltype(IC)::value]); });
    } else out_store<1>(c, a, xres[1], out);
}

__global__ void __launch_bounds__(256, 1) fused_kernel(const u16* __restrict__ x0, const u16* __restrict__ kin, const u16* __restrict__ vin,
                                                       const u16* __restrict__ sk, const u16* __restrict__ hk, const u16* __restrict__ Dv,
                                                       const u16* __restrict__ w1, const u16* __restrict__ w2,
                                                       unsigned char* __restrict__ ws, u16* __restrict__ out) {
    extern __shared__ __attribute__((aligned(16))) char smem[];
    Ctx c;
    c.L = (LAS char*)smem;
    c.lbase = (u32)(uintptr_t)c.L;
    const int b = blockIdx.x, tid = threadIdx.x;
    c.w = __builtin_amdgcn_readfirstlane(tid >> 6);
    c.rws = mkrs(ws, WS_BYTES);
    c.tmo = (GAS u32*)(ws + 256);

    __amdgpu_buffer_rsrc_t rcode;
    {
        unsigned long long pc; u32 len;
        asm volatile("s_getpc_b64 %0\n.Lfk_begin:\n\ts_mov_b32 %1, .Lfk_end-.Lfk_begin" : "=s"(pc), "=s"(len));
        rcode = mkrs((const void*)pc, len);
    }
    if (b < 32) {
        if (c.w < 2) touch(c.lbase + L_JK, (tid & 63) * 128, rcode, (((b >> 3) * 2 + c.w) & 7) * 8192);
        const v2u ep = __builtin_amdgcn_raw_buffer_load_b64(c.rws, 0, OFF_EP, AUX_SC1);
        v4u xr[2][7];
#pragma unroll
        for (int h = 0; h < 2; ++h)
#pragma unroll
            for (int i = 0; i < 7; ++i) xr[h][i] = *(const v4u*)(x0 + (b * 64 + 2 * (tid >> 3) + h) * D + (i * 8 + (tid & 7)) * 8);
        MEMBAR();
        u32 res[2];
#pragma unroll
        for (int h = 0; h < 2; ++h) {
            float s1 = 0.f, s2 = 0.f;
#pragma unroll
            for (int i = 0; i < 7; ++i)
#pragma unroll
                for (int e = 0; e < 4; ++e) { s1 = dot2(xr[h][i][e], 0x3f803f80u, s1); s2 = dot2(xr[h][i][e], xr[h][i][e], s2); }
#pragma unroll
            for (int o = 1; o < 8; o <<= 1) { s1 += __shfl_xor(s1, o); s2 += __shfl_xor(s2, o); }
            const float m = s1 * (1.0f / D);
            res[h] = pk(m, ln_sd(fmaxf(__builtin_fmaf(-m, m, s2 * (1.0f / D)), 0.f)));
        }
        if ((tid & 7) == 0) st128(v4u{res[0], ep.x + 1u, res[1], ep.y + 1u}, c.rws, (u32)(b * 32 + (tid >> 3)) * 16, OFF_ST0);
        const int rg = tid >> 3, c8 = min(tid & 7, 6);
        v4u wr[14];
#pragma unroll
        for (int i = 0; i < 14; ++i) wr[i] = *(const v4u*)(w1 + (rg * 14 + i) * F + b * 56 + c8 * 8);
        float cs[8] = {0, 0, 0, 0, 0, 0, 0, 0};
#pragma unroll
        for (int i = 0; i < 14; ++i)
#pragma unroll
            for (int e = 0; e < 4; ++e) { cs[2 * e] += bflo(wr[i][e]); cs[2 * e + 1] += bfhi(wr[i][e]); }
        LAS float* sf = (LAS float*)c.L;
#pragma unroll
        for (int e = 0; e < 8; ++e) sf[rg * 65 + (tid & 7) * 8 + e] = cs[e];
        bar();
        if (tid < 56) {
            float t = 0.f;
#pragma unroll
            for (int i = 0; i < 32; ++i) t += sf[i * 65 + tid];
            const float t2 = __shfl_down(t, 1);
            if ((tid & 1) == 0) st128(v4u{__float_as_uint(t), ep.x + 1u, __float_as_uint(t2), ep.y + 1u}, c.rws, (u32)(b * 28 + (tid >> 1)) * 16, OFF_CSG);
        }
        WAIT_VM(0);
        bar();
        {
            const __amdgpu_buffer_rsrc_t rw2 = mkrs(w2, D * F * 2);
#pragma unroll
            for (int i = 0; i < 13; ++i) touch(c.lbase + L_JK, (tid & 63) * 128, rw2, (u32)(i * 16 + (b >> 3) * 4 + c.w) * 8192);
        }
        WAIT_VM(0);
        return;
    }
    const int wb = b - 32;
    { const int xcd = wb & 7, idx = wb >> 3; c.grp = xcd * 4 + idx / 7; c.j = idx % 7; }
    c.gi = c.grp & 3;
    const int w = c.w;
    c.rw1 = mkrs(w1, D * F * 2); c.rw2 = mkrs(w2, D * F * 2);
    c.kb0 = (w + c.gi + 2) & 3;
    const int cp = (wb & 7) * 28 + (wb >> 3);
    W1T W1; W2T W2;

    u32 dval;
    {
        const int g = wb % 7, rb = wb / 7, lane = tid & 63;
        const int rg = (lane & 7) * 2 + ((lane >> 4) & 1), cl = ((lane >> 3) & 1) + 2 * (lane >> 5), cq = 4 * w + cl;
        const __amdgpu_buffer_rsrc_t rk = mkrs(kin, TSZ), rv = mkrs(vin, TSZ), rx = mkrs(x0, TSZ), rh = mkrs(hk, TSZ), rs_ = mkrs(sk, D * 8), rd = mkrs(Dv, D * 2);

        const u32 tb = (u32)((rb * 64) * 896 + g * 128);
        const u32 tv0 = (u32)((lane >> 3) * 896) + (((lane & 7) ^ ((lane >> 5) + 2 * w)) & 7) * 16;
        dma16<false>(c.lbase + T_X + w * 1024, tv0, rx, tb + w * 7168);
        dma16<false>(c.lbase + T_X + (w + 4) * 1024, tv0, rx, tb + (w + 4) * 7168);
        dma16<false>(c.lbase + T_K + w * 1024, tv0 + tb + (w - 1) * 7168, rk, 0u);
        dma16<false>(c.lbase + T_K + (w + 4) * 1024, tv0, rk, tb + (w + 3) * 7168);
        dma16<false>(c.lbase + T_V + w * 1024, tv0, rv, tb + w * 7168);
        dma16<false>(c.lbase + T_V + (w + 4) * 1024, tv0, rv, tb + (w + 4) * 7168);
        if (w == 0) dma16<false>(c.lbase + T_K + 8192, tv0, rk, tb + 7 * 7168);
        else dma16<false>(c.lbase + T_S + (w - 1) * 1024, (u32)lane * 16, rs_, g * 512);
        v4u ep4 = ld128_asm<true>(c.rws, 0u, OFF_EP);
        v4u he[2];
        he[0] = ld128_asm<false>(rh, tid * 16, cp * 8192); he[1] = ld128_asm<false>(rh, tid * 16, cp * 8192 + 4096);
        v4u dv4 = ld128_asm<false>(rd, 0u, cp * 4);
        {
            if (w == 0) touch(c.lbase + L_JK + 3072, (u32)lane * 524288u, c.rws, 4096u);
            else if (w < 3) touch(c.lbase + L_JK + 3072, (u32)lane * 262144u, w == 1 ? c.rw1 : c.rw2, 0u);
            else touch(c.lbase + L_JK + 3072, (u32)lane * 128u, rcode, (u32)((wb ^ (wb >> 3)) & 7) * 8192);
        }

        asm volatile("s_waitcnt vmcnt(4)" : "+v"(ep4) :: "memory");
        c.tagA = __builtin_amdgcn_readfirstlane(ep4.x) + 1u; c.tagB = __builtin_amdgcn_readfirstlane(ep4.y) + 1u;
        bar();

        const u32 a1 = (u32)(rg * 512 + (((cq >> 1) ^ rg) & 7) * 16 + (cq & 1) * 8);
        const u32 ak1 = (u32)(rg * 512 + 1024 + (((cq >> 1) ^ (rg + 2)) & 7) * 16 + (cq & 1) * 8);
        const u32 ak0 = (u32)(rg * 512 + 512 + (((cq >> 1) ^ (rg + 1)) & 7) * 16 + (cq & 1) * 8);
        v2u kr[7], vr[4], xr[4];
#pragma unroll
        for (int i = 0; i < 4; ++i) xr[i] = *(LAS v2u*)(c.L + T_X + a1 + i * 128);
        const v4u w01 = *(LAS v4u*)(c.L + T_S + cq * 32), w23 = *(LAS v4u*)(c.L + T_S + cq * 32 + 16);
#pragma unroll
        for (int i = 0; i < 3; ++i) kr[i] = *(LAS v2u*)(c.L + T_K + ak0 + (i + 1) * 128);
#pragma unroll
        for (int i = 0; i < 4; ++i) { kr[3 + i] = *(LAS v2u*)(c.L + T_K + ak1 + i * 128); vr[i] = *(LAS v2u*)(c.L + T_V + a1 + i * 128); }
#pragma unroll
        for (int h = 0; h < 2; ++h) {
            auto r0 = __builtin_amdgcn_permlane16_swap(__builtin_amdgcn_perm(xr[1][h], xr[0][h], 0x05040100u), __builtin_amdgcn_perm(xr[1][h], xr[0][h], 0x07060302u), false, false);
            auto r1 = __builtin_amdgcn_permlane16_swap(__builtin_amdgcn_perm(xr[3][h], xr[2][h], 0x05040100u), __builtin_amdgcn_perm(xr[3][h], xr[2][h], 0x07060302u), false, false);
            st128(v4u{r0[0], r1[0], r0[1], r1[1]}, c.rws, (u32)((cl * 4 + ((lane >> 4) & 1)) * 4096 + (lane & 7) * 16), OFF_X0G + (g * 64 + 16 * w + 2 * h) * 4096 + rb * 128);
        }

        v4u kg[4];
#pragma unroll
        for (int e = 0; e < 4; ++e) {
            const v4u& wq = e < 2 ? w01 : w23;
            const float w0 = bflo(wq[(e & 1) * 2]), w1_ = bfhi(wq[(e & 1) * 2]), w2_ = bflo(wq[(e & 1) * 2 + 1]), w3 = bfhi(wq[(e & 1) * 2 + 1]);
            float kk[7], o[4];
#pragma unroll
            for (int i = 0; i < 7; ++i) kk[i] = (e & 1) ? bfhi(kr[i][e >> 1]) : bflo(kr[i][e >> 1]);
#pragma unroll
            for (int i = 0; i < 4; i += 2) {
                u32 a_ = pk(kk[i] * w3, kk[i + 1] * w3), b_ = pk(kk[i + 1] * w2_, kk[i + 2] * w2_);
                a_ = pk(bflo(a_) + bflo(b_), bfhi(a_) + bfhi(b_)); b_ = pk(kk[i + 2] * w1_, kk[i + 3] * w1_);
                a_ = pk(bflo(a_) + bflo(b_), bfhi(a_) + bfhi(b_)); b_ = pk(kk[i + 3] * w0, kk[i + 4] * w0);
                a_ = pk(bflo(a_) + bflo(b_), bfhi(a_) + bfhi(b_));
                o[i] = bflo(a_) * ((e & 1) ? bfhi(vr[i][e >> 1]) : bflo(vr[i][e >> 1]));
                o[i + 1] = bfhi(a_) * ((e & 1) ? bfhi(vr[i + 1][e >> 1]) : bflo(vr[i + 1][e >> 1]));
            }
            kg[e] = v4u{pk(o[3], o[2]), c.tagA, pk(o[1], o[0]), c.tagB};
        }

        asm volatile("s_waitcnt vmcnt(0)\n\ts_barrier" : "+v"(he[0]), "+v"(he[1]), "+v"(dv4), "+v"(kg[0]), "+v"(kg[1]), "+v"(kg[2]), "+v"(kg[3]) :: "memory");

#pragma unroll
        for (int e = 0; e < 4; ++e) st128(kg[e], c.rws, (u32)(cl * 4 * 8192 + (15 - rg) * 16), OFF_KVG + ((g * 64 + 16 * w + e) * 512 + 496 - 16 * rb) * 16);
        {
            const WSt wt = w_st<0>(c, lane);
            sfor<0, NW1>([&](auto NC) { w_dma<decltype(NC)::value>(c, wt, w_soff<decltype(NC)::value>(c)); });
        }

        dval = (w >> 1) ? dv4.x >> 16 : dv4.x & 0xffffu;
#pragma unroll
        for (int cc = 0; cc < 2; ++cc) {
            LAS char* cb = c.L + CV_B + cc * CV_CH;
            *(LAS v4u*)(cb + CV_HE + 32 + tid * 16) = he[cc];
            if (tid < 2) { *(LAS v4u*)(cb + CV_HE + tid * 16) = v4u{0, 0, 0, 0}; *(LAS v4u*)(cb + CV_HO + tid * 16) = v4u{0, 0, 0, 0}; }
            if (tid < 32) *(LAS v4u*)(cb + CV_KV + 4096 + tid * 16) = v4u{0, 0, 0, 0};
        }
        bar();
#pragma unroll
        for (int cc = 0; cc < 2; ++cc) {
            const v4u e = he[cc];
            const u32 pv = *(LAS u32*)(c.L + CV_B + cc * CV_CH + CV_HE + 28 + tid * 16);
            *(LAS v4u*)(c.L + CV_B + cc * CV_CH + CV_HO + 32 + tid * 16) = v4u{__builtin_amdgcn_alignbit(e.x, pv, 16), __builtin_amdgcn_alignbit(e.y, e.x, 16),
                                                                            __builtin_amdgcn_alignbit(e.z, e.y, 16), __builtin_amdgcn_alignbit(e.w, e.z, 16)};
        }
        bar();

        SB();
    }
    {
        const int lane = lane_id(), i16 = lane & 15, q = lane >> 4, cc = w >> 1, p = w & 1, ch = 2 * cp + cc;
        LAS char* cb = c.L + CV_B + cc * CV_CH;
        const int o0 = i16 + 1 + 32 * p + 8 * q;
        LAS char* ab = cb + ((o0 & 1) ? CV_HO + 4 : CV_HE) + (o0 >> 1) * 4;
        v4u A[32];
        v4u AD;
        {
            const int jj = p ? -1 : 15 - i16 - 8 * q;
            const u32 dv = dval << ((jj & 1) * 16);
#pragma unroll
            for (int d = 0; d < 4; ++d) AD[d] = (jj >= 0 && (jj >> 1) == d) ? dv : 0u;
        }
        const Ctx cs_ = fresh(c);
        const u32 wl = wl1_of(cs_, lane);
        const WSt wt = w_st<0>(cs_, lane);
        v4u sg[4], g[4];
        SB();
        sfor<0, 8>([&](auto NC) {
            constexpr int n = decltype(NC)::value;
#pragma unroll
            for (int t = n * 4; t < n * 4 + 4; ++t) { LAS u32* pp = (LAS u32*)(ab + t * 128); A[t] = v4u{pp[0], pp[1], pp[2], pp[3]}; }
            SB();
        });

        {
            unsigned spins = 0;
            auto bad4 = [&](const v4u (&x)[4]) { u32 d = 0; for (int t = 0; t < 4; ++t) d = tag_bad(x[t], c.tagA, c.tagB, d); return ANY(d != 0u); };
            NOUNROLL do {
                MEMBAR();
#pragma unroll
                for (int i = 0; i < 4; ++i) g[i] = ld128(c.rws, tid * 16, OFF_KVG + cp * 16384 + i * 4096);
            } while (bad4(g) && !spin_fail(c, spins, 5u));

#pragma unroll
            for (int i = 0; i < 4; ++i) *(LAS v2u*)(c.L + CV_B + (i >> 1) * CV_CH + CV_KV + (tid + 256 * (i & 1)) * 8) = v2u{g[i].x, g[i].z};
        }
        bar();

        LAS char* bb = cb + CV_KV + (2032 - 16 * i16 + 32 * p + 8 * q) * 2;
        f32x4 acc[8];
        float xs[4][4], qs[4][4];
        v4u B[32];
        v2u xg[4];
        B[0] = *(LAS v4u*)(bb + 3 * 128); B[1] = *(LAS v4u*)(bb + 2 * 128); B[2] = *(LAS v4u*)(bb + 128);
        const u32 s4 = (u32)(256 * p + 4 * i16 + q), xo = OFF_X0G + ch * 4096;
        SB();
        sfor<0, 32>([&](auto OC) {
            constexpr int oi = decltype(OC)::value, om = oi - 3;
            constexpr int nm = 8 - (om > 0 ? (om + 3) / 4 : 0) + ((om >= 0 && (om & 3) == 0) ? 1 : 0);
            if constexpr (oi == 9) { bar(); SB(); }
            if constexpr (oi + 3 < 32) B[oi + 3] = *(LAS v4u*)(bb - (om + 3) * 128);
#pragma unroll
            for (int cb_ = 7; cb_ >= 0; --cb_)
                if (4 * cb_ - om >= 0) acc[cb_] = mfma16(A[4 * cb_ - om], B[oi], oi == 0 ? f32x4{0, 0, 0, 0} : acc[cb_]);
            if constexpr (om >= 0 && (om & 3) == 0) acc[om >> 2] = mfma16(AD, B[oi], acc[om >> 2]);
            if constexpr (oi < 2) { sg[2 * oi] = ld128_asm<true>(c.rws, tid * 16, OFF_ST0 + oi * 8192); sg[2 * oi + 1] = ld128_asm<true>(c.rws, tid * 16, OFF_ST0 + oi * 8192 + 4096); }
            if constexpr (oi == 2 || oi == 3) {
                asm volatile("buffer_load_dwordx2 %0, %2, %3, %4 offen offset:%5 sc1\n\tbuffer_load_dwordx2 %1, %2, %3, %4 offen offset:%6 sc1"
                             : "=&v"(xg[2 * oi - 4]), "=&v"(xg[2 * oi - 3]) : "v"(s4 * 8), "s"(c.rws), "s"(xo), "i"((oi - 2) * 1024), "i"((oi - 2) * 1024 + 512) : "memory");
            }
            if constexpr (oi < NW1) w_read1<oi>(cs_, wl, W1, W2);
            if constexpr (oi >= 4 && (oi & 1) == 0 && NW1 + (oi - 4) / 2 < 12) w_dma<NW1 + (oi - 4) / 2>(cs_, wt, w_soff<NW1 + (oi - 4) / 2>(cs_));
            if constexpr (oi == 7) {
                asm volatile("s_waitcnt vmcnt(4)" : "+v"(sg[0]), "+v"(sg[1]), "+v"(sg[2]), "+v"(sg[3]), "+v"(xg[0]), "+v"(xg[1]), "+v"(xg[2]), "+v"(xg[3]) :: "memory");
                auto bad = [&]() { u32 d = 0; for (int t = 0; t < 4; ++t) d = tag_bad(sg[t], c.tagA, c.tagB, d); return ANY(d != 0u); };
                if (bad()) {
                    unsigned spins = 0;
                    NOUNROLL do {
                        MEMBAR();
#pragma unroll
                        for (int t = 0; t < 4; ++t) sg[t] = ld128(c.rws, tid * 16, OFF_ST0 + t * 4096);
                    } while (bad() && !spin_fail(c, spins, 6u));
                }
#pragma unroll
                for (int t = 0; t < 4; ++t) *(LAS v2u*)(c.L + CV_ST + (tid + 256 * t) * 8) = v2u{sg[t].x, sg[t].z};
            }
            if constexpr (oi >= 9 && oi < 13) sg[oi - 9] = *(LAS v4u*)(c.L + CV_ST + s4 * 16 + (oi - 9) * 1024);
            if constexpr (oi >= 11 && oi < 19) {
                constexpr int t = (oi - 11) >> 1, hf = (oi - 11) & 1;
                LV(xg[t]); LV(sg[t]);
                const u32 xw = xg[t][hf];
                const u32 sa = sg[t][2 * hf], sb = sg[t][2 * hf + 1];
                xs[t][2 * hf] = bflo(xw); xs[t][2 * hf + 1] = bfhi(xw);
                qs[t][2 * hf] = (xs[t][2 * hf] - bflo(sa)) * __builtin_amdgcn_rcpf(bfhi(sa));
                qs[t][2 * hf + 1] = (xs[t][2 * hf + 1] - bflo(sb)) * __builtin_amdgcn_rcpf(bfhi(sb));
                asm volatile("" : "+v"(qs[t][2 * hf]), "+v"(qs[t][2 * hf + 1]));
            }
            if constexpr (oi == 23 || oi == 25) {
                const v4u ff = {~0u, ~0u, ~0u, ~0u};
                st128(ff, c.rws, lane * 16, OFF_X3 + (cs_.grp * 56 + (oi - 23) * 14 + 4 * cs_.j + cs_.w) * 1024);
            }
            ilv<nm, 2>();
            SB();
        });

        LAS char* ex = c.L;
        f32x4 fin[4];
        if (p == 0) {
#pragma unroll
            for (int t = 0; t < 4; ++t) { *(LAS f32x4*)(ex + w * 4096 + t * 1024 + lane * 16) = acc[4 + t]; fin[t] = acc[t]; }
        } else {
#pragma unroll
            for (int t = 0; t < 4; ++t) { *(LAS f32x4*)(ex + w * 4096 + t * 1024 + lane * 16) = acc[t]; fin[t] = acc[4 + t]; }
        }
        bar();

        const u32 toff = (u32)((32 * p + 8 * (q & 1) + (i16 >> 1)) * 28672 + ch * 64 + (i16 & 1) * 32 + (q >> 1) * 16);
        v2u y[4], x1[4];
#pragma unroll
        for (int t = 0; t < 4; ++t) {
            fin[t] += *(LAS f32x4*)(ex + (w ^ 1) * 4096 + t * 1024 + lane * 16);
            y[t] = v2u{pk(fin[t][0], fin[t][1]), pk(fin[t][2], fin[t][3])};
            x1[t] = v2u{pk(__builtin_fmaf(qs[t][0], bflo(y[t].x), xs[t][0]), __builtin_fmaf(qs[t][1], bfhi(y[t].x), xs[t][1])),
                        pk(__builtin_fmaf(qs[t][2], bflo(y[t].y), xs[t][2]), __builtin_fmaf(qs[t][3], bfhi(y[t].y), xs[t][3]))};
        }
        auto merge = [&](const v2u& a, const v2u& b_) {
            auto r0 = __builtin_amdgcn_permlane16_swap(a.x, b_.x, false, false);
            auto r1 = __builtin_amdgcn_permlane16_swap(a.y, b_.y, false, false);
            return v4u{r0[0], r1[0], r0[1], r1[1]};
        };
        st128(merge(x1[0], x1[1]), c.rws, toff, OFF_X1);
        st128(merge(x1[2], x1[3]), c.rws, toff, OFF_X1 + 16 * 28672);
        st128(merge(y[0], y[1]), c.rws, toff, OFF_YT);
        st128(merge(y[2], y[3]), c.rws, toff, OFF_YT + 16 * 28672);

        sfor<12, 12 + NW1>([&](auto NC) { w_dma<decltype(NC)::value>(cs_, wt, w_soff<decltype(NC)::value>(cs_)); });
        WAIT_VM(6 + 2 * NW1);
        sfor<NW1, 12>([&](auto NC) { w_read1<decltype(NC)::value>(cs_, wl, W1, W2); });
        WAIT_LGKM(0);
        sfor<12 + NW1, 20>([&](auto NC) { w_dma<decltype(NC)::value>(cs_, wt, w_soff<decltype(NC)::value>(cs_)); });

        WAIT_VM(16);
        bar();
        if (tid < 8) __builtin_amdgcn_raw_buffer_store_b64(v2u{c.tagA, c.tagB}, c.rws, (u32)(tid * 2048 + wb * 8), OFF_FL, AUX_SC1);
        MEMBAR();

        sfor<20, 24>([&](auto NC) { w_dma<decltype(NC)::value>(cs_, wt, w_soff<decltype(NC)::value>(cs_)); });
        WAIT_VM(12);
        sfor<12, 18>([&](auto NC) { w_read1<decltype(NC)::value>(cs_, wl, W1, W2); });
        WAIT_LGKM(0);
        sfor<24, 28>([&](auto NC) { w_dma<decltype(NC)::value>(cs_, wt, w_soff<decltype(NC)::value>(cs_)); });

    }
    Ad ad;
    ad.l16 = lane_id() * 16;
    ad.hst = OFF_H + (c.grp * 224 + 16 * c.j + 4 * w) * 1024;
    ad.x3o = OFF_X3 + (c.grp * 56 + 4 * c.j + w) * 1024;
    ad.sto = OFF_ST + c.grp * 2 * ST_HG;
    asm volatile("" : "+s"(ad.hst), "+s"(ad.x3o), "+s"(ad.sto));
    const u32 swl = wl2_of(c, ad.l16 >> 4);
    const WSt w2t = w_st<28>(c, ad.l16 >> 4);
    u32 wso[7];
#pragma unroll
    for (int m = 0; m < 7; ++m) {
        const int mm = member(c, m);
        wso[m] = (256 * mm + 64 * w) * (D * 2) + 128 * c.j; ad.so[m] = ad.hst + (mm - c.j) * 16384;
        asm volatile("" : "+s"(wso[m]), "+s"(ad.so[m]));
    }
    auto w2_dma = [&](auto NC) { constexpr int n = decltype(NC)::value; w_dma<n>(c, w2t, wso[k_mem(w2_step_of(n))] + k_sub(w2_step_of(n)) * 16 * (D * 2)); };
    {
        v4u cg = ld128_asm<true>(c.rws, tok32(ad), OFF_CSG + c.j * 2048 + w * 512);
        sfor<28, 30>(w2_dma);
        WAIT_VM(12);
        w_read<18, 24>(c, W1, W2);

        if (w == 0) {
            unsigned spins = 0;
            NOUNROLL for (;;) {
                MEMBAR();
                const v4u f0 = ld128(c.rws, ad.l16, OFF_FL + (wb & 7) * 2048);
                const v4u f1 = ld128(c.rws, 1024 + min(ad.l16, 752u), OFF_FL + (wb & 7) * 2048);
                const u32 d = xor_or(f0.x, c.tagA, xor_or(f0.z, c.tagA, xor_or(f1.x, c.tagA, xor_or(f1.z, c.tagA, tag_bad(f0, c.tagB, c.tagB, tag_bad(f1, c.tagB, c.tagB, 0u))))));
                if (!ANY(d != 0u)) break;
                if (spin_fail(c, spins, 7u)) break;
            }
            if (wb == 0 && ad.l16 == 0) __builtin_amdgcn_raw_buffer_store_b64(v2u{c.tagA, c.tagB}, c.rws, 0, OFF_EP, AUX_SC1);
        }
        bar();

        {
            const u32 xb = c.lbase + L_X + w * 7168, xs_ = OFF_X1 + c.grp * 57344 + w * 7168;
            sfor<0, 14>([&](auto IC) {
                constexpr int i = decltype(IC)::value, o = (i / 7) * 28672 + (i % 7) * 1024;
                dma16<true>(xb + o, ad.l16, c.rws, xs_ + o);
            });
        }

        WAIT_VM(14);
        w_read<24, 28>(c, W1, W2);
        w_read1<28>(c, swl, W1, W2); w_read1<29>(c, swl, W1, W2);
        w_neg<28>(W2); w_neg<29>(W2);
        LV(cg);
        if (ANY(tag_bad(cg, c.tagA, c.tagB, 0u) != 0u)) {
            unsigned spins = 0;
            NOUNROLL do { MEMBAR(); cg = ld128(c.rws, tok32(ad), OFF_CSG + c.j * 2048 + w * 512); } while (ANY(tag_bad(cg, c.tagA, c.tagB, 0u) != 0u) && !spin_fail(c, spins, 8u));
        }
        *(LAS v2u*)(c.L + L_CS + w * 256 + (tok32(ad) >> 1)) = v2u{cg.x, cg.z};
        WAIT_VM(7);
        bar();

    }

    pin_a<0, 25>(W1);
    v4u hb[56], xres[2];
    G1Out ga, gb;
    v2u gate;
    f32x4 csq[3];
    St1 st;
    ad.ca = L_CS + (w * 64 + (ad.l16 >> 9) * 4) * 4;
    using M0 = std::integral_constant<int, 0>; using M1 = std::integral_constant<int, 1>;
    {
        st.ad = xtr_of(ad.l16 >> 4) + w * 7168;
        auto stats = [&](auto MC, auto HC, auto FC, G1Out& o) {
            constexpr int H = decltype(HC)::value, f = decltype(FC)::value;
            constexpr bool TR = decltype(MC)::value == 0;
            if constexpr (f == 0) { st.s1 = 0.f; st.s2 = 0.f; }
            if constexpr (f >= 2 && f < 23) { constexpr int ph = (f - 2) % 3, k = (f - 2) / 3; st1_acc<k, ph * 3, ph == 2 ? 8 : ph * 3 + 3>(st); }
            if constexpr (f % 3 == 0 && f < 21) st1_read<TR, H, f / 3>(c, st);
            if constexpr (f == 23) part_put(c, ad, L_SP + H * 1024, st.s1, st.s2);
            if constexpr (f == 24) { SB(); asm volatile("s_waitcnt lgkmcnt(%0)\n\ts_barrier" :: "i"(TR ? 2 : 1) : "memory"); }
            if constexpr (f == 25) { const f32x2 t = part_get(c, ad, L_SP + H * 1024); ln_scalars(t[0], t[1], o.mb, o.r); asm volatile("" : "+v"(o.mb), "+v"(o.r)); }
        };
        auto stage = [&](auto GC_, auto XC) {
            constexpr int g = decltype(GC_)::value, extra = decltype(XC)::value;
            if constexpr (g >= 15 && g < 43 && (g & 1)) w_neg<30 + (g - 15) / 2>(W2);
            if constexpr (g == 41) *(LAS v4u*)(c.L + (ad.l16 + w * 1024) + L_JK) = W2[15][1];
            if constexpr (g >= 13 && g < 41 && (g & 1)) { WAIT_VM(12 + extra); w_read1<30 + (g - 13) / 2>(c, swl, W1, W2); }
            if constexpr (g < 52 && (g & 1) == 0) w2_dma(std::integral_constant<int, 30 + g / 2>{});
        };
        constexpr unsigned REG = 0x9AAAAAAu;
        g1_main<true, 0, REG>(c, ad, W1, ga.acc, [&](auto FC) {
            constexpr int f = decltype(FC)::value;
            stage(FC, M0{});
            stats(M0{}, M0{}, FC, ga);
            if constexpr (f >= 26) csq[f - 26] = epi_cs(c, ad, f - 26);
        });

        auto side_b = [&](auto MC, auto FC) {
            constexpr int f = decltype(FC)::value, M = decltype(MC)::value;
            if constexpr (f < 6) csq[(f + 2) % 3] = epi_cs(c, ad, f + 2);
            if constexpr (f < 8) g1_epi<0, M, f>(c, ad, ga, csq[f % 3], hb[f >> 1]);
            if constexpr (f < 8 && (f & 1)) h_store<0, f / 2>(c, ad, hb[f >> 1]);
            if constexpr (f == 18) gate = gate_ld<true>(c, ad, 0);
            if constexpr (f == 19) xres[0] = xres_ld<0, M>(c, ad);
            if constexpr (f == 23) {
                SB();
                asm volatile("s_waitcnt vmcnt(%1)" : "+v"(gate) : "i"(M == 0 ? 4 : 0) : "memory");
                gate_wait<M>(c, ad, 0, gate);
            }
            if constexpr (f >= 24) hb[f - 20] = h_next<f - 20>(c, ad);
            if constexpr (f >= 26) csq[f - 26] = epi_cs(c, ad, f - 26);
        };
        const u32 ra = L_W + w * 24576 + 1024 + ad.l16;
        v4u rm;
        g1_main<true, 1, REG>(c, ad, W1, gb.acc, [&](auto FC) {
            constexpr int f = decltype(FC)::value;
            stage(std::integral_constant<int, 28 + f>{}, std::integral_constant<int, (f < 8 ? f / 2 : 4)>{});
            if constexpr (f >= 16 && f < 28 && (f & 1) == 0) { LV(rm); *(LAS v4u*)(c.L + ra + ((8 + (f - 16) / 2) % 12) * 2048) = rm ^ SGN; }
            if constexpr (f >= 14 && f < 26 && (f & 1) == 0) { WAIT_VM(f < 24 ? 14 : 12); rm = *(LAS v4u*)(c.L + ra + ((8 + (f - 14) / 2) % 12) * 2048); }
            stats(M0{}, M1{}, FC, gb);
            side_b(M0{}, FC);
        });

        {
            WAIT_VM(4);
            v4u t[6];
#pragma unroll
            for (int i = 0; i < 6; ++i) t[i] = *(LAS v4u*)(c.L + ra + ((14 + i) % 12) * 2048);
#pragma unroll
            for (int i = 0; i < 6; ++i) *(LAS v4u*)(c.L + ra + ((14 + i) % 12) * 2048) = t[i] ^ SGN;
            SB();
        }
        pin_a<0, 25>(W1);

        g2_pair<0>(c, ad, W2, hb, gb, csq, ga.r, xres, c.rws);
        pin_a<0, 25>(W1);

        v4u t[7];
        st.ad = ad.l16 + w * 7168;

        bar();

        g1_main<false, 0, REG>(c, ad, W1, ga.acc, [&](auto FC) {
            constexpr int f = decltype(FC)::value;
            stats(M1{}, M0{}, FC, ga);
            if constexpr (f >= 12 && f < 19) t[f - 12] = x3_ld1<1, f - 12>(c, ad);
            if constexpr (f == 24) x3_wait<1>(c, ad, t);
            if constexpr (f >= 25) { x3_put1<1, 2 * (f - 25)>(c, ad, t[2 * (f - 25)]); x3_put1<1, 2 * (f - 25) + 1>(c, ad, t[2 * (f - 25) + 1]); }
            if constexpr (f == 27) x3_put1<1, 6>(c, ad, t[6]);
            if constexpr (f >= 26) csq[f - 26] = epi_cs(c, ad, f - 26);
        });

        bar();

        g1_main<false, 1, REG>(c, ad, W1, gb.acc, [&](auto FC) {
            stats(M1{}, M1{}, FC, gb);
            side_b(M1{}, FC);
        });

        const __attribute__((address_space(4))) char* ka = (const __attribute__((address_space(4))) char*)__builtin_amdgcn_kernarg_segment_ptr();
        asm volatile("" : "+s"(ka));
        g2_pair<1>(c, ad, W2, hb, gb, csq, ga.r, xres, mkrs(*(void* const __attribute__((address_space(4)))*)(ka + 72), TSZ));

    }
    asm volatile(".Lfk_end:");
}

extern "C" void kernel_launch(void* const* d_in, const int* in_sizes, int n_in,
                              void* d_out, int out_size, void* d_ws, size_t ws_size,
                              hipStream_t stream) {
    (void)hipFuncSetAttribute((const void*)fused_kernel, hipFuncAttributeMaxDynamicSharedMemorySize, L_TOT);
    fused_kernel<<<256, 256, L_TOT, stream>>>((const u16*)d_in[0], (const u16*)d_in[1], (const u16*)d_in[2], (const u16*)d_in[3], (const u16*)d_in[4],
                                              (const u16*)d_in[5], (const u16*)d_in[7], (const u16*)d_in[8], (unsigned char*)d_ws, (u16*)d_out);
}
```

```cpp
#include <hip/hip_runtime.h>
#include <stdint.h>
#include <type_traits>

typedef unsigned short u16;
typedef unsigned u32;
typedef u32 v4u __attribute__((ext_vector_type(4)));
typedef u32 v2u __attribute__((ext_vector_type(2)));
typedef float f32x2 __attribute__((ext_vector_type(2)));
typedef float f32x4 __attribute__((ext_vector_type(4)));
typedef float f32x16 __attribute__((ext_vector_type(16)));
typedef __bf16 bf16x8 __attribute__((ext_vector_type(8)));
typedef __bf16 bf16x2 __attribute__((ext_vector_type(2)));
typedef short s16x4 __attribute__((ext_vector_type(4)));
typedef short s16x2 __attribute__((ext_vector_type(2)));
typedef unsigned short u16x2 __attribute__((ext_vector_type(2)));
#define LAS __attribute__((address_space(3)))
#define GAS __attribute__((address_space(1)))
#define DI __device__ __forceinline__
#define RLX_AGENT __ATOMIC_RELAXED, __HIP_MEMORY_SCOPE_AGENT
#define SB() __builtin_amdgcn_sched_barrier(0)
#define NOUNROLL _Pragma("unroll 1")
#define MEMBAR() asm volatile("" ::: "memory")
template <class T> __device__ __forceinline__ void LV(T& x) { asm volatile("" : "+v"(x)); }
template <class T> __device__ __forceinline__ void LA(T& x) { asm volatile("" : "+a"(x)); }
#define ANY(x) (__builtin_amdgcn_ballot_w64(x) != 0ull)

constexpr int S = 2048, D = 448, F = 1792;
constexpr float EPS = 1e-5f;
constexpr u32 TSZ = S * D * 2;
constexpr u32 OFF_EP  = 0;
constexpr u32 OFF_FL  = 4096;
constexpr u32 OFF_ST0 = OFF_FL + 16384;
constexpr u32 OFF_CSG = OFF_ST0 + 16384;
constexpr u32 OFF_ST  = OFF_CSG + 16384;
constexpr u32 OFF_KVG = 1048576;
constexpr u32 OFF_X0G = OFF_KVG + 2 * TSZ;
constexpr u32 OFF_X1  = OFF_X0G + 2 * TSZ;
constexpr u32 OFF_YT  = OFF_X1 + TSZ;
constexpr u32 OFF_X3  = OFF_YT + TSZ;
constexpr u32 OFF_H   = OFF_X3 + TSZ;
constexpr u32 WS_BYTES = OFF_H + 64 * 112 * 1024;
static_assert(OFF_ST + 64 * 24 * 512 <= OFF_KVG, "ws map");
constexpr int RSRC_W3 = 0x00020000;
constexpr int AUX_SC1 = 16;
constexpr unsigned SPIN_CAP = 1u << 14;
constexpr u32 SGN = 0x80008000u;

template <int I, int N, class Fn> DI void sfor(Fn&& fn) { if constexpr (I < N) { fn(std::integral_constant<int, I>{}); sfor<I + 1, N>(fn); } }

DI float bflo(u32 x) { return __uint_as_float(x << 16); }
DI float bfhi(u32 x) { return __uint_as_float(x & 0xffff0000u); }
DI u32 pk(float a, float b) { f32x2 v = {a, b}; return __builtin_bit_cast(u32, __builtin_convertvector(v, bf16x2)); }
DI float rbf(float a) { return bflo(pk(a, a)); }
DI __amdgpu_buffer_rsrc_t mkrs(const void* p, u32 bytes) { return __builtin_amdgcn_make_buffer_rsrc((void*)p, 0, (int)bytes, RSRC_W3); }
DI f32x4 mfma16(v4u a, v4u b, f32x4 c) { return __builtin_amdgcn_mfma_f32_16x16x32_bf16(__builtin_bit_cast(bf16x8, a), __builtin_bit_cast(bf16x8, b), c, 0, 0, 0); }
DI f32x16 mfma32(v4u a, v4u b, f32x16 c) { return __builtin_amdgcn_mfma_f32_32x32x16_bf16(__builtin_bit_cast(bf16x8, a), __builtin_bit_cast(bf16x8, b), c, 0, 0, 0); }
DI f32x16 zero16() { f32x16 z; for (int i = 0; i < 16; ++i) z[i] = 0.f; return z; }
DI v2u trd(LAS char* p) { return __builtin_bit_cast(v2u, __builtin_amdgcn_ds_read_tr16_b64_v4i16((LAS s16x4*)p)); }
DI v4u trd2(LAS char* p, int second) { v2u a = trd(p), b = trd(p + second); return v4u{a.x, a.y, b.x, b.y}; }
DI float dot2(u32 a, u32 b, float c) { return __builtin_amdgcn_fdot2_f32_bf16(__builtin_bit_cast(bf16x2, a), __builtin_bit_cast(bf16x2, b), c, false); }
DI v4u ld128(__amdgpu_buffer_rsrc_t rs, u32 vo, u32 so) { return __builtin_amdgcn_raw_buffer_load_b128(rs, vo, so, AUX_SC1); }
template <bool SC1, int OFF = 0> DI v4u ld128_asm(__amdgpu_buffer_rsrc_t rs, u32 vo, u32 so) {
    v4u r;
    if constexpr (SC1) asm volatile("buffer_load_dwordx4 %0, %1, %2, %3 offen offset:%4 sc1" : "=v"(r) : "v"(vo), "s"(rs), "s"(so), "i"(OFF) : "memory");
    else asm volatile("buffer_load_dwordx4 %0, %1, %2, %3 offen offset:%4" : "=v"(r) : "v"(vo), "s"(rs), "s"(so), "i"(OFF) : "memory");
    return r;
}
template <int OFF = 0> DI void st128(v4u v, __amdgpu_buffer_rsrc_t rs, u32 vo, u32 so) {
    asm volatile("buffer_store_dwordx4 %0, %1, %2, %3 offen offset:%4 sc1\n\ts_nop 1" :: "v"(v), "v"(vo), "s"(rs), "s"(so), "i"(OFF) : "memory");
}
DI int lane_id() { int l; asm volatile("v_mbcnt_lo_u32_b32 %0, -1, 0\n\tv_mbcnt_hi_u32_b32 %0, -1, %0" : "=v"(l)); return l; }
DI float hsum(float a) {
    const u32 v = __float_as_uint(a);
    auto t = __builtin_amdgcn_permlane32_swap(v, v, false, false);
    return __uint_as_float(t[0]) + __uint_as_float(t[1]);
}
DI float ln_sd(float var) { return rbf(__builtin_amdgcn_sqrtf(rbf(rbf(var) + EPS))); }
DI void ln_scalars(float s1, float s2, float& m, float& r) {
    m = s1 * (1.0f / D);
    r = __builtin_amdgcn_rsqf(fmaxf(__builtin_fmaf(-m, m, s2 * (1.0f / D)), 0.f) + EPS);
}
DI u32 xor_or(u32 a, u32 b, u32 c) { return __builtin_amdgcn_bitop3_b32(a, b, c, 0xBE); }
DI u32 tag_bad(const v4u& g, u32 tA, u32 tB, u32 acc) { return xor_or(g.w, tB, xor_or(g.y, tA, acc)); }
template <bool SC1> DI void dma16(u32 ldsaddr, u32 voff, __amdgpu_buffer_rsrc_t rs, u32 soff) {
    if constexpr (SC1) asm volatile("s_mov_b32 m0, %0\n\ts_nop 0\n\tbuffer_load_dwordx4 %1, %2, %3 offen sc1 lds" :: "s"(ldsaddr), "v"(voff), "s"(rs), "s"(soff) : "memory", "m0");
    else asm volatile("s_mov_b32 m0, %0\n\ts_nop 0\n\tbuffer_load_dwordx4 %1, %2, %3 offen lds" :: "s"(ldsaddr), "v"(voff), "s"(rs), "s"(soff) : "memory", "m0");
}
template <int LI> DI void dma_pair(u32 ldsbase, u32 vo0, u32 vo1, __amdgpu_buffer_rsrc_t rs, u32 soff) {
    asm volatile("s_add_u32 m0, %0, %5\n\ts_nop 0\n\tbuffer_load_dwordx4 %1, %3, %4 offen lds\n\tbuffer_load_dwordx4 %2, %3, %4 offen offset:1024 lds"
                 :: "s"(ldsbase), "v"(vo0), "v"(vo1), "s"(rs), "s"(soff), "i"(LI) : "memory", "m0", "scc");
}
DI void touch(u32 junk, u32 voff, __amdgpu_buffer_rsrc_t rs, u32 soff) {
    asm volatile("s_mov_b32 m0, %0\n\ts_nop 0\n\tbuffer_load_dword %1, %2, %3 offen lds" :: "s"(junk), "v"(voff), "s"(rs), "s"(soff) : "memory", "m0");
}
#define WAIT_VM(n) asm volatile("s_waitcnt vmcnt(%0)" :: "i"(n) : "memory")
#define WAIT_LGKM(n) asm volatile("s_waitcnt lgkmcnt(%0)" :: "i"(n) : "memory")
DI void bar() { asm volatile("s_waitcnt lgkmcnt(0)\n\ts_barrier" ::: "memory"); }
DI int wrap7(int x) { return x >= 7 ? x - 7 : x; }
template <int N, int K> DI void ilv() { if constexpr (N > 0) { __builtin_amdgcn_sched_group_barrier(0x8, 1, 0); __builtin_amdgcn_sched_group_barrier(0x366, K, 0); ilv<N - 1, K>(); } }

constexpr int L_X = 0;
constexpr int L_W = 57344;
constexpr int L_M = L_W + 98304;
constexpr int L_SP = L_M;
constexpr int L_CS = L_M + 2048;
constexpr int L_S2 = L_M + 3072;
constexpr int L_JK = L_M + 4096;
constexpr int L_TOT = L_M + 8192;
constexpr int T_K = 0, T_V = 9216, T_X = 17408, T_S = L_JK;
constexpr int CV_B = 25600;
constexpr int CV_HE = 0, CV_HO = 4160, CV_KV = CV_HO + 4160, CV_CH = CV_KV + 4608;
constexpr int CV_ST = 16384;
static_assert(CV_B + 2 * CV_CH <= L_W, "lds map");

struct Ctx {
    LAS char* L;
    __amdgpu_buffer_rsrc_t rws, rw1, rw2;
    GAS u32* tmo;
    int w, j, grp, kb0, gi;
    u32 lbase, tagA, tagB;
};
DI Ctx fresh(const Ctx& c0) { Ctx c = c0; asm volatile("" : "+s"(c.w), "+s"(c.j), "+s"(c.grp), "+s"(c.kb0), "+s"(c.gi)); return c; }
DI bool spin_fail(const Ctx& c, unsigned& spins, u32 code) {
    if (++spins > SPIN_CAP) { __hip_atomic_store(c.tmo, code, RLX_AGENT); return true; }
    __builtin_amdgcn_s_sleep(1);
    return false;
}
DI u32 xtr_of(int lane) { return (u32)(((lane >> 5) * 4 + ((lane >> 2) & 3)) * 64 + ((lane >> 4) & 1) * 32 + (lane & 3) * 8); }
DI u32 wl1_of(const Ctx& c, int lane) {
    const int rr = (lane >> 5) * 4 + ((lane >> 2) & 3);
    return (u32)(L_W + c.w * 24576 + rr * 128 + ((rr >> 1) & 1) * 64 + ((lane >> 4) & 1) * 32 + (lane & 3) * 8);
}
DI u32 wl2_of(const Ctx& c, int lane) { return wl1_of(c, lane) ^ (u32)((c.w >> 1) * 64 + (c.w & 1) * 32); }
DI int member(const Ctx& c, int m) {
    if (m == 0) return c.j;
    int t = m - 1 + 2 * c.gi; t = t >= 6 ? t - 6 : t; t = t >= 6 ? t - 6 : t;
    return wrap7(c.j + 1 + t);
}
typedef v4u W1T[28][2];
typedef v4u W2T[16][2];
typedef f32x16 AccT[2];

DI constexpr int k_mem(int f) { return f < 4 ? 0 : 1 + (f - 4) % 6; }
DI constexpr int k_sub(int f) { return f < 4 ? f : (f - 4) / 6; }
DI constexpr int w2_step_of(int n) { return n < 40 ? 2 * (n - 28) : (n < 44 ? n - 16 : 2 * (n - 44) + 1); }
template <int N> DI u32 w_soff(const Ctx& c) {
    if constexpr (N < 28) return (N % 7 + 7 * ((N / 7 + c.kb0) & 3)) * 16 * (F * 2) + (256 * c.j + 64 * c.w) * 2;
    else return (256 * member(c, k_mem(w2_step_of(N))) + 64 * c.w + 16 * k_sub(w2_step_of(N))) * (D * 2) + 128 * c.j;
}
template <int N> DI u32 w_voff(int lane) { return (lane >> 3) * ((N < 28 ? F : D) * 2) + ((lane & 7) ^ (((lane >> 4) & 1) << 2)) * 16; }
struct WSt { u32 ring, vo0, vo1; };
template <int N> DI WSt w_st(const Ctx& c, int lane) { WSt t; t.ring = c.lbase + L_W + c.w * 24576; t.vo0 = w_voff<N>(lane); t.vo1 = t.vo0 + 8 * ((N < 28 ? F : D) * 2) - 1024; return t; }
template <int N> DI void w_dma(const Ctx& c, const WSt& t, u32 so) { dma_pair<(N % 12) * 2048>(t.ring, t.vo0, t.vo1, N < 28 ? c.rw1 : c.rw2, so); }
DI v4u w_frag(const Ctx& c, u32 wl, int slot, int t) { return trd2(c.L + (wl ^ (u32)(t * 64)) + slot * 2048, 1024); }
template <int N> DI void w_read1(const Ctx& c, u32 wl, W1T& W1, W2T& W2) {
#pragma unroll
    for (int t = 0; t < 2; ++t) {
        if constexpr (N < 28) W1[N][t] = w_frag(c, wl, N % 12, t);
        else W2[N - 28][t] = w_frag(c, wl, N % 12, t);
    }
}
template <int N> DI void w_neg(W2T& W2) {
#pragma unroll
    for (int t = 0; t < 2; ++t) { LV(W2[N - 28][t]); W2[N - 28][t].z ^= SGN; W2[N - 28][t].w ^= SGN; asm volatile("" : "+v"(W2[N - 28][t])); }
}
template <int N0, int N1> DI void w_read(const Ctx& c0, W1T& W1, W2T& W2) {
    const Ctx c = fresh(c0);
    const u32 wl = wl1_of(c, lane_id());
    sfor<N0, N1>([&](auto NC) { w_read1<decltype(NC)::value>(c, wl, W1, W2); });
}

template <int N0, int N1> DI void pin_a(W1T& W1) {
    if constexpr (N0 < N1) { asm volatile("" : "+a"(W1[N0][0]), "+a"(W1[N0][1])); pin_a<N0 + 1, N1>(W1); }
}
struct G1Out { AccT acc; float mb, r; };
DI constexpr bool reg_start(unsigned long long end, int f) { return f == 0 || ((end >> (f - 1)) & 1ull); }
DI constexpr int reg_len(unsigned long long end, int f) { int n = 1; while (!reg_start(end, f - n + 1)) ++n; return n; }
struct Ad {
    u32 l16;
    u32 ca;
    u32 hst;
    u32 x3o;
    u32 sto;
    u32 so[7];
};
constexpr u32 ST_HG = 3 * 4096;
DI u32 tok32(const Ad& a) { return a.l16 & 496u; }

template <bool TR, int HALF, unsigned END, class Side>
DI void g1_main(const Ctx& c, Ad& a, const W1T& W1, AccT& acc, Side&& side) {
    const u32 lp = TR ? xtr_of(a.l16 >> 4) : a.l16;
    int kb = c.kb0; asm volatile("" : "+s"(kb));
    u32 la = 0;
    auto ldb = [&](int f) {
        if (f % 7 == 0) la = lp + ((f / 7 + kb) & 3) * 7168;
        LAS char* p = c.L + la + (L_X + HALF * 28672 + (f % 7) * 1024);
        if constexpr (TR) return trd2(p, 512);
        else return *(LAS v4u*)p;
    };
    v4u B[28];
    B[0] = ldb(0); B[1] = ldb(1); B[2] = ldb(2);
    SB();
    sfor<0, 28>([&](auto FC) {
        constexpr int f = decltype(FC)::value;
        if constexpr (reg_start(END, f)) asm volatile("" : "+v"(a.l16), "+v"(a.ca));
        acc[0] = mfma32(W1[f][0], B[f], f == 0 ? zero16() : acc[0]);
        acc[1] = mfma32(W1[f][1], B[f], f == 0 ? zero16() : acc[1]);
        if constexpr (f + 3 < 28) B[f + 3] = ldb(f + 3);
        side(FC);
        if constexpr ((END >> f) & 1u) { ilv<2 * reg_len(END, f), 5>(); SB(); }
    });
}
struct St1 { u32 ad; v4u fr[2]; float s1, s2; };
template <bool TR, int HALF, int K> DI void st1_read(const Ctx& c, St1& s) {
    if constexpr (TR) s.fr[K & 1] = trd2(c.L + s.ad + (L_X + HALF * 28672 + K * 1024), 512);
    else s.fr[K & 1] = *(LAS v4u*)(c.L + s.ad + (L_X + HALF * 28672 + K * 1024));
}
template <int K, int E0, int E1> DI void st1_acc(St1& s) {
#pragma unroll
    for (int e = E0; e < E1; ++e) { if (e == E0) LV(s.fr[K & 1]); const u32 x = s.fr[K & 1][e >> 1]; if (e & 1) s.s2 = dot2(x, x, s.s2); else s.s1 = dot2(x, 0x3f803f80u, s.s1); }
}
DI void part_put(const Ctx& c, const Ad& a, int off, float s1, float s2) { *(LAS f32x2*)(c.L + off + tok32(a) * 2 + c.w * 8) = f32x2{hsum(s1), hsum(s2)}; }
DI f32x2 part_get(const Ctx& c, const Ad& a, int off) {
    const f32x4 u = *(LAS f32x4*)(c.L + off + tok32(a) * 2), v = *(LAS f32x4*)(c.L + off + tok32(a) * 2 + 16);
    return f32x2{(u[0] + u[2]) + (v[0] + v[2]), (u[1] + u[3]) + (v[1] + v[3])};
}
DI void frag_sums(const v4u& p, float& s1, float& s2) {
    s1 = 0.f; s2 = 0.f;
#pragma unroll
    for (int e = 0; e < 4; ++e) { s1 = dot2(p[e], 0x3f803f80u, s1); s2 = dot2(p[e], p[e], s2); }
}
template <int HALF, int KIND> DI void stat_pub(const Ctx& c, const Ad& a, int off, int who) {
    if (c.w == who) {
        f32x2 t = part_get(c, a, off);
        const bool z = c.j == 6 && a.l16 >= 512u;
        st128(v4u{z ? 0u : __float_as_uint(t[0]), c.tagA, z ? 0u : __float_as_uint(t[1]), c.tagB}, c.rws, z ? a.l16 : tok32(a), a.sto + HALF * ST_HG + KIND * 4096 + c.j * 512);
    }
}
template <bool DO> DI void stat_pub_if(const Ctx& c, const Ad& a) { if constexpr (DO) stat_pub<0, 0>(c, a, L_S2, 0); }
struct Gath { v4u g[4]; };
template <int HALF, int KIND, int I> DI void gath_ld(const Ctx& c, const Ad& a, Gath& g) {
    g.g[I] = ld128(c.rws, a.l16 + I * 1024, a.sto + HALF * ST_HG + KIND * 4096);
}
DI bool gath_bad(const Ctx& c, const Gath& g) {
    u32 d = 0;
#pragma unroll
    for (int i = 0; i < 4; ++i) d = tag_bad(g.g[i], c.tagA, c.tagB, d);
    return ANY(d != 0u);
}
template <int HALF, int KIND> DI void gath_wait(const Ctx& c, const Ad& a, Gath& g) {
    if (gath_bad(c, g)) {
        unsigned spins = 0;
        NOUNROLL do { MEMBAR(); sfor<0, 4>([&](auto IC) { gath_ld<HALF, KIND, decltype(IC)::value>(c, a, g); }); } while (gath_bad(c, g) && !spin_fail(c, spins, 2u));
    }
}
DI void gath_ln(const Gath& g, float& m, float& r) {
    const float t1 = (__uint_as_float(g.g[0].x) + __uint_as_float(g.g[1].x)) + (__uint_as_float(g.g[2].x) + __uint_as_float(g.g[3].x));
    const float t2 = (__uint_as_float(g.g[0].z) + __uint_as_float(g.g[1].z)) + (__uint_as_float(g.g[2].z) + __uint_as_float(g.g[3].z));
    ln_scalars(hsum(t1), hsum(t2), m, r);
}
DI f32x4 epi_cs(const Ctx& c, const Ad& a, int E) { return *(LAS f32x4*)(c.L + a.ca + E * 32); }
template <int HALF, int MLP, int E>
DI void g1_epi(const Ctx& c, const Ad& a, G1Out& o, f32x4& cs, v4u& fr) {
    constexpr int t = E >> 2, r0 = 4 * (E & 3), g = E & 1;
    constexpr bool neg = (g ^ MLP) != 0;
    float h[4];
    LV(cs); LA(o.acc[t]);
#pragma unroll
    for (int e = 0; e < 4; ++e) h[e] = __builtin_fmaf(-o.mb, cs[e], o.acc[t][r0 + e]);
#pragma unroll
    for (int e = 0; e < 2; ++e) {
        const u32 p = __builtin_bit_cast(u32, __builtin_elementwise_max(__builtin_bit_cast(s16x2, pk(h[2 * e], h[2 * e + 1])), s16x2{0, 0}));
        fr[2 * g + e] = neg ? (p | SGN) : p;
    }
    if (g == 0) LV(fr);
}
template <int HALF, int K> DI void h_store(const Ctx& c, const Ad& a, const v4u& fr) { st128<K * 1024>(fr, c.rws, a.l16, a.hst + HALF * 114688); }

constexpr int NW1 = 2;
constexpr int SLP1 = 16;
constexpr int HR = 10, GL = 14, GC = 20;
template <int MLP> DI bool h_bad(const v4u& a, const v4u& b) {
    const u32 lo = MLP == 0 ? (a.x | a.y | b.x | b.y) : ~(a.x & a.y & b.x & b.y);
    const u32 hi = MLP == 0 ? ~(a.z & a.w & b.z & b.w) : (a.z | a.w | b.z | b.w);
    return ((lo | hi) & SGN) != 0u;
}
DI u32 h_base(const Ctx& c, int half) { return OFF_H + ((c.grp * 2 + half) * 112 + 4 * c.w) * 1024; }
template <bool ASM> DI v2u gate_ld(const Ctx& c, const Ad& a, int half) {
    const u32 vo = min(a.l16, 96u) * 1024, so = a.hst - c.j * 16384 + half * 114688 + 4;
    if constexpr (ASM) { v2u r; asm volatile("buffer_load_dwordx2 %0, %1, %2, %3 offen sc1" : "=v"(r) : "v"(vo), "s"(c.rws), "s"(so) : "memory"); return r; }
    else return __builtin_amdgcn_raw_buffer_load_b64(c.rws, vo, so, AUX_SC1);
}
template <int MLP> DI bool gate_bad(v2u x) { return ((MLP == 0 ? (x.x | ~x.y) : (~x.x | x.y)) & SGN) != 0u; }
template <int MLP> DI void gate_wait(const Ctx& c, const Ad& a, int half, v2u x) {
    if (ANY(gate_bad<MLP>(x))) {
        unsigned spins = 0;
        NOUNROLL do { MEMBAR(); x = gate_ld<false>(c, a, half); } while (ANY(gate_bad<MLP>(x)) && !spin_fail(c, spins, 1u));
    }
}
template <int N> DI v4u h_next(const Ctx& c, Ad& a) {
    constexpr int f = N >= 28 ? N - 28 : N;
    if constexpr (N >= 32 && N < 38) a.so[k_mem(f)] += 114688u;
    v4u r = __builtin_amdgcn_raw_buffer_load_b128(c.rws, a.l16 + k_sub(f) * 1024, a.so[k_mem(f)], AUX_SC1);
    if constexpr (N >= 50) a.so[k_mem(f)] -= 114688u;
    return r;
}
template <int MLP, int F_> DI void h_check(const Ctx& c0, v4u& a, v4u& b) {
    if (__builtin_expect(ANY(h_bad<MLP>(a, b)), 0)) {
        const Ctx c = fresh(c0);
        constexpr int half = F_ >= 28, f = F_ - 28 * half;
        const u32 vo = lane_id() * 16 + k_sub(f) * 1024, s0 = h_base(c, half) + member(c, k_mem(f)) * 16384, s1 = h_base(c, half) + member(c, k_mem(f + 1)) * 16384;
        unsigned spins = 0;
        NOUNROLL do {
            MEMBAR();
            a = ld128(c.rws, vo, s0); b = ld128(c.rws, vo, s1);
        } while (ANY(h_bad<MLP>(a, b)) && !spin_fail(c, spins, 3u));
    }
}
template <int HALF, int P> DI void red_write(const Ctx& c, const Ad& a, const AccT& acc) {
    LAS char* dst = c.L + (a.l16 + (P ^ c.w) * 6144) + (L_X + (1 - HALF) * 28672 + (P - 1) * 2048);
    const f32x16& x = acc[P >> 1];
    constexpr int r0 = (P & 1) * 8;
    *(LAS f32x4*)dst = f32x4{x[r0], x[r0 + 1], x[r0 + 2], x[r0 + 3]};
    *(LAS f32x4*)(dst + 1024) = f32x4{x[r0 + 4], x[r0 + 5], x[r0 + 6], x[r0 + 7]};
}
template <int HALF> DI void red_read(const Ctx& c, const Ad& a, f32x4 (&t)[6]) {
    LAS char* src = c.L + (a.l16 + c.w * 6144) + (L_X + (1 - HALF) * 28672);
#pragma unroll
    for (int i = 0; i < 6; ++i) t[i] = *(LAS f32x4*)(src + i * 1024);
}
template <int K> DI void post_half(const f32x4& o, float r, v4u& xres) {
#pragma unroll
    for (int e = 0; e < 2; ++e) xres[2 * K + e] = pk(__builtin_fmaf(o[2 * e], r, bflo(xres[2 * K + e])), __builtin_fmaf(o[2 * e + 1], r, bfhi(xres[2 * K + e])));
}
template <int HALF> DI void out_store(const Ctx& c, const Ad& a, const v4u& x, __amdgpu_buffer_rsrc_t rout) {
    auto r0 = __builtin_amdgcn_permlane32_swap(x.x, x.z, false, false);
    auto r1 = __builtin_amdgcn_permlane32_swap(x.y, x.w, false, false);
    const v4u v = {r0[0], r1[0], r0[1], r1[1]};
    const u32 vo = tok32(a) * (D / 8) + (a.l16 >> 9) * 16, so = (u32)((c.grp * 64 + HALF * 32) * D + 64 * c.j + 16 * c.w) * 2;
    asm volatile("buffer_store_dwordx4 %0, %1, %2, %3 offen\n\ts_nop 1" :: "v"(v), "v"(vo), "s"(rout), "s"(so) : "memory");
}
template <int HALF, int MLP> DI v4u xres_ld(const Ctx& c, const Ad& a) {
    if constexpr (MLP == 0) return trd2(c.L + (xtr_of(a.l16 >> 4) + (4 * c.j + c.w) * 1024) + (L_X + HALF * 28672), 512);
    else return *(LAS v4u*)(c.L + (a.l16 + (4 * c.j + c.w) * 1024) + (L_X + HALF * 28672));
}
template <int HALF> DI v4u y_ld(const Ctx& c, const Ad& a) { return trd2(c.L + (xtr_of(a.l16 >> 4) + c.w * 1024) + (L_X + HALF * 28672 + 24576), 512); }
DI void x3_make(v4u& x, const v4u& yf, float m, float r) {
    const float n = -m * r;
#pragma unroll
    for (int e = 0; e < 4; ++e) {
        const float p = bflo(x[e]), q = bfhi(x[e]);
        x[e] = pk(__builtin_fmaf(__builtin_fmaf(p, r, n), bflo(yf[e]), p), __builtin_fmaf(__builtin_fmaf(q, r, n), bfhi(yf[e]), q));
    }
}
template <int HALF> DI void x3_store(const Ctx& c, const Ad& a, const v4u& x) { st128(x, c.rws, a.l16, a.x3o + HALF * 28672); }
template <int HALF, int I> DI v4u x3_ld1(const Ctx& c, const Ad& a) {
    return ld128(c.rws, a.l16 + (I & 3) * 1024, a.x3o + (6 * c.w - 4 * c.j + (I & 4)) * 1024 + HALF * 28672);
}
DI u32 max3u(u32 a, u32 b, u32 c) { u32 r; asm("v_max3_u32 %0, %1, %2, %3" : "=v"(r) : "v"(a), "v"(b), "v"(c)); return r; }
DI bool x3_bad(const v4u (&t)[7]) {
    u32 mx = 0;
#pragma unroll
    for (int i = 0; i < 7; ++i) mx = max3u(max3u(mx, t[i].x, t[i].y), t[i].z, t[i].w);
    return ANY(mx == ~0u);
}
template <int HALF> DI void x3_wait(const Ctx& c, const Ad& a, v4u (&t)[7]) {
    if (x3_bad(t)) {
        unsigned spins = 0;
        NOUNROLL do { MEMBAR(); sfor<0, 7>([&](auto IC) { t[decltype(IC)::value] = x3_ld1<HALF, decltype(IC)::value>(c, a); }); } while (x3_bad(t) && !spin_fail(c, spins, 4u));
    }
}
template <int HALF, int I> DI void x3_put1(const Ctx& c, const Ad& a, const v4u& t) { *(LAS v4u*)(c.L + (a.l16 + c.w * 7168) + (L_X + HALF * 28672 + I * 1024)) = t; }

template <int MLP>
DI void g2_pair(const Ctx& c, Ad& a, const W2T& W2, v4u (&hb)[56], G1Out& gb, f32x4 (&csq)[3], float r0, v4u (&xres)[2], __amdgpu_buffer_rsrc_t out) {
    const u32 wl0 = wl2_of(c, a.l16 >> 4), wl1 = wl0 ^ 64u;
    AccT acc[2];
    v2u gate = {0, 0};
    v4u wl[2], fr, yf;
    f32x4 o[2], u[2];
    float s1, s2;
    Gath gs;
    const float ra_ = MLP ? -r0 : r0, rb_ = MLP ? -gb.r : gb.r;
    auto wres = [&](int slot, int k) { return trd2(c.L + (k ? wl1 : wl0) + slot * 2048, 1024); };
    auto fetch = [&](auto NC) {
        constexpr int N = decltype(NC)::value;
        if constexpr (N < 56) {
            if constexpr (N >= 28 && N < 32) hb[N] = *(LAS v4u*)(c.L + (a.l16 + c.w * 4096) + (L_X + (N - 28) * 1024));
            else hb[N] = h_next<N>(c, a);
        }
    };
    auto rd = [&](int i) { return *(LAS f32x4*)(c.L + (a.l16 + c.w * 6144) + (L_X + 28672 + i * 1024)); };
    auto own = [&](int k) { return f32x4{acc[0][0][4 * k], acc[0][0][4 * k + 1], acc[0][0][4 * k + 2], acc[0][0][4 * k + 3]}; };
    wl[0] = wres(8, 0); wl[1] = wres(8, 1);
    SB();
    sfor<0, 28>([&](auto PC) {
        constexpr int F_ = 2 * decltype(PC)::value, H = F_ >= 28, f = F_ - 28 * H;
        constexpr bool res = f < 24;
        constexpr int r0_ = f < 24 ? f >> 1 : f - 12;
        if constexpr (F_ == 24 || F_ == 30 || (MLP == 0 && F_ == 36)) { bar(); SB(); stat_pub_if<MLP == 0 && F_ == 36>(c, a); }
        asm volatile("" : "+v"(a.l16), "+v"(a.ca));
        acc[H][0] = mfma32(W2[r0_][0], hb[F_], f == 0 ? zero16() : acc[H][0]);
        acc[H][1] = mfma32(W2[r0_][1], hb[F_], f == 0 ? zero16() : acc[H][1]);
        fetch(std::integral_constant<int, F_ + (F_ < 8 ? HR - 2 : HR)>{});
        fetch(std::integral_constant<int, F_ + 1 + (F_ < 8 ? HR - 2 : HR)>{});
        if constexpr (F_ == 6) { fetch(std::integral_constant<int, F_ + HR>{}); fetch(std::integral_constant<int, F_ + HR + 1>{}); }
        if constexpr (F_ < 6) csq[(F_ + 2) % 3] = epi_cs(c, a, F_ + 2);
        if constexpr (F_ < 8) g1_epi<1, MLP, F_>(c, a, gb, csq[F_ % 3], fr);
        if constexpr (F_ < 6) csq[F_ % 3] = epi_cs(c, a, F_ + 3);
        if constexpr (F_ < 8) g1_epi<1, MLP, F_ + 1>(c, a, gb, csq[(F_ + 1) % 3], fr);
        if constexpr (F_ == 32) { LA(acc[0][0]); LV(o[0]); LV(u[0]); LV(u[1]); o[0] = (o[0] + own(0)) + (u[0] + u[1]); post_half<0>(o[0], ra_, xres[0]); LV(xres[0]); }
        if constexpr (F_ == 34) { LA(acc[0][0]); LV(o[0]); LV(u[0]); LV(u[1]); o[0] = (o[0] + own(1)) + (u[0] + u[1]); post_half<1>(o[0], ra_, xres[0]); LV(xres[0]); }
        acc[H][0] = mfma32(res ? wl[0] : W2[r0_ + 1][0], hb[F_ + 1], acc[H][0]);
        acc[H][1] = mfma32((res || f == 26) ? wl[1] : W2[r0_ + 1][1], hb[F_ + 1], acc[H][1]);
        if constexpr (f < 22) { wl[0] = wres((9 + (f >> 1)) % 12, 0); wl[1] = wres((9 + (f >> 1)) % 12, 1); }
        if constexpr (f == 24) wl[1] = *(LAS v4u*)(c.L + (a.l16 + c.w * 1024) + L_JK);
        if constexpr (F_ == 26) { wl[0] = wres(8, 0); wl[1] = wres(8, 1); }
        if constexpr (F_ == 22) xres[1] = xres_ld<1, MLP>(c, a);
        if constexpr (F_ == 30) { u[0] = rd(0); u[1] = rd(2); o[0] = rd(4); }
        if constexpr (F_ == 32) { u[0] = rd(1); u[1] = rd(3); o[0] = rd(5); }
        if constexpr (MLP == 0 && F_ == 54) yf = y_ld<0>(c, a);
        if constexpr (F_ == GL) gate = gate_ld<false>(c, a, 1);
        if constexpr (F_ < 8) { *(LAS v4u*)(c.L + (a.l16 + c.w * 4096) + (L_X + (F_ / 2) * 1024)) = fr; h_store<1, F_ / 2>(c, a, fr); }
        if constexpr (MLP == 0 && (F_ == 24 || F_ == 26))
            dma16<true>(c.lbase + L_X + (F_ - 24) / 2 * 28672 + 24576 + c.w * 1024, a.l16, c.rws, OFF_YT + (c.grp * 2 + (F_ - 24) / 2) * 28672 + c.j * 4096 + c.w * 1024);
        if constexpr (F_ == 28) { red_write<0, 1>(c, a, acc[0]); red_write<0, 2>(c, a, acc[0]); red_write<0, 3>(c, a, acc[0]); }
        if constexpr (MLP == 0 && F_ == 34) { frag_sums(xres[0], s1, s2); part_put(c, a, L_S2, s1, s2); }
        if constexpr (MLP == 1 && F_ == 36) out_store<0>(c, a, xres[0], out);
        ilv<4, 5>();
        if constexpr (F_ + 2 < 56 && f + 2 != 28 && f + 2 >= 4) h_check<MLP, F_ + 2>(c, hb[F_ + 2], hb[F_ + 3]);
        if constexpr (F_ == GC) gate_wait<MLP>(c, a, 1, gate);
        SB();
    });
    static_assert(GC + 2 + HR <= 32 && (GC & 1) == 0 && (HR & 1) == 0, "half B's first foreign fragment is requested behind the gate");

    red_write<1, 1>(c, a, acc[1]); red_write<1, 2>(c, a, acc[1]); red_write<1, 3>(c, a, acc[1]);
    if constexpr (MLP == 0) sfor<0, 4>([&](auto IC) { gath_ld<0, 0, decltype(IC)::value>(c, a, gs); });
    bar();
    f32x4 t[6];
    red_read<1>(c, a, t);
    o[0] = (f32x4{acc[1][0][0], acc[1][0][1], acc[1][0][2], acc[1][0][3]} + t[0]) + (t[2] + t[4]);
    o[1] = (f32x4{acc[1][0][4], acc[1][0][5], acc[1][0][6], acc[1][0][7]} + t[1]) + (t[3] + t[5]);
    post_half<0>(o[0], rb_, xres[1]); post_half<1>(o[1], rb_, xres[1]);
    if constexpr (MLP == 0) {
        frag_sums(xres[1], s1, s2); part_put(c, a, L_S2, s1, s2);

        bar();
        stat_pub<1, 0>(c, a, L_S2, 1);

        gath_wait<0, 0>(c, a, gs);
        gath_ln(gs, s1, s2);
        x3_make(xres[0], yf, s1, s2);
        x3_store<0>(c, a, xres[0]);

        yf = y_ld<1>(c, a);
        __builtin_amdgcn_s_sleep(SLP1);
        v4u x[7];
        unsigned spins = 0;
        NOUNROLL do {
            MEMBAR();
            sfor<0, 4>([&](auto IC) { gath_ld<1, 0, decltype(IC)::value>(c, a, gs); });
            sfor<0, 7>([&](auto IC) { x[decltype(IC)::value] = x3_ld1<0, decltype(IC)::value>(c, a); });
        } while ((gath_bad(c, gs) || x3_bad(x)) && !spin_fail(c, spins, 4u));

        gath_ln(gs, s1, s2);
        x3_make(xres[1], yf, s1, s2);
        x3_store<1>(c, a, xres[1]);
        sfor<0, 7>([&](auto IC) { x3_put1<0, decltype(IC)::value>(c, a, x[decltype(IC)::value]); });
    } else out_store<1>(c, a, xres[1], out);
}

__global__ void __launch_bounds__(256, 1) fused_kernel(const u16* __restrict__ x0, const u16* __restrict__ kin, const u16* __restrict__ vin,
                                                       const u16* __restrict__ sk, const u16* __restrict__ hk, const u16* __restrict__ Dv,
                                                       const u16* __restrict__ w1, const u16* __restrict__ w2,
                                                       unsigned char* __restrict__ ws, u16* __restrict__ out) {
    extern __shared__ __attribute__((aligned(16))) char smem[];
    Ctx c;
    c.L = (LAS char*)smem;
    c.lbase = (u32)(uintptr_t)c.L;
    const int b = blockIdx.x, tid = threadIdx.x;
    c.w = __builtin_amdgcn_readfirstlane(tid >> 6);
    c.rws = mkrs(ws, WS_BYTES);
    c.tmo = (GAS u32*)(ws + 256);

    __amdgpu_buffer_rsrc_t rcode;
    {
        unsigned long long pc; u32 len;
        asm volatile("s_getpc_b64 %0\n.Lfk_begin:\n\ts_mov_b32 %1, .Lfk_end-.Lfk_begin" : "=s"(pc), "=s"(len));
        rcode = mkrs((const void*)pc, len);
    }
    if (b < 32) {
        if (c.w < 2) touch(c.lbase + L_JK, (tid & 63) * 128, rcode, (((b >> 3) * 2 + c.w) & 7) * 8192);
        const v2u ep = __builtin_amdgcn_raw_buffer_load_b64(c.rws, 0, OFF_EP, AUX_SC1);
        v4u xr[2][7];
#pragma unroll
        for (int h = 0; h < 2; ++h)
#pragma unroll
            for (int i = 0; i < 7; ++i) xr[h][i] = *(const v4u*)(x0 + (b * 64 + 2 * (tid >> 3) + h) * D + (i * 8 + (tid & 7)) * 8);
        MEMBAR();
        u32 res[2];
#pragma unroll
        for (int h = 0; h < 2; ++h) {
            float s1 = 0.f, s2 = 0.f;
#pragma unroll
            for (int i = 0; i < 7; ++i)
#pragma unroll
                for (int e = 0; e < 4; ++e) { s1 = dot2(xr[h][i][e], 0x3f803f80u, s1); s2 = dot2(xr[h][i][e], xr[h][i][e], s2); }
#pragma unroll
            for (int o = 1; o < 8; o <<= 1) { s1 += __shfl_xor(s1, o); s2 += __shfl_xor(s2, o); }
            const float m = s1 * (1.0f / D);
            res[h] = pk(m, ln_sd(fmaxf(__builtin_fmaf(-m, m, s2 * (1.0f / D)), 0.f)));
        }
        if ((tid & 7) == 0) st128(v4u{res[0], ep.x + 1u, res[1], ep.y + 1u}, c.rws, (u32)(b * 32 + (tid >> 3)) * 16, OFF_ST0);
        const int rg = tid >> 3, c8 = min(tid & 7, 6);
        v4u wr[14];
#pragma unroll
        for (int i = 0; i < 14; ++i) wr[i] = *(const v4u*)(w1 + (rg * 14 + i) * F + b * 56 + c8 * 8);
        float cs[8] = {0, 0, 0, 0, 0, 0, 0, 0};
#pragma unroll
        for (int i = 0; i < 14; ++i)
#pragma unroll
            for (int e = 0; e < 4; ++e) { cs[2 * e] += bflo(wr[i][e]); cs[2 * e + 1] += bfhi(wr[i][e]); }
        LAS float* sf = (LAS float*)c.L;
#pragma unroll
        for (int e = 0; e < 8; ++e) sf[rg * 65 + (tid & 7) * 8 + e] = cs[e];
        bar();
        if (tid < 56) {
            float t = 0.f;
#pragma unroll
            for (int i = 0; i < 32; ++i) t += sf[i * 65 + tid];
            const float t2 = __shfl_down(t, 1);
            if ((tid & 1) == 0) st128(v4u{__float_as_uint(t), ep.x + 1u, __float_as_uint(t2), ep.y + 1u}, c.rws, (u32)(b * 28 + (tid >> 1)) * 16, OFF_CSG);
        }
        WAIT_VM(0);
        bar();
        {
            const __amdgpu_buffer_rsrc_t rw2 = mkrs(w2, D * F * 2);
#pragma unroll
            for (int i = 0; i < 13; ++i) touch(c.lbase + L_JK, (tid & 63) * 128, rw2, (u32)(i * 16 + (b >> 3) * 4 + c.w) * 8192);
        }
        WAIT_VM(0);
        return;
    }
    unsigned long long t_in; asm volatile("s_memrealtime %0\n\ts_waitcnt lgkmcnt(0)" : "=s"(t_in) :: "memory");
    const int wb = b - 32;
    { const int xcd = wb & 7, idx = wb >> 3; c.grp = xcd * 4 + idx / 7; c.j = idx % 7; }
    c.gi = c.grp & 3;
    const int w = c.w;
    c.rw1 = mkrs(w1, D * F * 2); c.rw2 = mkrs(w2, D * F * 2);
    c.kb0 = (w + c.gi + 2) & 3;
    const int cp = (wb & 7) * 28 + (wb >> 3);
    W1T W1; W2T W2;

    u32 dval; unsigned long long t_m;
    {
        const int g = wb % 7, rb = wb / 7, lane = tid & 63;
        const int rg = (lane & 7) * 2 + ((lane >> 4) & 1), cl = ((lane >> 3) & 1) + 2 * (lane >> 5), cq = 4 * w + cl;
        const __amdgpu_buffer_rsrc_t rk = mkrs(kin, TSZ), rv = mkrs(vin, TSZ), rx = mkrs(x0, TSZ), rh = mkrs(hk, TSZ), rs_ = mkrs(sk, D * 8), rd = mkrs(Dv, D * 2);

        const u32 tb = (u32)((rb * 64) * 896 + g * 128);
        const u32 tv0 = (u32)((lane >> 3) * 896) + (((lane & 7) ^ ((lane >> 5) + 2 * w)) & 7) * 16;
        dma16<false>(c.lbase + T_X + w * 1024, tv0, rx, tb + w * 7168);
        dma16<false>(c.lbase + T_X + (w + 4) * 1024, tv0, rx, tb + (w + 4) * 7168);
        dma16<false>(c.lbase + T_K + w * 1024, tv0 + tb + (w - 1) * 7168, rk, 0u);
        dma16<false>(c.lbase + T_K + (w + 4) * 1024, tv0, rk, tb + (w + 3) * 7168);
        dma16<false>(c.lbase + T_V + w * 1024, tv0, rv, tb + w * 7168);
        dma16<false>(c.lbase + T_V + (w + 4) * 1024, tv0, rv, tb + (w + 4) * 7168);
        if (w == 0) dma16<false>(c.lbase + T_K + 8192, tv0, rk, tb + 7 * 7168);
        else dma16<false>(c.lbase + T_S + (w - 1) * 1024, (u32)lane * 16, rs_, g * 512);
        v4u ep4 = ld128_asm<true>(c.rws, 0u, OFF_EP);
        v4u he[2];
        he[0] = ld128_asm<false>(rh, tid * 16, cp * 8192); he[1] = ld128_asm<false>(rh, tid * 16, cp * 8192 + 4096);
        v4u dv4 = ld128_asm<false>(rd, 0u, cp * 4);
        {
            if (w == 0) touch(c.lbase + L_JK + 3072, (u32)lane * 524288u, c.rws, 4096u);
            else if (w < 3) touch(c.lbase + L_JK + 3072, (u32)lane * 262144u, w == 1 ? c.rw1 : c.rw2, 0u);
            else touch(c.lbase + L_JK + 3072, (u32)lane * 128u, rcode, (u32)((wb ^ (wb >> 3)) & 7) * 8192);
        }

        asm volatile("s_waitcnt vmcnt(4)" : "+v"(ep4) :: "memory");
        c.tagA = __builtin_amdgcn_readfirstlane(ep4.x) + 1u; c.tagB = __builtin_amdgcn_readfirstlane(ep4.y) + 1u;
        bar();

        asm volatile("s_memrealtime %0\n\ts_waitcnt lgkmcnt(0)" : "=s"(t_m) :: "memory");
        const u32 a1 = (u32)(rg * 512 + (((cq >> 1) ^ rg) & 7) * 16 + (cq & 1) * 8);
        const u32 ak1 = (u32)(rg * 512 + 1024 + (((cq >> 1) ^ (rg + 2)) & 7) * 16 + (cq & 1) * 8);
        const u32 ak0 = (u32)(rg * 512 + 512 + (((cq >> 1) ^ (rg + 1)) & 7) * 16 + (cq & 1) * 8);
        v2u kr[7], vr[4], xr[4];
#pragma unroll
        for (int i = 0; i < 4; ++i) xr[i] = *(LAS v2u*)(c.L + T_X + a1 + i * 128);
        const v4u w01 = *(LAS v4u*)(c.L + T_S + cq * 32), w23 = *(LAS v4u*)(c.L + T_S + cq * 32 + 16);
#pragma unroll
        for (int i = 0; i < 3; ++i) kr[i] = *(LAS v2u*)(c.L + T_K + ak0 + (i + 1) * 128);
#pragma unroll
        for (int i = 0; i < 4; ++i) { kr[3 + i] = *(LAS v2u*)(c.L + T_K + ak1 + i * 128); vr[i] = *(LAS v2u*)(c.L + T_V + a1 + i * 128); }
#pragma unroll
        for (int h = 0; h < 2; ++h) {
            auto r0 = __builtin_amdgcn_permlane16_swap(__builtin_amdgcn_perm(xr[1][h], xr[0][h], 0x05040100u), __builtin_amdgcn_perm(xr[1][h], xr[0][h], 0x07060302u), false, false);
            auto r1 = __builtin_amdgcn_permlane16_swap(__builtin_amdgcn_perm(xr[3][h], xr[2][h], 0x05040100u), __builtin_amdgcn_perm(xr[3][h], xr[2][h], 0x07060302u), false, false);
            st128(v4u{r0[0], r1[0], r0[1], r1[1]}, c.rws, (u32)((cl * 4 + ((lane >> 4) & 1)) * 4096 + (lane & 7) * 16), OFF_X0G + (g * 64 + 16 * w + 2 * h) * 4096 + rb * 128);
        }

        v4u kg[4];
#pragma unroll
        for (int e = 0; e < 4; ++e) {
            const v4u& wq = e < 2 ? w01 : w23;
            const float w0 = bflo(wq[(e & 1) * 2]), w1_ = bfhi(wq[(e & 1) * 2]), w2_ = bflo(wq[(e & 1) * 2 + 1]), w3 = bfhi(wq[(e & 1) * 2 + 1]);
            float kk[7], o[4];
#pragma unroll
            for (int i = 0; i < 7; ++i) kk[i] = (e & 1) ? bfhi(kr[i][e >> 1]) : bflo(kr[i][e >> 1]);
#pragma unroll
            for (int i = 0; i < 4; i += 2) {
                u32 a_ = pk(kk[i] * w3, kk[i + 1] * w3), b_ = pk(kk[i + 1] * w2_, kk[i + 2] * w2_);
                a_ = pk(bflo(a_) + bflo(b_), bfhi(a_) + bfhi(b_)); b_ = pk(kk[i + 2] * w1_, kk[i + 3] * w1_);
                a_ = pk(bflo(a_) + bflo(b_), bfhi(a_) + bfhi(b_)); b_ = pk(kk[i + 3] * w0, kk[i + 4] * w0);
                a_ = pk(bflo(a_) + bflo(b_), bfhi(a_) + bfhi(b_));
                o[i] = bflo(a_) * ((e & 1) ? bfhi(vr[i][e >> 1]) : bflo(vr[i][e >> 1]));
                o[i + 1] = bfhi(a_) * ((e & 1) ? bfhi(vr[i + 1][e >> 1]) : bflo(vr[i + 1][e >> 1]));
            }
            kg[e] = v4u{pk(o[3], o[2]), c.tagA, pk(o[1], o[0]), c.tagB};
        }

        asm volatile("s_waitcnt vmcnt(0)\n\ts_barrier" : "+v"(he[0]), "+v"(he[1]), "+v"(dv4), "+v"(kg[0]), "+v"(kg[1]), "+v"(kg[2]), "+v"(kg[3]) :: "memory");

#pragma unroll
        for (int e = 0; e < 4; ++e) st128(kg[e], c.rws, (u32)(cl * 4 * 8192 + (15 - rg) * 16), OFF_KVG + ((g * 64 + 16 * w + e) * 512 + 496 - 16 * rb) * 16);
        {
            const WSt wt = w_st<0>(c, lane);
            sfor<0, NW1>([&](auto NC) { w_dma<decltype(NC)::value>(c, wt, w_soff<decltype(NC)::value>(c)); });
        }

        dval = (w >> 1) ? dv4.x >> 16 : dv4.x & 0xffffu;
#pragma unroll
        for (int cc = 0; cc < 2; ++cc) {
            LAS char* cb = c.L + CV_B + cc * CV_CH;
            *(LAS v4u*)(cb + CV_HE + 32 + tid * 16) = he[cc];
            if (tid < 2) { *(LAS v4u*)(cb + CV_HE + tid * 16) = v4u{0, 0, 0, 0}; *(LAS v4u*)(cb + CV_HO + tid * 16) = v4u{0, 0, 0, 0}; }
            if (tid < 32) *(LAS v4u*)(cb + CV_KV + 4096 + tid * 16) = v4u{0, 0, 0, 0};
        }
        bar();
#pragma unroll
        for (int cc = 0; cc < 2; ++cc) {
            const v4u e = he[cc];
            const u32 pv = *(LAS u32*)(c.L + CV_B + cc * CV_CH + CV_HE + 28 + tid * 16);
            *(LAS v4u*)(c.L + CV_B + cc * CV_CH + CV_HO + 32 + tid * 16) = v4u{__builtin_amdgcn_alignbit(e.x, pv, 16), __builtin_amdgcn_alignbit(e.y, e.x, 16),
                                                                            __builtin_amdgcn_alignbit(e.z, e.y, 16), __builtin_amdgcn_alignbit(e.w, e.z, 16)};
        }
        bar();

        SB();
    }
    {
        const int lane = lane_id(), i16 = lane & 15, q = lane >> 4, cc = w >> 1, p = w & 1, ch = 2 * cp + cc;
        LAS char* cb = c.L + CV_B + cc * CV_CH;
        const int o0 = i16 + 1 + 32 * p + 8 * q;
        LAS char* ab = cb + ((o0 & 1) ? CV_HO + 4 : CV_HE) + (o0 >> 1) * 4;
        v4u A[32];
        v4u AD;
        {
            const int jj = p ? -1 : 15 - i16 - 8 * q;
            const u32 dv = dval << ((jj & 1) * 16);
#pragma unroll
            for (int d = 0; d < 4; ++d) AD[d] = (jj >= 0 && (jj >> 1) == d) ? dv : 0u;
        }
        const Ctx cs_ = fresh(c);
        const u32 wl = wl1_of(cs_, lane);
        const WSt wt = w_st<0>(cs_, lane);
        v4u sg[4], g[4];
        SB();
        sfor<0, 8>([&](auto NC) {
            constexpr int n = decltype(NC)::value;
#pragma unroll
            for (int t = n * 4; t < n * 4 + 4; ++t) { LAS u32* pp = (LAS u32*)(ab + t * 128); A[t] = v4u{pp[0], pp[1], pp[2], pp[3]}; }
            SB();
        });

        {
            unsigned spins = 0;
            auto bad4 = [&](const v4u (&x)[4]) { u32 d = 0; for (int t = 0; t < 4; ++t) d = tag_bad(x[t], c.tagA, c.tagB, d); return ANY(d != 0u); };
            NOUNROLL do {
                MEMBAR();
#pragma unroll
                for (int i = 0; i < 4; ++i) g[i] = ld128(c.rws, tid * 16, OFF_KVG + cp * 16384 + i * 4096);
            } while (bad4(g) && !spin_fail(c, spins, 5u));

#pragma unroll
            for (int i = 0; i < 4; ++i) *(LAS v2u*)(c.L + CV_B + (i >> 1) * CV_CH + CV_KV + (tid + 256 * (i & 1)) * 8) = v2u{g[i].x, g[i].z};
        }
        bar();

        LAS char* bb = cb + CV_KV + (2032 - 16 * i16 + 32 * p + 8 * q) * 2;
        f32x4 acc[8];
        float xs[4][4], qs[4][4];
        v4u B[32];
        v2u xg[4];
        B[0] = *(LAS v4u*)(bb + 3 * 128); B[1] = *(LAS v4u*)(bb + 2 * 128); B[2] = *(LAS v4u*)(bb + 128);
        const u32 s4 = (u32)(256 * p + 4 * i16 + q), xo = OFF_X0G + ch * 4096;
        SB();
        sfor<0, 32>([&](auto OC) {
            constexpr int oi = decltype(OC)::value, om = oi - 3;
            constexpr int nm = 8 - (om > 0 ? (om + 3) / 4 : 0) + ((om >= 0 && (om & 3) == 0) ? 1 : 0);
            if constexpr (oi == 9) { bar(); SB(); }
            if constexpr (oi + 3 < 32) B[oi + 3] = *(LAS v4u*)(bb - (om + 3) * 128);
#pragma unroll
            for (int cb_ = 7; cb_ >= 0; --cb_)
                if (4 * cb_ - om >= 0) acc[cb_] = mfma16(A[4 * cb_ - om], B[oi], oi == 0 ? f32x4{0, 0, 0, 0} : acc[cb_]);
            if constexpr (om >= 0 && (om & 3) == 0) acc[om >> 2] = mfma16(AD, B[oi], acc[om >> 2]);
            if constexpr (oi < 2) { sg[2 * oi] = ld128_asm<true>(c.rws, tid * 16, OFF_ST0 + oi * 8192); sg[2 * oi + 1] = ld128_asm<true>(c.rws, tid * 16, OFF_ST0 + oi * 8192 + 4096); }
            if constexpr (oi == 2 || oi == 3) {
                asm volatile("buffer_load_dwordx2 %0, %2, %3, %4 offen offset:%5 sc1\n\tbuffer_load_dwordx2 %1, %2, %3, %4 offen offset:%6 sc1"
                             : "=&v"(xg[2 * oi - 4]), "=&v"(xg[2 * oi - 3]) : "v"(s4 * 8), "s"(c.rws), "s"(xo), "i"((oi - 2) * 1024), "i"((oi - 2) * 1024 + 512) : "memory");
            }
            if constexpr (oi < NW1) w_read1<oi>(cs_, wl, W1, W2);
            if constexpr (oi >= 4 && (oi & 1) == 0 && NW1 + (oi - 4) / 2 < 12) w_dma<NW1 + (oi - 4) / 2>(cs_, wt, w_soff<NW1 + (oi - 4) / 2>(cs_));
            if constexpr (oi == 7) {
                asm volatile("s_waitcnt vmcnt(4)" : "+v"(sg[0]), "+v"(sg[1]), "+v"(sg[2]), "+v"(sg[3]), "+v"(xg[0]), "+v"(xg[1]), "+v"(xg[2]), "+v"(xg[3]) :: "memory");
                auto bad = [&]() { u32 d = 0; for (int t = 0; t < 4; ++t) d = tag_bad(sg[t], c.tagA, c.tagB, d); return ANY(d != 0u); };
                if (bad()) {
                    unsigned spins = 0;
                    NOUNROLL do {
                        MEMBAR();
#pragma unroll
                        for (int t = 0; t < 4; ++t) sg[t] = ld128(c.rws, tid * 16, OFF_ST0 + t * 4096);
                    } while (bad() && !spin_fail(c, spins, 6u));
                }
#pragma unroll
                for (int t = 0; t < 4; ++t) *(LAS v2u*)(c.L + CV_ST + (tid + 256 * t) * 8) = v2u{sg[t].x, sg[t].z};
            }
            if constexpr (oi >= 9 && oi < 13) sg[oi - 9] = *(LAS v4u*)(c.L + CV_ST + s4 * 16 + (oi - 9) * 1024);
            if constexpr (oi >= 11 && oi < 19) {
                constexpr int t = (oi - 11) >> 1, hf = (oi - 11) & 1;
                LV(xg[t]); LV(sg[t]);
                const u32 xw = xg[t][hf];
                const u32 sa = sg[t][2 * hf], sb = sg[t][2 * hf + 1];
                xs[t][2 * hf] = bflo(xw); xs[t][2 * hf + 1] = bfhi(xw);
                qs[t][2 * hf] = (xs[t][2 * hf] - bflo(sa)) * __builtin_amdgcn_rcpf(bfhi(sa));
                qs[t][2 * hf + 1] = (xs[t][2 * hf + 1] - bflo(sb)) * __builtin_amdgcn_rcpf(bfhi(sb));
                asm volatile("" : "+v"(qs[t][2 * hf]), "+v"(qs[t][2 * hf + 1]));
            }
            if constexpr (oi == 23 || oi == 25) {
                const v4u ff = {~0u, ~0u, ~0u, ~0u};
                st128(ff, c.rws, lane * 16, OFF_X3 + (cs_.grp * 56 + (oi - 23) * 14 + 4 * cs_.j + cs_.w) * 1024);
            }
            ilv<nm, 2>();
            SB();
        });

        LAS char* ex = c.L;
        f32x4 fin[4];
        if (p == 0) {
#pragma unroll
            for (int t = 0; t < 4; ++t) { *(LAS f32x4*)(ex + w * 4096 + t * 1024 + lane * 16) = acc[4 + t]; fin[t] = acc[t]; }
        } else {
#pragma unroll
            for (int t = 0; t < 4; ++t) { *(LAS f32x4*)(ex + w * 4096 + t * 1024 + lane * 16) = acc[t]; fin[t] = acc[4 + t]; }
        }
        bar();

        const u32 toff = (u32)((32 * p + 8 * (q & 1) + (i16 >> 1)) * 28672 + ch * 64 + (i16 & 1) * 32 + (q >> 1) * 16);
        v2u y[4], x1[4];
#pragma unroll
        for (int t = 0; t < 4; ++t) {
            fin[t] += *(LAS f32x4*)(ex + (w ^ 1) * 4096 + t * 1024 + lane * 16);
            y[t] = v2u{pk(fin[t][0], fin[t][1]), pk(fin[t][2], fin[t][3])};
            x1[t] = v2u{pk(__builtin_fmaf(qs[t][0], bflo(y[t].x), xs[t][0]), __builtin_fmaf(qs[t][1], bfhi(y[t].x), xs[t][1])),
                        pk(__builtin_fmaf(qs[t][2], bflo(y[t].y), xs[t][2]), __builtin_fmaf(qs[t][3], bfhi(y[t].y), xs[t][3]))};
        }
        auto merge = [&](const v2u& a, const v2u& b_) {
            auto r0 = __builtin_amdgcn_permlane16_swap(a.x, b_.x, false, false);
            auto r1 = __builtin_amdgcn_permlane16_swap(a.y, b_.y, false, false);
            return v4u{r0[0], r1[0], r0[1], r1[1]};
        };
        st128(merge(x1[0], x1[1]), c.rws, toff, OFF_X1);
        st128(merge(x1[2], x1[3]), c.rws, toff, OFF_X1 + 16 * 28672);
        st128(merge(y[0], y[1]), c.rws, toff, OFF_YT);
        st128(merge(y[2], y[3]), c.rws, toff, OFF_YT + 16 * 28672);

        sfor<12, 12 + NW1>([&](auto NC) { w_dma<decltype(NC)::value>(cs_, wt, w_soff<decltype(NC)::value>(cs_)); });
        WAIT_VM(6 + 2 * NW1);
        sfor<NW1, 12>([&](auto NC) { w_read1<decltype(NC)::value>(cs_, wl, W1, W2); });
        WAIT_LGKM(0);
        sfor<12 + NW1, 20>([&](auto NC) { w_dma<decltype(NC)::value>(cs_, wt, w_soff<decltype(NC)::value>(cs_)); });

        WAIT_VM(16);
        bar();
        if (tid < 8) __builtin_amdgcn_raw_buffer_store_b64(v2u{c.tagA, c.tagB}, c.rws, (u32)(tid * 2048 + wb * 8), OFF_FL, AUX_SC1);
        MEMBAR();

        sfor<20, 24>([&](auto NC) { w_dma<decltype(NC)::value>(cs_, wt, w_soff<decltype(NC)::value>(cs_)); });
        WAIT_VM(12);
        sfor<12, 18>([&](auto NC) { w_read1<decltype(NC)::value>(cs_, wl, W1, W2); });
        WAIT_LGKM(0);
        sfor<24, 28>([&](auto NC) { w_dma<decltype(NC)::value>(cs_, wt, w_soff<decltype(NC)::value>(cs_)); });

    }
    Ad ad;
    ad.l16 = lane_id() * 16;
    ad.hst = OFF_H + (c.grp * 224 + 16 * c.j + 4 * w) * 1024;
    ad.x3o = OFF_X3 + (c.grp * 56 + 4 * c.j + w) * 1024;
    ad.sto = OFF_ST + c.grp * 2 * ST_HG;
    asm volatile("" : "+s"(ad.hst), "+s"(ad.x3o), "+s"(ad.sto));
    const u32 swl = wl2_of(c, ad.l16 >> 4);
    const WSt w2t = w_st<28>(c, ad.l16 >> 4);
    u32 wso[7];
#pragma unroll
    for (int m = 0; m < 7; ++m) {
        const int mm = member(c, m);
        wso[m] = (256 * mm + 64 * w) * (D * 2) + 128 * c.j; ad.so[m] = ad.hst + (mm - c.j) * 16384;
        asm volatile("" : "+s"(wso[m]), "+s"(ad.so[m]));
    }
    auto w2_dma = [&](auto NC) { constexpr int n = decltype(NC)::value; w_dma<n>(c, w2t, wso[k_mem(w2_step_of(n))] + k_sub(w2_step_of(n)) * 16 * (D * 2)); };
    {
        v4u cg = ld128_asm<true>(c.rws, tok32(ad), OFF_CSG + c.j * 2048 + w * 512);
        sfor<28, 30>(w2_dma);
        WAIT_VM(12);
        w_read<18, 24>(c, W1, W2);

        if (w == 0) {
            unsigned spins = 0;
            NOUNROLL for (;;) {
                MEMBAR();
                const v4u f0 = ld128(c.rws, ad.l16, OFF_FL + (wb & 7) * 2048);
                const v4u f1 = ld128(c.rws, 1024 + min(ad.l16, 752u), OFF_FL + (wb & 7) * 2048);
                const u32 d = xor_or(f0.x, c.tagA, xor_or(f0.z, c.tagA, xor_or(f1.x, c.tagA, xor_or(f1.z, c.tagA, tag_bad(f0, c.tagB, c.tagB, tag_bad(f1, c.tagB, c.tagB, 0u))))));
                if (!ANY(d != 0u)) break;
                if (spin_fail(c, spins, 7u)) break;
            }
            if (wb == 0 && ad.l16 == 0) __builtin_amdgcn_raw_buffer_store_b64(v2u{c.tagA, c.tagB}, c.rws, 0, OFF_EP, AUX_SC1);
        }
        bar();

        {
            const u32 xb = c.lbase + L_X + w * 7168, xs_ = OFF_X1 + c.grp * 57344 + w * 7168;
            sfor<0, 14>([&](auto IC) {
                constexpr int i = decltype(IC)::value, o = (i / 7) * 28672 + (i % 7) * 1024;
                dma16<true>(xb + o, ad.l16, c.rws, xs_ + o);
            });
        }

        WAIT_VM(14);
        w_read<24, 28>(c, W1, W2);
        w_read1<28>(c, swl, W1, W2); w_read1<29>(c, swl, W1, W2);
        w_neg<28>(W2); w_neg<29>(W2);
        LV(cg);
        if (ANY(tag_bad(cg, c.tagA, c.tagB, 0u) != 0u)) {
            unsigned spins = 0;
            NOUNROLL do { MEMBAR(); cg = ld128(c.rws, tok32(ad), OFF_CSG + c.j * 2048 + w * 512); } while (ANY(tag_bad(cg, c.tagA, c.tagB, 0u) != 0u) && !spin_fail(c, spins, 8u));
        }
        *(LAS v2u*)(c.L + L_CS + w * 256 + (tok32(ad) >> 1)) = v2u{cg.x, cg.z};
        WAIT_VM(7);
        bar();

    }

    pin_a<0, 25>(W1);
    v4u hb[56], xres[2];
    G1Out ga, gb;
    v2u gate;
    f32x4 csq[3];
    St1 st;
    ad.ca = L_CS + (w * 64 + (ad.l16 >> 9) * 4) * 4;
    using M0 = std::integral_constant<int, 0>; using M1 = std::integral_constant<int, 1>;
    {
        st.ad = xtr_of(ad.l16 >> 4) + w * 7168;
        auto stats = [&](auto MC, auto HC, auto FC, G1Out& o) {
            constexpr int H = decltype(HC)::value, f = decltype(FC)::value;
            constexpr bool TR = decltype(MC)::value == 0;
            if constexpr (f == 0) { st.s1 = 0.f; st.s2 = 0.f; }
            if constexpr (f >= 2 && f < 23) { constexpr int ph = (f - 2) % 3, k = (f - 2) / 3; st1_acc<k, ph * 3, ph == 2 ? 8 : ph * 3 + 3>(st); }
            if constexpr (f % 3 == 0 && f < 21) st1_read<TR, H, f / 3>(c, st);
            if constexpr (f == 23) part_put(c, ad, L_SP + H * 1024, st.s1, st.s2);
            if constexpr (f == 24) { SB(); asm volatile("s_waitcnt lgkmcnt(%0)\n\ts_barrier" :: "i"(TR ? 2 : 1) : "memory"); }
            if constexpr (f == 25) { const f32x2 t = part_get(c, ad, L_SP + H * 1024); ln_scalars(t[0], t[1], o.mb, o.r); asm volatile("" : "+v"(o.mb), "+v"(o.r)); }
        };
        auto stage = [&](auto GC_, auto XC) {
            constexpr int g = decltype(GC_)::value, extra = decltype(XC)::value;
            if constexpr (g >= 15 && g < 43 && (g & 1)) w_neg<30 + (g - 15) / 2>(W2);
            if constexpr (g == 41) *(LAS v4u*)(c.L + (ad.l16 + w * 1024) + L_JK) = W2[15][1];
            if constexpr (g >= 13 && g < 41 && (g & 1)) { WAIT_VM(12 + extra); w_read1<30 + (g - 13) / 2>(c, swl, W1, W2); }
            if constexpr (g < 52 && (g & 1) == 0) w2_dma(std::integral_constant<int, 30 + g / 2>{});
        };
        constexpr unsigned REG = 0x9AAAAAAu;
        g1_main<true, 0, REG>(c, ad, W1, ga.acc, [&](auto FC) {
            constexpr int f = decltype(FC)::value;
            stage(FC, M0{});
            stats(M0{}, M0{}, FC, ga);
            if constexpr (f >= 26) csq[f - 26] = epi_cs(c, ad, f - 26);
        });

        auto side_b = [&](auto MC, auto FC) {
            constexpr int f = decltype(FC)::value, M = decltype(MC)::value;
            if constexpr (f < 6) csq[(f + 2) % 3] = epi_cs(c, ad, f + 2);
            if constexpr (f < 8) g1_epi<0, M, f>(c, ad, ga, csq[f % 3], hb[f >> 1]);
            if constexpr (f < 8 && (f & 1)) h_store<0, f / 2>(c, ad, hb[f >> 1]);
            if constexpr (f == 18) gate = gate_ld<true>(c, ad, 0);
            if constexpr (f == 19) xres[0] = xres_ld<0, M>(c, ad);
            if constexpr (f == 23) {
                SB();
                asm volatile("s_waitcnt vmcnt(%1)" : "+v"(gate) : "i"(M == 0 ? 4 : 0) : "memory");
                gate_wait<M>(c, ad, 0, gate);
            }
            if constexpr (f >= 24) hb[f - 20] = h_next<f - 20>(c, ad);
            if constexpr (f >= 26) csq[f - 26] = epi_cs(c, ad, f - 26);
        };
        const u32 ra = L_W + w * 24576 + 1024 + ad.l16;
        v4u rm;
        g1_main<true, 1, REG>(c, ad, W1, gb.acc, [&](auto FC) {
            constexpr int f = decltype(FC)::value;
            stage(std::integral_constant<int, 28 + f>{}, std::integral_constant<int, (f < 8 ? f / 2 : 4)>{});
            if constexpr (f >= 16 && f < 28 && (f & 1) == 0) { LV(rm); *(LAS v4u*)(c.L + ra + ((8 + (f - 16) / 2) % 12) * 2048) = rm ^ SGN; }
            if constexpr (f >= 14 && f < 26 && (f & 1) == 0) { WAIT_VM(f < 24 ? 14 : 12); rm = *(LAS v4u*)(c.L + ra + ((8 + (f - 14) / 2) % 12) * 2048); }
            stats(M0{}, M1{}, FC, gb);
            side_b(M0{}, FC);
        });

        {
            WAIT_VM(4);
            v4u t[6];
#pragma unroll
            for (int i = 0; i < 6; ++i) t[i] = *(LAS v4u*)(c.L + ra + ((14 + i) % 12) * 2048);
#pragma unroll
            for (int i = 0; i < 6; ++i) *(LAS v4u*)(c.L + ra + ((14 + i) % 12) * 2048) = t[i] ^ SGN;
            SB();
        }
        pin_a<0, 25>(W1);

        g2_pair<0>(c, ad, W2, hb, gb, csq, ga.r, xres, c.rws);
        pin_a<0, 25>(W1);

        v4u t[7];
        st.ad = ad.l16 + w * 7168;

        bar();

        g1_main<false, 0, REG>(c, ad, W1, ga.acc, [&](auto FC) {
            constexpr int f = decltype(FC)::value;
            stats(M1{}, M0{}, FC, ga);
            if constexpr (f >= 12 && f < 19) t[f - 12] = x3_ld1<1, f - 12>(c, ad);
            if constexpr (f == 24) x3_wait<1>(c, ad, t);
            if constexpr (f >= 25) { x3_put1<1, 2 * (f - 25)>(c, ad, t[2 * (f - 25)]); x3_put1<1, 2 * (f - 25) + 1>(c, ad, t[2 * (f - 25) + 1]); }
            if constexpr (f == 27) x3_put1<1, 6>(c, ad, t[6]);
            if constexpr (f >= 26) csq[f - 26] = epi_cs(c, ad, f - 26);
        });

        bar();

        g1_main<false, 1, REG>(c, ad, W1, gb.acc, [&](auto FC) {
            stats(M1{}, M1{}, FC, gb);
            side_b(M1{}, FC);
        });

        const __attribute__((address_space(4))) char* ka = (const __attribute__((address_space(4))) char*)__builtin_amdgcn_kernarg_segment_ptr();
        asm volatile("" : "+s"(ka));
        g2_pair<1>(c, ad, W2, hb, gb, csq, ga.r, xres, mkrs(*(void* const __attribute__((address_space(4)))*)(ka + 72), TSZ));

        { unsigned long long t_e; asm volatile("s_memrealtime %0\n\ts_waitcnt lgkmcnt(0)" : "=s"(t_e) :: "memory");
          const unsigned long long lim = t_e + (t_m - t_in);
          for (;;) { unsigned long long t_n; asm volatile("s_memrealtime %0\n\ts_waitcnt lgkmcnt(0)" : "=s"(t_n) :: "memory"); if (t_n >= lim) break; __builtin_amdgcn_s_sleep(2); } }
    }
    asm volatile(".Lfk_end:");
}

extern "C" void kernel_launch(void* const* d_in, const int* in_sizes, int n_in,
                              void* d_out, int out_size, void* d_ws, size_t ws_size,
                              hipStream_t stream) {
    (void)hipFuncSetAttribute((const void*)fused_kernel, hipFuncAttributeMaxDynamicSharedMemorySize, L_TOT);
    fused_kernel<<<256, 256, L_TOT, stream>>>((const u16*)d_in[0], (const u16*)d_in[1], (const u16*)d_in[2], (const u16*)d_in[3], (const u16*)d_in[4],
                                              (const u16*)d_in[5], (const u16*)d_in[7], (const u16*)d_in[8], (unsigned char*)d_ws, (u16*)d_out);
}
```

```cpp
#include <hip/hip_runtime.h>
#include <stdint.h>
#include <type_traits>

typedef unsigned short u16;
typedef unsigned u32;
typedef u32 v4u __attribute__((ext_vector_type(4)));
typedef u32 v2u __attribute__((ext_vector_type(2)));
typedef float f32x2 __attribute__((ext_vector_type(2)));
typedef float f32x4 __attribute__((ext_vector_type(4)));
typedef float f32x16 __attribute__((ext_vector_type(16)));
typedef __bf16 bf16x8 __attribute__((ext_vector_type(8)));
typedef __bf16 bf16x2 __attribute__((ext_vector_type(2)));
typedef short s16x4 __attribute__((ext_vector_type(4)));
typedef short s16x2 __attribute__((ext_vector_type(2)));
typedef unsigned short u16x2 __attribute__((ext_vector_type(2)));
#define LAS __attribute__((address_space(3)))
#define GAS __attribute__((address_space(1)))
#define DI __device__ __forceinline__
#define RLX_AGENT __ATOMIC_RELAXED, __HIP_MEMORY_SCOPE_AGENT
#define SB() __builtin_amdgcn_sched_barrier(0)
#define NOUNROLL _Pragma("unroll 1")
#define MEMBAR() asm volatile("" ::: "memory")
template <class T> __device__ __forceinline__ void LV(T& x) { asm volatile("" : "+v"(x)); }
template <class T> __device__ __forceinline__ void LA(T& x) { asm volatile("" : "+a"(x)); }
#define ANY(x) (__builtin_amdgcn_ballot_w64(x) != 0ull)

constexpr int S = 2048, D = 448, F = 1792;
constexpr float EPS = 1e-5f;
constexpr u32 TSZ = S * D * 2;
constexpr u32 OFF_EP  = 0;
constexpr u32 OFF_FL  = 4096;
constexpr u32 OFF_ST0 = OFF_FL + 16384;
constexpr u32 OFF_CSG = OFF_ST0 + 16384;
constexpr u32 OFF_ST  = OFF_CSG + 16384;
constexpr u32 OFF_KVG = 1048576;
constexpr u32 OFF_X0G = OFF_KVG + 2 * TSZ;
constexpr u32 OFF_X1  = OFF_X0G + 2 * TSZ;
constexpr u32 OFF_YT  = OFF_X1 + TSZ;
constexpr u32 OFF_X3  = OFF_YT + TSZ;
constexpr u32 OFF_H   = OFF_X3 + TSZ;
constexpr u32 OFF_WT2 = OFF_H + 64 * 112 * 1024;
constexpr u32 OFF_FL2 = 2048;
constexpr u32 WS_BYTES = OFF_WT2 + D * F * 2;
static_assert(OFF_ST + 64 * 24 * 512 <= OFF_KVG, "ws map");
#define STAMP(i) do {} while (0)
#define STAMP2(i) do {} while (0)
#define STAMP4(i) do {} while (0)
#define STAMP3(i) do {} while (0)
constexpr int RSRC_W3 = 0x00020000;
constexpr int AUX_SC1 = 16;
constexpr unsigned SPIN_CAP = 1u << 14;
constexpr u32 SGN = 0x80008000u;

template <int I, int N, class Fn> DI void sfor(Fn&& fn) { if constexpr (I < N) { fn(std::integral_constant<int, I>{}); sfor<I + 1, N>(fn); } }

DI float bflo(u32 x) { return __uint_as_float(x << 16); }
DI float bfhi(u32 x) { return __uint_as_float(x & 0xffff0000u); }
DI u32 pk(float a, float b) { f32x2 v = {a, b}; return __builtin_bit_cast(u32, __builtin_convertvector(v, bf16x2)); }
DI float rbf(float a) { return bflo(pk(a, a)); }
DI __amdgpu_buffer_rsrc_t mkrs(const void* p, u32 bytes) { return __builtin_amdgcn_make_buffer_rsrc((void*)p, 0, (int)bytes, RSRC_W3); }
DI f32x4 mfma16(v4u a, v4u b, f32x4 c) { return __builtin_amdgcn_mfma_f32_16x16x32_bf16(__builtin_bit_cast(bf16x8, a), __builtin_bit_cast(bf16x8, b), c, 0, 0, 0); }
DI f32x16 mfma32(v4u a, v4u b, f32x16 c) { return __builtin_amdgcn_mfma_f32_32x32x16_bf16(__builtin_bit_cast(bf16x8, a), __builtin_bit_cast(bf16x8, b), c, 0, 0, 0); }
DI f32x16 zero16() { f32x16 z; for (int i = 0; i < 16; ++i) z[i] = 0.f; return z; }
DI v2u trd(LAS char* p) { return __builtin_bit_cast(v2u, __builtin_amdgcn_ds_read_tr16_b64_v4i16((LAS s16x4*)p)); }
DI v4u trd2(LAS char* p, int second) { v2u a = trd(p), b = trd(p + second); return v4u{a.x, a.y, b.x, b.y}; }
DI float dot2(u32 a, u32 b, float c) { return __builtin_amdgcn_fdot2_f32_bf16(__builtin_bit_cast(bf16x2, a), __builtin_bit_cast(bf16x2, b), c, false); }
DI v4u ld128(__amdgpu_buffer_rsrc_t rs, u32 vo, u32 so) { return __builtin_amdgcn_raw_buffer_load_b128(rs, vo, so, AUX_SC1); }
template <bool SC1, int OFF = 0> DI v4u ld128_asm(__amdgpu_buffer_rsrc_t rs, u32 vo, u32 so) {
    v4u r;
    if constexpr (SC1) asm volatile("buffer_load_dwordx4 %0, %1, %2, %3 offen offset:%4 sc1" : "=v"(r) : "v"(vo), "s"(rs), "s"(so), "i"(OFF) : "memory");
    else asm volatile("buffer_load_dwordx4 %0, %1, %2, %3 offen offset:%4" : "=v"(r) : "v"(vo), "s"(rs), "s"(so), "i"(OFF) : "memory");
    return r;
}
template <int OFF = 0> DI void st128(v4u v, __amdgpu_buffer_rsrc_t rs, u32 vo, u32 so) {
    asm volatile("buffer_store_dwordx4 %0, %1, %2, %3 offen offset:%4 sc1\n\ts_nop 1" :: "v"(v), "v"(vo), "s"(rs), "s"(so), "i"(OFF) : "memory");
}
DI int lane_id() { int l; asm volatile("v_mbcnt_lo_u32_b32 %0, -1, 0\n\tv_mbcnt_hi_u32_b32 %0, -1, %0" : "=v"(l)); return l; }
DI float hsum(float a) {
    const u32 v = __float_as_uint(a);
    auto t = __builtin_amdgcn_permlane32_swap(v, v, false, false);
    return __uint_as_float(t[0]) + __uint_as_float(t[1]);
}
DI float ln_sd(float var) { return rbf(__builtin_amdgcn_sqrtf(rbf(rbf(var) + EPS))); }
DI void ln_scalars(float s1, float s2, float& m, float& r) {
    m = s1 * (1.0f / D);
    r = __builtin_amdgcn_rsqf(fmaxf(__builtin_fmaf(-m, m, s2 * (1.0f / D)), 0.f) + EPS);
}
DI u32 xor_or(u32 a, u32 b, u32 c) { return __builtin_amdgcn_bitop3_b32(a, b, c, 0xBE); }
DI u32 tag_bad(const v4u& g, u32 tA, u32 tB, u32 acc) { return xor_or(g.w, tB, xor_or(g.y, tA, acc)); }
template <bool SC1> DI void dma16(u32 ldsaddr, u32 voff, __amdgpu_buffer_rsrc_t rs, u32 soff) {
    if constexpr (SC1) asm volatile("s_mov_b32 m0, %0\n\ts_nop 0\n\tbuffer_load_dwordx4 %1, %2, %3 offen sc1 lds" :: "s"(ldsaddr), "v"(voff), "s"(rs), "s"(soff) : "memory", "m0");
    else asm volatile("s_mov_b32 m0, %0\n\ts_nop 0\n\tbuffer_load_dwordx4 %1, %2, %3 offen lds" :: "s"(ldsaddr), "v"(voff), "s"(rs), "s"(soff) : "memory", "m0");
}
template <int LI> DI void dma_pair(u32 ldsbase, u32 vo0, u32 vo1, __amdgpu_buffer_rsrc_t rs, u32 soff) {
    asm volatile("s_add_u32 m0, %0, %5\n\ts_nop 0\n\tbuffer_load_dwordx4 %1, %3, %4 offen lds\n\tbuffer_load_dwordx4 %2, %3, %4 offen offset:1024 lds"
                 :: "s"(ldsbase), "v"(vo0), "v"(vo1), "s"(rs), "s"(soff), "i"(LI) : "memory", "m0", "scc");
}
template <int LI> DI void dma_pair_sc1(u32 ldsbase, u32 vo, __amdgpu_buffer_rsrc_t rs, u32 soff) {
    asm volatile("s_add_u32 m0, %0, %4\n\ts_nop 0\n\tbuffer_load_dwordx4 %1, %2, %3 offen sc1 lds\n\tbuffer_load_dwordx4 %1, %2, %3 offen offset:1024 sc1 lds"
                 :: "s"(ldsbase), "v"(vo), "s"(rs), "s"(soff), "i"(LI) : "memory", "m0", "scc");
}
DI void touch(u32 junk, u32 voff, __amdgpu_buffer_rsrc_t rs, u32 soff) {
    asm volatile("s_mov_b32 m0, %0\n\ts_nop 0\n\tbuffer_load_dword %1, %2, %3 offen lds" :: "s"(junk), "v"(voff), "s"(rs), "s"(soff) : "memory", "m0");
}
#define WAIT_VM(n) asm volatile("s_waitcnt vmcnt(%0)" :: "i"(n) : "memory")
#define WAIT_LGKM(n) asm volatile("s_waitcnt lgkmcnt(%0)" :: "i"(n) : "memory")
DI void bar() { asm volatile("s_waitcnt lgkmcnt(0)\n\ts_barrier" ::: "memory"); }
DI int wrap7(int x) { return x >= 7 ? x - 7 : x; }
template <int N, int K> DI void ilv() { if constexpr (N > 0) { __builtin_amdgcn_sched_group_barrier(0x8, 1, 0); __builtin_amdgcn_sched_group_barrier(0x366, K, 0); ilv<N - 1, K>(); } }

constexpr int L_X = 0;
constexpr int L_W = 57344;
constexpr int L_M = L_W + 98304;
constexpr int L_SP = L_M;
constexpr int L_CS = L_M + 2048;
constexpr int L_S2 = L_M + 3072;
constexpr int L_JK = L_M + 4096;
constexpr int L_TOT = L_M + 8192;
constexpr int T_K = 0, T_V = 9216, T_X = 17408, T_S = L_JK;
constexpr int CV_B = 25600;
constexpr int CV_HE = 0, CV_HO = 4160, CV_KV = CV_HO + 4160, CV_CH = CV_KV + 4608;
constexpr int CV_ST = 16384;
static_assert(CV_B + 2 * CV_CH <= L_W, "lds map");

struct Ctx {
    LAS char* L;
    __amdgpu_buffer_rsrc_t rws, rw1, rw2;
    GAS u32* tmo;
    int w, j, grp, kb0, gi;
    u32 lbase, tagA, tagB;
};
DI Ctx fresh(const Ctx& c0) { Ctx c = c0; asm volatile("" : "+s"(c.w), "+s"(c.j), "+s"(c.grp), "+s"(c.kb0), "+s"(c.gi)); return c; }
DI bool spin_fail(const Ctx& c, unsigned& spins, u32 code) {
    if (++spins > SPIN_CAP) { __hip_atomic_store(c.tmo, code, RLX_AGENT); return true; }
    __builtin_amdgcn_s_sleep(1);
    return false;
}
DI u32 xtr_of(int lane) { return (u32)(((lane >> 5) * 4 + ((lane >> 2) & 3)) * 64 + ((lane >> 4) & 1) * 32 + (lane & 3) * 8); }
DI u32 wl1_of(const Ctx& c, int lane) {
    const int rr = (lane >> 5) * 4 + ((lane >> 2) & 3);
    return (u32)(L_W + c.w * 24576 + rr * 128 + ((rr >> 1) & 1) * 64 + ((lane >> 4) & 1) * 32 + (lane & 3) * 8);
}
DI u32 wl2_of(const Ctx& c, int lane) { return wl1_of(c, lane) ^ (u32)((c.w >> 1) * 64 + (c.w & 1) * 32); }
DI int member(const Ctx& c, int m) {
    if (m == 0) return c.j;
    int t = m - 1 + 2 * c.gi; t = t >= 6 ? t - 6 : t; t = t >= 6 ? t - 6 : t;
    return wrap7(c.j + 1 + t);
}
typedef v4u W1T[28][2];
typedef v4u W2T[16][2];
typedef f32x16 AccT[2];

DI constexpr int k_mem(int f) { return f < 4 ? 0 : 1 + (f - 4) % 6; }
DI constexpr int k_sub(int f) { return f < 4 ? f : (f - 4) / 6; }
DI constexpr int w2_step_of(int n) { return n < 40 ? 2 * (n - 28) : (n < 44 ? n - 16 : 2 * (n - 44) + 1); }
template <int N> DI u32 w_soff(const Ctx& c) {
    if constexpr (N < 28) return (N % 7 + 7 * ((N / 7 + c.kb0) & 3)) * 16 * (F * 2) + (256 * c.j + 64 * c.w) * 2;
    else return (256 * member(c, k_mem(w2_step_of(N))) + 64 * c.w + 16 * k_sub(w2_step_of(N))) * (D * 2) + 128 * c.j;
}
template <int N> DI u32 w_voff(int lane) { return (lane >> 3) * ((N < 28 ? F : D) * 2) + ((lane & 7) ^ (((lane >> 4) & 1) << 2)) * 16; }
struct WSt { u32 ring, vo0, vo1; };
template <int N> DI WSt w_st(const Ctx& c, int lane) { WSt t; t.ring = c.lbase + L_W + c.w * 24576; t.vo0 = w_voff<N>(lane); t.vo1 = t.vo0 + 8 * ((N < 28 ? F : D) * 2) - 1024; return t; }
template <int N> DI void w_dma(const Ctx& c, const WSt& t, u32 so) { dma_pair<(N % 12) * 2048>(t.ring, t.vo0, t.vo1, N < 28 ? c.rw1 : c.rw2, so); }
DI v4u w_frag(const Ctx& c, u32 wl, int slot, int t) { return trd2(c.L + (wl ^ (u32)(t * 64)) + slot * 2048, 1024); }
template <int N> DI void w_read1(const Ctx& c, u32 wl, W1T& W1, W2T& W2) {
#pragma unroll
    for (int t = 0; t < 2; ++t) {
        if constexpr (N < 28) W1[N][t] = w_frag(c, wl, N % 12, t);
        else W2[N - 28][t] = w_frag(c, wl, N % 12, t);
    }
}
template <int N> DI void w_neg(W2T& W2) {
#pragma unroll
    for (int t = 0; t < 2; ++t) { LV(W2[N - 28][t]); W2[N - 28][t].z ^= SGN; W2[N - 28][t].w ^= SGN; asm volatile("" : "+v"(W2[N - 28][t])); }
}
template <int N0, int N1> DI void w_read(const Ctx& c0, W1T& W1, W2T& W2) {
    const Ctx c = fresh(c0);
    const u32 wl = wl1_of(c, lane_id());
    sfor<N0, N1>([&](auto NC) { w_read1<decltype(NC)::value>(c, wl, W1, W2); });
}

constexpr int PS0 = 0, PS1 = 33;
template <int N0, int N1> DI void pin_a(W1T& W1) {
    if constexpr (N0 < N1) { asm volatile("" : "+a"(W1[N0][0]), "+a"(W1[N0][1])); pin_a<N0 + 1, N1>(W1); }
}
template <int N0, int N1> DI void pin_v(W2T& W2) { if constexpr (N0 < N1) { LV(W2[N0][0]); if constexpr (N0 < 15) LV(W2[N0][1]); pin_v<N0 + 1, N1>(W2); } }
struct G1Out { AccT acc; float mb, r; };
DI constexpr bool reg_start(unsigned long long end, int f) { return f == 0 || ((end >> (f - 1)) & 1ull); }
DI constexpr int reg_len(unsigned long long end, int f) { int n = 1; while (!reg_start(end, f - n + 1)) ++n; return n; }
struct Ad {
    u32 l16;
    u32 ca;
    u32 hst;
    u32 x3o;
    u32 sto;
    u32 so[7];
};
constexpr u32 ST_HG = 3 * 4096;
DI u32 tok32(const Ad& a) { return a.l16 & 496u; }

template <bool TR, int HALF, int S0, unsigned END, class Side>
DI void g1_main(const Ctx& c, Ad& a, const W1T& W1, AccT& acc, Side&& side) {
    const u32 lp = TR ? xtr_of(a.l16 >> 4) : a.l16;
    int kb = c.kb0; asm volatile("" : "+s"(kb));
    u32 la = 0;
    auto ldb = [&](int f) {
        if (f % 7 == 0) la = lp + ((f / 7 + kb) & 3) * 7168;
        LAS char* p = c.L + la + (L_X + HALF * 28672 + (f % 7) * 1024);
        if constexpr (TR) return trd2(p, 512);
        else return *(LAS v4u*)p;
    };
    v4u B[28];
    B[0] = ldb(0); B[1] = ldb(1); B[2] = ldb(2);
    SB();
    sfor<0, 28>([&](auto FC) {
        constexpr int f = decltype(FC)::value;
        if constexpr (f % 4 == 0) STAMP2(S0 + f / 4);
        if constexpr (reg_start(END, f)) asm volatile("" : "+v"(a.l16), "+v"(a.ca));
        acc[0] = mfma32(W1[f][0], B[f], f == 0 ? zero16() : acc[0]);
        acc[1] = mfma32(W1[f][1], B[f], f == 0 ? zero16() : acc[1]);
        if constexpr (f + 3 < 28) B[f + 3] = ldb(f + 3);
        side(FC);
        if constexpr ((END >> f) & 1u) { ilv<2 * reg_len(END, f), 5>(); SB(); }
    });
}
struct St1 { u32 ad; v4u fr[2]; float s1, s2; };
template <bool TR, int HALF, int K> DI void st1_read(const Ctx& c, St1& s) {
    if constexpr (TR) s.fr[K & 1] = trd2(c.L + s.ad + (L_X + HALF * 28672 + K * 1024), 512);
    else s.fr[K & 1] = *(LAS v4u*)(c.L + s.ad + (L_X + HALF * 28672 + K * 1024));
}
template <int K, int E0, int E1> DI void st1_acc(St1& s) {
#pragma unroll
    for (int e = E0; e < E1; ++e) { if (e == E0) LV(s.fr[K & 1]); const u32 x = s.fr[K & 1][e >> 1]; if (e & 1) s.s2 = dot2(x, x, s.s2); else s.s1 = dot2(x, 0x3f803f80u, s.s1); }
}
DI void part_put(const Ctx& c, const Ad& a, int off, float s1, float s2) { *(LAS f32x2*)(c.L + off + tok32(a) * 2 + c.w * 8) = f32x2{hsum(s1), hsum(s2)}; }
DI f32x2 part_get(const Ctx& c, const Ad& a, int off) {
    const f32x4 u = *(LAS f32x4*)(c.L + off + tok32(a) * 2), v = *(LAS f32x4*)(c.L + off + tok32(a) * 2 + 16);
    return f32x2{(u[0] + u[2]) + (v[0] + v[2]), (u[1] + u[3]) + (v[1] + v[3])};
}
DI void frag_sums(const v4u& p, float& s1, float& s2) {
    s1 = 0.f; s2 = 0.f;
#pragma unroll
    for (int e = 0; e < 4; ++e) { s1 = dot2(p[e], 0x3f803f80u, s1); s2 = dot2(p[e], p[e], s2); }
}
template <int HALF, int KIND> DI void stat_pub(const Ctx& c, const Ad& a, int off, int who) {
    if (c.w == who) {
        f32x2 t = part_get(c, a, off);
        const bool z = c.j == 6 && a.l16 >= 512u;
        st128(v4u{z ? 0u : __float_as_uint(t[0]), c.tagA, z ? 0u : __float_as_uint(t[1]), c.tagB}, c.rws, z ? a.l16 : tok32(a), a.sto + HALF * ST_HG + KIND * 4096 + c.j * 512);
    }
}
template <bool DO> DI void stat_pub_if(const Ctx& c, const Ad& a) { if constexpr (DO) stat_pub<0, 0>(c, a, L_S2, 0); }
struct Gath { v4u g[4]; };
template <int HALF, int KIND, int I> DI void gath_ld(const Ctx& c, const Ad& a, Gath& g) {
    g.g[I] = ld128(c.rws, a.l16 + I * 1024, a.sto + HALF * ST_HG + KIND * 4096);
}
DI bool gath_bad(const Ctx& c, const Gath& g) {
    u32 d = 0;
#pragma unroll
    for (int i = 0; i < 4; ++i) d = tag_bad(g.g[i], c.tagA, c.tagB, d);
    return ANY(d != 0u);
}
template <int HALF, int KIND> DI void gath_wait(const Ctx& c, const Ad& a, Gath& g) {
    if (gath_bad(c, g)) {
        unsigned spins = 0;
        NOUNROLL do { MEMBAR(); sfor<0, 4>([&](auto IC) { gath_ld<HALF, KIND, decltype(IC)::value>(c, a, g); }); } while (gath_bad(c, g) && !spin_fail(c, spins, 2u));
    }
}
DI void gath_ln(const Gath& g, float& m, float& r) {
    const float t1 = (__uint_as_float(g.g[0].x) + __uint_as_float(g.g[1].x)) + (__uint_as_float(g.g[2].x) + __uint_as_float(g.g[3].x));
    const float t2 = (__uint_as_float(g.g[0].z) + __uint_as_float(g.g[1].z)) + (__uint_as_float(g.g[2].z) + __uint_as_float(g.g[3].z));
    ln_scalars(hsum(t1), hsum(t2), m, r);
}
DI f32x4 epi_cs(const Ctx& c, const Ad& a, int E) { return *(LAS f32x4*)(c.L + a.ca + E * 32); }
template <int HALF, int MLP, int E>
DI void g1_epi(const Ctx& c, const Ad& a, G1Out& o, f32x4& cs, v4u& fr) {
    constexpr int t = E >> 2, r0 = 4 * (E & 3), g = E & 1;
    constexpr bool neg = (g ^ MLP) != 0;
    float h[4];
    LV(cs); LA(o.acc[t]);
#pragma unroll
    for (int e = 0; e < 4; ++e) h[e] = __builtin_fmaf(-o.mb, cs[e], o.acc[t][r0 + e]);
#pragma unroll
    for (int e = 0; e < 2; ++e) {
        const u32 p = __builtin_bit_cast(u32, __builtin_elementwise_max(__builtin_bit_cast(s16x2, pk(h[2 * e], h[2 * e + 1])), s16x2{0, 0}));
        fr[2 * g + e] = neg ? (p | SGN) : p;
    }
    if (g == 0) LV(fr);
}
template <int HALF, int K> DI void h_store(const Ctx& c, const Ad& a, const v4u& fr) { st128<K * 1024>(fr, c.rws, a.l16, a.hst + HALF * 114688); }

#define NW0_ 0
#define NT0_ 0
constexpr int NT0 = NT0_;
#define NW1_ 2
constexpr int NW1 = NW1_;
#define KP_ 4
constexpr int KP = KP_;
#define TLBW_ 1
constexpr int TLBW = TLBW_;
#define SLP0 0
#define HT1 0
#define HT2 13
#define NW2 0
#define NW3 4
#define SLP1 16
constexpr int NW0 = NW0_;
#define EPE 8
constexpr int HR = 10, GL = 14, GC = 20;
template <int MLP> DI bool h_bad(const v4u& a, const v4u& b) {
    const u32 lo = MLP == 0 ? (a.x | a.y | b.x | b.y) : ~(a.x & a.y & b.x & b.y);
    const u32 hi = MLP == 0 ? ~(a.z & a.w & b.z & b.w) : (a.z | a.w | b.z | b.w);
    return ((lo | hi) & SGN) != 0u;
}
DI u32 h_base(const Ctx& c, int half) { return OFF_H + ((c.grp * 2 + half) * 112 + 4 * c.w) * 1024; }
template <bool ASM> DI v2u gate_ld(const Ctx& c, const Ad& a, int half) {
    const u32 vo = min(a.l16, 96u) * 1024, so = a.hst - c.j * 16384 + half * 114688 + 4;
    if constexpr (ASM) { v2u r; asm volatile("buffer_load_dwordx2 %0, %1, %2, %3 offen sc1" : "=v"(r) : "v"(vo), "s"(c.rws), "s"(so) : "memory"); return r; }
    else return __builtin_amdgcn_raw_buffer_load_b64(c.rws, vo, so, AUX_SC1);
}
template <int MLP> DI bool gate_bad(v2u x) { return ((MLP == 0 ? (x.x | ~x.y) : (~x.x | x.y)) & SGN) != 0u; }
template <int MLP> DI void gate_wait(const Ctx& c, const Ad& a, int half, v2u x) {
    if (ANY(gate_bad<MLP>(x))) {
        unsigned spins = 0;
        NOUNROLL do { MEMBAR(); x = gate_ld<false>(c, a, half); } while (ANY(gate_bad<MLP>(x)) && !spin_fail(c, spins, 1u));
    }
}
template <int N> DI v4u h_next(const Ctx& c, Ad& a) {
    constexpr int f = N >= 28 ? N - 28 : N;
    if constexpr (N >= 32 && N < 38) a.so[k_mem(f)] += 114688u;
    v4u r = __builtin_amdgcn_raw_buffer_load_b128(c.rws, a.l16 + k_sub(f) * 1024, a.so[k_mem(f)], AUX_SC1);
    if constexpr (N >= 50) a.so[k_mem(f)] -= 114688u;
    return r;
}
template <int MLP, int F_> DI void h_check(const Ctx& c0, v4u& a, v4u& b) {
    if (__builtin_expect(ANY(h_bad<MLP>(a, b)), 0)) {
        const Ctx c = fresh(c0);
        constexpr int half = F_ >= 28, f = F_ - 28 * half;
        const u32 vo = lane_id() * 16 + k_sub(f) * 1024, s0 = h_base(c, half) + member(c, k_mem(f)) * 16384, s1 = h_base(c, half) + member(c, k_mem(f + 1)) * 16384;
        unsigned spins = 0;
        NOUNROLL do {
            MEMBAR();
            a = ld128(c.rws, vo, s0); b = ld128(c.rws, vo, s1);
        } while (ANY(h_bad<MLP>(a, b)) && !spin_fail(c, spins, 3u));
    }
}
template <int HALF, int P> DI void red_write(const Ctx& c, const Ad& a, const AccT& acc) {
    LAS char* dst = c.L + (a.l16 + (P ^ c.w) * 6144) + (L_X + (1 - HALF) * 28672 + (P - 1) * 2048);
    const f32x16& x = acc[P >> 1];
    constexpr int r0 = (P & 1) * 8;
    *(LAS f32x4*)dst = f32x4{x[r0], x[r0 + 1], x[r0 + 2], x[r0 + 3]};
    *(LAS f32x4*)(dst + 1024) = f32x4{x[r0 + 4], x[r0 + 5], x[r0 + 6], x[r0 + 7]};
}
template <int HALF> DI void red_read(const Ctx& c, const Ad& a, f32x4 (&t)[6]) {
    LAS char* src = c.L + (a.l16 + c.w * 6144) + (L_X + (1 - HALF) * 28672);
#pragma unroll
    for (int i = 0; i < 6; ++i) t[i] = *(LAS f32x4*)(src + i * 1024);
}
template <int K> DI void post_half(const f32x4& o, float r, v4u& xres) {
#pragma unroll
    for (int e = 0; e < 2; ++e) xres[2 * K + e] = pk(__builtin_fmaf(o[2 * e], r, bflo(xres[2 * K + e])), __builtin_fmaf(o[2 * e + 1], r, bfhi(xres[2 * K + e])));
}
template <int HALF> DI void out_store(const Ctx& c, const Ad& a, const v4u& x, __amdgpu_buffer_rsrc_t rout) {
    auto r0 = __builtin_amdgcn_permlane32_swap(x.x, x.z, false, false);
    auto r1 = __builtin_amdgcn_permlane32_swap(x.y, x.w, false, false);
    const v4u v = {r0[0], r1[0], r0[1], r1[1]};
    const u32 vo = tok32(a) * (D / 8) + (a.l16 >> 9) * 16, so = (u32)((c.grp * 64 + HALF * 32) * D + 64 * c.j + 16 * c.w) * 2;
    asm volatile("buffer_store_dwordx4 %0, %1, %2, %3 offen\n\ts_nop 1" :: "v"(v), "v"(vo), "s"(rout), "s"(so) : "memory");
}
template <int HALF, int MLP> DI v4u xres_ld(const Ctx& c, const Ad& a) {
    if constexpr (MLP == 0) return trd2(c.L + (xtr_of(a.l16 >> 4) + (4 * c.j + c.w) * 1024) + (L_X + HALF * 28672), 512);
    else return *(LAS v4u*)(c.L + (a.l16 + (4 * c.j + c.w) * 1024) + (L_X + HALF * 28672));
}
template <int HALF> DI v4u y_ld(const Ctx& c, const Ad& a) { return trd2(c.L + (xtr_of(a.l16 >> 4) + c.w * 1024) + (L_X + HALF * 28672 + 24576), 512); }
DI void x3_make(v4u& x, const v4u& yf, float m, float r) {
    const float n = -m * r;
#pragma unroll
    for (int e = 0; e < 4; ++e) {
        const float p = bflo(x[e]), q = bfhi(x[e]);
        x[e] = pk(__builtin_fmaf(__builtin_fmaf(p, r, n), bflo(yf[e]), p), __builtin_fmaf(__builtin_fmaf(q, r, n), bfhi(yf[e]), q));
    }
}
template <int HALF> DI void x3_store(const Ctx& c, const Ad& a, const v4u& x) { st128(x, c.rws, a.l16, a.x3o + HALF * 28672); }
template <int HALF, int I> DI v4u x3_ld1(const Ctx& c, const Ad& a) {
    return ld128(c.rws, a.l16 + (I & 3) * 1024, a.x3o + (6 * c.w - 4 * c.j + (I & 4)) * 1024 + HALF * 28672);
}
DI u32 max3u(u32 a, u32 b, u32 c) { u32 r; asm("v_max3_u32 %0, %1, %2, %3" : "=v"(r) : "v"(a), "v"(b), "v"(c)); return r; }
DI bool x3_bad(const v4u (&t)[7]) {
    u32 mx = 0;
#pragma unroll
    for (int i = 0; i < 7; ++i) mx = max3u(max3u(mx, t[i].x, t[i].y), t[i].z, t[i].w);
    return ANY(mx == ~0u);
}
template <bool EQ> DI void gate_spin(const Ctx& c, u32 vo, u32 so, u32 word, u32 code) {
    unsigned spins = 0;
    NOUNROLL for (;;) {
        MEMBAR();
        const u32 x = __builtin_amdgcn_raw_buffer_load_b32(c.rws, vo, so, AUX_SC1);
        if (!ANY(EQ ? x == word : x != word)) break;
        if (spin_fail(c, spins, code)) break;
    }
}
template <int HALF> DI void x3_wait(const Ctx& c, const Ad& a, v4u (&t)[7]) {
    if (x3_bad(t)) {
        unsigned spins = 0;
        NOUNROLL do { MEMBAR(); sfor<0, 7>([&](auto IC) { t[decltype(IC)::value] = x3_ld1<HALF, decltype(IC)::value>(c, a); }); } while (x3_bad(t) && !spin_fail(c, spins, 4u));
    }
}
template <int HALF, int I> DI void x3_put1(const Ctx& c, const Ad& a, const v4u& t) { *(LAS v4u*)(c.L + (a.l16 + c.w * 7168) + (L_X + HALF * 28672 + I * 1024)) = t; }

template <int MLP>
DI void g2_pair(const Ctx& c, Ad& a, const W2T& W2, v4u (&hb)[56], G1Out& gb, f32x4 (&csq)[3], float r0, v4u (&xres)[2], __amdgpu_buffer_rsrc_t out) {
    const u32 wl0 = a.l16 + L_W + c.w * 24576;
    AccT acc[2];
    v2u gate = {0, 0};
    v4u wl[2], fr, yf;
    f32x4 o[2], u[2];
    float s1, s2;
    Gath gs;
    const float ra_ = MLP ? -r0 : r0, rb_ = MLP ? -gb.r : gb.r;
    auto wres = [&](int slot, int k) { return *(LAS v4u*)(c.L + wl0 + (slot * 2048 + k * 1024)); };
    auto fetch = [&](auto NC) {
        constexpr int N = decltype(NC)::value;
        if constexpr (N < 56) {
            if constexpr (N >= 28 && N < 32) hb[N] = *(LAS v4u*)(c.L + (a.l16 + c.w * 4096) + (L_X + (N - 28) * 1024));
            else hb[N] = h_next<N>(c, a);
        }
    };
    auto rd = [&](int i) { return *(LAS f32x4*)(c.L + (a.l16 + c.w * 6144) + (L_X + 28672 + i * 1024)); };
    auto own = [&](int k) { return f32x4{acc[0][0][4 * k], acc[0][0][4 * k + 1], acc[0][0][4 * k + 2], acc[0][0][4 * k + 3]}; };
    wl[0] = wres(0, 0); wl[1] = wres(0, 1);
    SB();
    sfor<0, 28>([&](auto PC) {
        constexpr int F_ = 2 * decltype(PC)::value, H = F_ >= 28, f = F_ - 28 * H;
        constexpr bool res = f < 24;
        constexpr int r0_ = f < 24 ? f >> 1 : f - 12;
        if constexpr (F_ == 24 || F_ == 30 || (MLP == 0 && F_ == 36)) { bar(); SB(); stat_pub_if<MLP == 0 && F_ == 36>(c, a); }
        if constexpr (F_ % 4 == 0) STAMP2((MLP ? PS1 : PS0) + 15 + F_ / 4);
        asm volatile("" : "+v"(a.l16), "+v"(a.ca));
        acc[H][0] = mfma32(W2[r0_][0], hb[F_], f == 0 ? zero16() : acc[H][0]);
        acc[H][1] = mfma32(W2[r0_][1], hb[F_], f == 0 ? zero16() : acc[H][1]);
        fetch(std::integral_constant<int, F_ + (F_ < 8 ? HR - 2 : HR)>{});
        fetch(std::integral_constant<int, F_ + 1 + (F_ < 8 ? HR - 2 : HR)>{});
        if constexpr (F_ == 6) { fetch(std::integral_constant<int, F_ + HR>{}); fetch(std::integral_constant<int, F_ + HR + 1>{}); }
        if constexpr (F_ < 6) csq[(F_ + 2) % 3] = epi_cs(c, a, F_ + 2);
        if constexpr (F_ < 8) g1_epi<1, MLP, F_>(c, a, gb, csq[F_ % 3], fr);
        if constexpr (F_ < 6) csq[F_ % 3] = epi_cs(c, a, F_ + 3);
        if constexpr (F_ < 8) g1_epi<1, MLP, F_ + 1>(c, a, gb, csq[(F_ + 1) % 3], fr);
        if constexpr (F_ == 32) { LA(acc[0][0]); LV(o[0]); LV(u[0]); LV(u[1]); o[0] = (o[0] + own(0)) + (u[0] + u[1]); post_half<0>(o[0], ra_, xres[0]); LV(xres[0]); }
        if constexpr (F_ == 34) { LA(acc[0][0]); LV(o[0]); LV(u[0]); LV(u[1]); o[0] = (o[0] + own(1)) + (u[0] + u[1]); post_half<1>(o[0], ra_, xres[0]); LV(xres[0]); }
        acc[H][0] = mfma32(res ? wl[0] : W2[r0_ + 1][0], hb[F_ + 1], acc[H][0]);
        acc[H][1] = mfma32((res || f == 26) ? wl[1] : W2[r0_ + 1][1], hb[F_ + 1], acc[H][1]);
        if constexpr (f < 22) { wl[0] = wres(1 + (f >> 1), 0); wl[1] = wres(1 + (f >> 1), 1); }
        if constexpr (f == 24) wl[1] = *(LAS v4u*)(c.L + (a.l16 + c.w * 1024) + L_JK);
        if constexpr (F_ == 26) { wl[0] = wres(0, 0); wl[1] = wres(0, 1); }
        if constexpr (F_ == 22) xres[1] = xres_ld<1, MLP>(c, a);
        if constexpr (F_ == 30) { u[0] = rd(0); u[1] = rd(2); o[0] = rd(4); }
        if constexpr (F_ == 32) { u[0] = rd(1); u[1] = rd(3); o[0] = rd(5); }
        if constexpr (MLP == 0 && F_ == 54) yf = y_ld<0>(c, a);
        if constexpr (F_ == GL) gate = gate_ld<false>(c, a, 1);
        if constexpr (F_ < 8) { *(LAS v4u*)(c.L + (a.l16 + c.w * 4096) + (L_X + (F_ / 2) * 1024)) = fr; h_store<1, F_ / 2>(c, a, fr); }
        if constexpr (MLP == 0 && (F_ == 24 || F_ == 26))
            dma16<true>(c.lbase + L_X + (F_ - 24) / 2 * 28672 + 24576 + c.w * 1024, a.l16, c.rws, OFF_YT + (c.grp * 2 + (F_ - 24) / 2) * 28672 + c.j * 4096 + c.w * 1024);
        if constexpr (F_ == 28) { red_write<0, 1>(c, a, acc[0]); red_write<0, 2>(c, a, acc[0]); red_write<0, 3>(c, a, acc[0]); }
        if constexpr (MLP == 0 && F_ == 34) { frag_sums(xres[0], s1, s2); part_put(c, a, L_S2, s1, s2); }
        if constexpr (MLP == 1 && F_ == 36) out_store<0>(c, a, xres[0], out);
        ilv<4, 5>();
        if constexpr (F_ + 2 < 56 && f + 2 != 28 && f + 2 >= 4) h_check<MLP, F_ + 2>(c, hb[F_ + 2], hb[F_ + 3]);
        if constexpr (F_ == GC) gate_wait<MLP>(c, a, 1, gate);
        SB();
    });
    static_assert(GC + 2 + HR <= 32 && (GC & 1) == 0 && (HR & 1) == 0, "half B's first foreign fragment is requested behind the gate");
    STAMP2((MLP ? PS1 : PS0) + 29);
    if constexpr (MLP == 0) STAMP4(0);
    red_write<1, 1>(c, a, acc[1]); red_write<1, 2>(c, a, acc[1]); red_write<1, 3>(c, a, acc[1]);
    if constexpr (MLP == 0) sfor<0, 4>([&](auto IC) { gath_ld<0, 0, decltype(IC)::value>(c, a, gs); });
    bar();
    if constexpr (MLP == 0) STAMP4(1);
    f32x4 t[6];
    red_read<1>(c, a, t);
    o[0] = (f32x4{acc[1][0][0], acc[1][0][1], acc[1][0][2], acc[1][0][3]} + t[0]) + (t[2] + t[4]);
    o[1] = (f32x4{acc[1][0][4], acc[1][0][5], acc[1][0][6], acc[1][0][7]} + t[1]) + (t[3] + t[5]);
    post_half<0>(o[0], rb_, xres[1]); post_half<1>(o[1], rb_, xres[1]);
    if constexpr (MLP == 0) {
        frag_sums(xres[1], s1, s2); part_put(c, a, L_S2, s1, s2);
        STAMP4(2);
        bar();
        stat_pub<1, 0>(c, a, L_S2, 1);
        STAMP4(3);
        gath_wait<0, 0>(c, a, gs);
        gath_ln(gs, s1, s2);
        x3_make(xres[0], yf, s1, s2);
        x3_store<0>(c, a, xres[0]);
        STAMP4(4);
        yf = y_ld<1>(c, a);
        __builtin_amdgcn_s_sleep(SLP1);
        v4u x[7];
        unsigned spins = 0;
        NOUNROLL do {
            MEMBAR();
            sfor<0, 4>([&](auto IC) { gath_ld<1, 0, decltype(IC)::value>(c, a, gs); });
            sfor<0, 7>([&](auto IC) { x[decltype(IC)::value] = x3_ld1<0, decltype(IC)::value>(c, a); });
        } while ((gath_bad(c, gs) || x3_bad(x)) && !spin_fail(c, spins, 4u));
        STAMP4(5);
        gath_ln(gs, s1, s2);
        x3_make(xres[1], yf, s1, s2);
        x3_store<1>(c, a, xres[1]);
        sfor<0, 7>([&](auto IC) { x3_put1<0, decltype(IC)::value>(c, a, x[decltype(IC)::value]); });
    } else out_store<1>(c, a, xres[1], out);
}

__global__ void __launch_bounds__(256, 1) fused_kernel(const u16* __restrict__ x0, const u16* __restrict__ kin, const u16* __restrict__ vin,
                                                       const u16* __restrict__ sk, const u16* __restrict__ hk, const u16* __restrict__ Dv,
                                                       const u16* __restrict__ w1, const u16* __restrict__ w2,
                                                       unsigned char* __restrict__ ws, u16* __restrict__ out) {
    extern __shared__ __attribute__((aligned(16))) char smem[];
    Ctx c;
    c.L = (LAS char*)smem;
    c.lbase = (u32)(uintptr_t)c.L;
    const int b = blockIdx.x, tid = threadIdx.x;
    c.w = __builtin_amdgcn_readfirstlane(tid >> 6);
    c.rws = mkrs(ws, WS_BYTES);
    c.tmo = (GAS u32*)(ws + 256);
    STAMP(0);
    STAMP3(0);

    if (b < 32) {
        const int lane = tid & 63;
        const __amdgpu_buffer_rsrc_t rx = mkrs(x0, TSZ), rw1 = mkrs(w1, D * F * 2), rw2 = mkrs(w2, D * F * 2);
        v4u ep = ld128_asm<true>(c.rws, 0u, OFF_EP);
        v4u xr[2][7];
        sfor<0, 14>([&](auto IC) { constexpr int i = decltype(IC)::value; xr[i / 7][i % 7] = ld128_asm<false, (i / 7) * 896 + (i % 7) * 128>(rx, (u32)((tid >> 3) * 1792 + (tid & 7) * 16), (u32)b * 64 * 896); });
        {
            unsigned long long pc; u32 back, len;
            asm volatile("s_getpc_b64 %0\n.Lfk_here:\n\ts_mov_b32 %1, .Lfk_here-.Lfk_begin\n\ts_mov_b32 %2, .Lfk_end-.Lfk_begin" : "=s"(pc), "=s"(back), "=s"(len));
            touch(c.lbase + L_JK, (u32)(lane & 31) * 128, mkrs((const void*)(pc - back), len), (u32)((b >> 3) * 4 + c.w) * 4096);
        }
        const int blk = min(b * 4 + c.w, 111);
        const u32 vo0 = w_voff<28>(lane), base = c.lbase + 16384 + c.w * 14336;
        asm volatile("s_waitcnt vmcnt(1)" : "+v"(ep), "+v"(xr[0][0]), "+v"(xr[0][1]), "+v"(xr[0][2]), "+v"(xr[0][3]), "+v"(xr[0][4]), "+v"(xr[0][5]), "+v"(xr[0][6]),
                     "+v"(xr[1][0]), "+v"(xr[1][1]), "+v"(xr[1][2]), "+v"(xr[1][3]), "+v"(xr[1][4]), "+v"(xr[1][5]), "+v"(xr[1][6]) :: "memory");
        const u32 tA = ep.x + 1u, tB = ep.y + 1u;
        u32 res[2];
#pragma unroll
        for (int h = 0; h < 2; ++h) {
            float s1 = 0.f, s2 = 0.f;
#pragma unroll
            for (int i = 0; i < 7; ++i)
#pragma unroll
                for (int e = 0; e < 4; ++e) { s1 = dot2(xr[h][i][e], 0x3f803f80u, s1); s2 = dot2(xr[h][i][e], xr[h][i][e], s2); }
#pragma unroll
            for (int o = 1; o < 8; o <<= 1) { s1 += __shfl_xor(s1, o); s2 += __shfl_xor(s2, o); }
            const float m = s1 * (1.0f / D);
            res[h] = pk(m, ln_sd(fmaxf(__builtin_fmaf(-m, m, s2 * (1.0f / D)), 0.f)));
        }
        if ((tid & 7) == 0) st128(v4u{res[0], tA, res[1], tB}, c.rws, (u32)(b * 32 + (tid >> 3)) * 16, OFF_ST0);
        sfor<0, 7>([&](auto JC) { dma_pair<decltype(JC)::value * 2048>(base, vo0, vo0 + 8 * (D * 2) - 1024, rw2, (u32)(blk * 16 * (D * 2) + decltype(JC)::value * 128)); });
        const int rg = tid >> 3, c8 = min(tid & 7, 6);
        v4u wr[14];
        sfor<0, 14>([&](auto IC) { wr[decltype(IC)::value] = ld128_asm<false>(rw1, (u32)(rg * 14 * (F * 2) + c8 * 16), (u32)(b * 112 + decltype(IC)::value * (F * 2))); });
        {
            Ctx h = c; h.w = (blk >> 2) & 3;
            const u32 wl = wl2_of(h, lane) - L_W - h.w * 24576 + 16384 + c.w * 14336;
            WAIT_VM(14);
#pragma unroll
            for (int jj = 0; jj < 7; ++jj)
#pragma unroll
                for (int t = 0; t < 2; ++t) {
                    v4u x = trd2(c.L + (wl ^ (u32)(t * 64)) + jj * 2048, 1024);
                    x.z ^= SGN; x.w ^= SGN;
                    st128(x, c.rws, (u32)lane * 16, OFF_WT2 + (u32)((((jj * 4 + h.w) * 7 + (blk >> 4)) * 4 + (blk & 3)) * 2 + t) * 1024);
                }
        }
        asm volatile("s_waitcnt vmcnt(14)" : "+v"(wr[0]), "+v"(wr[1]), "+v"(wr[2]), "+v"(wr[3]), "+v"(wr[4]), "+v"(wr[5]), "+v"(wr[6]),
                     "+v"(wr[7]), "+v"(wr[8]), "+v"(wr[9]), "+v"(wr[10]), "+v"(wr[11]), "+v"(wr[12]), "+v"(wr[13]) :: "memory");
        float cs[8] = {0, 0, 0, 0, 0, 0, 0, 0};
#pragma unroll
        for (int i = 0; i < 14; ++i)
#pragma unroll
            for (int e = 0; e < 4; ++e) { cs[2 * e] += bflo(wr[i][e]); cs[2 * e + 1] += bfhi(wr[i][e]); }
        LAS float* sf = (LAS float*)c.L;
#pragma unroll
        for (int e = 0; e < 8; ++e) sf[rg * 65 + (tid & 7) * 8 + e] = cs[e];
        WAIT_VM(0);
        bar();
        if (tid == 64) __builtin_amdgcn_raw_buffer_store_b64(v2u{tA, tB}, c.rws, (u32)b * 8, OFF_FL2, AUX_SC1);
        if (tid < 56) {
            float t = 0.f;
#pragma unroll
            for (int i = 0; i < 32; ++i) t += sf[i * 65 + tid];
            const float t2 = __shfl_down(t, 1);
            if ((tid & 1) == 0) st128(v4u{__float_as_uint(t), tA, __float_as_uint(t2), tB}, c.rws, (u32)(b * 28 + (tid >> 1)) * 16, OFF_CSG);
        }
        return;
    }
    asm volatile(".Lfk_begin:");
    const int wb = b - 32;
    { const int xcd = wb & 7, idx = wb >> 3; c.grp = xcd * 4 + idx / 7; c.j = idx % 7; }
    c.gi = c.grp & 3;
    const int w = c.w;
    c.rw1 = mkrs(w1, D * F * 2); c.rw2 = mkrs(w2, D * F * 2);
    c.kb0 = (w + c.gi + 2) & 3;
    const int cp = (wb & 7) * 28 + (wb >> 3);
    W1T W1; W2T W2;

    u32 dval;
    {
        const int g = wb % 7, rb = wb / 7, lane = tid & 63;
        const int rg = (lane & 7) * 2 + ((lane >> 4) & 1), cl = ((lane >> 3) & 1) + 2 * (lane >> 5), cq = 4 * w + cl;
        const __amdgpu_buffer_rsrc_t rk = mkrs(kin, TSZ), rv = mkrs(vin, TSZ), rx = mkrs(x0, TSZ), rh = mkrs(hk, TSZ), rs_ = mkrs(sk, D * 8), rd = mkrs(Dv, D * 2);
        STAMP3(1);
        const u32 tb = (u32)((rb * 64) * 896 + g * 128);
        const u32 tv0 = (u32)((lane >> 3) * 896) + (((lane & 7) ^ ((lane >> 5) + 2 * w)) & 7) * 16;
        dma16<false>(c.lbase + T_X + w * 1024, tv0, rx, tb + w * 7168);
        dma16<false>(c.lbase + T_X + (w + 4) * 1024, tv0, rx, tb + (w + 4) * 7168);
        dma16<false>(c.lbase + T_K + w * 1024, tv0 + tb + (w - 1) * 7168, rk, 0u);
        dma16<false>(c.lbase + T_K + (w + 4) * 1024, tv0, rk, tb + (w + 3) * 7168);
        dma16<false>(c.lbase + T_V + w * 1024, tv0, rv, tb + w * 7168);
        dma16<false>(c.lbase + T_V + (w + 4) * 1024, tv0, rv, tb + (w + 4) * 7168);
        if (w == 0) dma16<false>(c.lbase + T_K + 8192, tv0, rk, tb + 7 * 7168);
        else dma16<false>(c.lbase + T_S + (w - 1) * 1024, (u32)lane * 16, rs_, g * 512);
        v4u ep4 = ld128_asm<true>(c.rws, 0u, OFF_EP);
        v4u he[2];
        he[0] = ld128_asm<false>(rh, tid * 16, cp * 8192); he[1] = ld128_asm<false>(rh, tid * 16, cp * 8192 + 4096);
        v4u dv4 = ld128_asm<false>(rd, 0u, cp * 4);
        {
            const WSt wt = w_st<0>(c, lane);
            sfor<0, NW0>([&](auto NC) { w_dma<decltype(NC)::value>(c, wt, w_soff<decltype(NC)::value>(c)); });
        }
        if constexpr (TLBW) {
            if (w == 0) touch(c.lbase + L_JK + 3072, (u32)lane * 524288u, c.rws, 4096u);
            else touch(c.lbase + L_JK + 3072, (u32)lane * 262144u, w == 1 ? c.rw1 : w == 2 ? c.rw2 : mkrs(out, TSZ), 0u);
        }
        if constexpr (NT0) touch(c.lbase + L_JK + 3072, (u32)lane * 128, c.rw1, (u32)((wb >> 3) * 4 + w) * 8192);
        STAMP3(2);
        asm volatile("s_waitcnt vmcnt(%1)" : "+v"(ep4) : "i"(3 + NT0 + TLBW + 2 * NW0) : "memory");
        c.tagA = __builtin_amdgcn_readfirstlane(ep4.x) + 1u; c.tagB = __builtin_amdgcn_readfirstlane(ep4.y) + 1u;
        bar();
        STAMP(1); STAMP3(3);
        const u32 a1 = (u32)(rg * 512 + (((cq >> 1) ^ rg) & 7) * 16 + (cq & 1) * 8);
        const u32 ak1 = (u32)(rg * 512 + 1024 + (((cq >> 1) ^ (rg + 2)) & 7) * 16 + (cq & 1) * 8);
        const u32 ak0 = (u32)(rg * 512 + 512 + (((cq >> 1) ^ (rg + 1)) & 7) * 16 + (cq & 1) * 8);
        v2u kr[7], vr[4], xr[4];
#pragma unroll
        for (int i = 0; i < 4; ++i) xr[i] = *(LAS v2u*)(c.L + T_X + a1 + i * 128);
        const v4u w01 = *(LAS v4u*)(c.L + T_S + cq * 32), w23 = *(LAS v4u*)(c.L + T_S + cq * 32 + 16);
#pragma unroll
        for (int i = 0; i < 3; ++i) kr[i] = *(LAS v2u*)(c.L + T_K + ak0 + (i + 1) * 128);
#pragma unroll
        for (int i = 0; i < 4; ++i) { kr[3 + i] = *(LAS v2u*)(c.L + T_K + ak1 + i * 128); vr[i] = *(LAS v2u*)(c.L + T_V + a1 + i * 128); }
#pragma unroll
        for (int h = 0; h < 2; ++h) {
            auto r0 = __builtin_amdgcn_permlane16_swap(__builtin_amdgcn_perm(xr[1][h], xr[0][h], 0x05040100u), __builtin_amdgcn_perm(xr[1][h], xr[0][h], 0x07060302u), false, false);
            auto r1 = __builtin_amdgcn_permlane16_swap(__builtin_amdgcn_perm(xr[3][h], xr[2][h], 0x05040100u), __builtin_amdgcn_perm(xr[3][h], xr[2][h], 0x07060302u), false, false);
            st128(v4u{r0[0], r1[0], r0[1], r1[1]}, c.rws, (u32)((cl * 4 + ((lane >> 4) & 1)) * 4096 + (lane & 7) * 16), OFF_X0G + (g * 64 + 16 * w + 2 * h) * 4096 + rb * 128);
        }
        STAMP3(27);
        v4u kg[4];
#pragma unroll
        for (int e = 0; e < 4; ++e) {
            const v4u& wq = e < 2 ? w01 : w23;
            const float w0 = bflo(wq[(e & 1) * 2]), w1_ = bfhi(wq[(e & 1) * 2]), w2_ = bflo(wq[(e & 1) * 2 + 1]), w3 = bfhi(wq[(e & 1) * 2 + 1]);
            float kk[7], o[4];
#pragma unroll
            for (int i = 0; i < 7; ++i) kk[i] = (e & 1) ? bfhi(kr[i][e >> 1]) : bflo(kr[i][e >> 1]);
#pragma unroll
            for (int i = 0; i < 4; i += 2) {
                u32 a_ = pk(kk[i] * w3, kk[i + 1] * w3), b_ = pk(kk[i + 1] * w2_, kk[i + 2] * w2_);
                a_ = pk(bflo(a_) + bflo(b_), bfhi(a_) + bfhi(b_)); b_ = pk(kk[i + 2] * w1_, kk[i + 3] * w1_);
                a_ = pk(bflo(a_) + bflo(b_), bfhi(a_) + bfhi(b_)); b_ = pk(kk[i + 3] * w0, kk[i + 4] * w0);
                a_ = pk(bflo(a_) + bflo(b_), bfhi(a_) + bfhi(b_));
                o[i] = bflo(a_) * ((e & 1) ? bfhi(vr[i][e >> 1]) : bflo(vr[i][e >> 1]));
                o[i + 1] = bfhi(a_) * ((e & 1) ? bfhi(vr[i + 1][e >> 1]) : bflo(vr[i + 1][e >> 1]));
            }
            kg[e] = v4u{pk(o[3], o[2]), c.tagA, pk(o[1], o[0]), c.tagB};
        }
        STAMP3(28);
        asm volatile("s_waitcnt vmcnt(0)\n\ts_barrier" : "+v"(he[0]), "+v"(he[1]), "+v"(dv4), "+v"(kg[0]), "+v"(kg[1]), "+v"(kg[2]), "+v"(kg[3]) :: "memory");
        STAMP3(29);
#pragma unroll
        for (int e = 0; e < 4; ++e) st128(kg[e], c.rws, (u32)(cl * 4 * 8192 + (15 - rg) * 16), OFF_KVG + ((g * 64 + 16 * w + e) * 512 + 496 - 16 * rb) * 16);
        {
            const WSt wt = w_st<0>(c, lane);
            sfor<NW0, NW1>([&](auto NC) { w_dma<decltype(NC)::value>(c, wt, w_soff<decltype(NC)::value>(c)); });
        }
        STAMP(2); STAMP3(4);
        dval = (w >> 1) ? dv4.x >> 16 : dv4.x & 0xffffu;
#pragma unroll
        for (int cc = 0; cc < 2; ++cc) {
            LAS char* cb = c.L + CV_B + cc * CV_CH;
            *(LAS v4u*)(cb + CV_HE + 32 + tid * 16) = he[cc];
            if (tid < 2) { *(LAS v4u*)(cb + CV_HE + tid * 16) = v4u{0, 0, 0, 0}; *(LAS v4u*)(cb + CV_HO + tid * 16) = v4u{0, 0, 0, 0}; }
            if (tid < 32) *(LAS v4u*)(cb + CV_KV + 4096 + tid * 16) = v4u{0, 0, 0, 0};
        }
        bar();
#pragma unroll
        for (int cc = 0; cc < 2; ++cc) {
            const v4u e = he[cc];
            const u32 pv = *(LAS u32*)(c.L + CV_B + cc * CV_CH + CV_HE + 28 + tid * 16);
            *(LAS v4u*)(c.L + CV_B + cc * CV_CH + CV_HO + 32 + tid * 16) = v4u{__builtin_amdgcn_alignbit(e.x, pv, 16), __builtin_amdgcn_alignbit(e.y, e.x, 16),
                                                                            __builtin_amdgcn_alignbit(e.z, e.y, 16), __builtin_amdgcn_alignbit(e.w, e.z, 16)};
        }
        bar();
        STAMP3(5);
        SB();
    }
    {
        const int lane = lane_id(), i16 = lane & 15, q = lane >> 4, cc = w >> 1, p = w & 1, ch = 2 * cp + cc;
        LAS char* cb = c.L + CV_B + cc * CV_CH;
        const int o0 = i16 + 1 + 32 * p + 8 * q;
        LAS char* ab = cb + ((o0 & 1) ? CV_HO + 4 : CV_HE) + (o0 >> 1) * 4;
        v4u A[32];
        v4u AD;
        {
            const int jj = p ? -1 : 15 - i16 - 8 * q;
            const u32 dv = dval << ((jj & 1) * 16);
#pragma unroll
            for (int d = 0; d < 4; ++d) AD[d] = (jj >= 0 && (jj >> 1) == d) ? dv : 0u;
        }
        const Ctx cs_ = fresh(c);
        const u32 wl = wl1_of(cs_, lane);
        const WSt wt = w_st<0>(cs_, lane);
        v4u sg[4], g[4];
        SB();
        sfor<0, 8>([&](auto NC) {
            constexpr int n = decltype(NC)::value;
#pragma unroll
            for (int t = n * 4; t < n * 4 + 4; ++t) { LAS u32* pp = (LAS u32*)(ab + t * 128); A[t] = v4u{pp[0], pp[1], pp[2], pp[3]}; }
            SB();
        });
        STAMP(3); STAMP3(6);
        {
            __builtin_amdgcn_s_sleep(SLP0);
            unsigned spins = 0;
            auto bad4 = [&](const v4u (&x)[4]) { u32 d = 0; for (int t = 0; t < 4; ++t) d = tag_bad(x[t], c.tagA, c.tagB, d); return ANY(d != 0u); };
            NOUNROLL do {
                MEMBAR();
#pragma unroll
                for (int i = 0; i < 4; ++i) g[i] = ld128(c.rws, tid * 16, OFF_KVG + cp * 16384 + i * 4096);
            } while (bad4(g) && !spin_fail(c, spins, 5u));
            STAMP3(30);
#pragma unroll
            for (int i = 0; i < 4; ++i) *(LAS v2u*)(c.L + CV_B + (i >> 1) * CV_CH + CV_KV + (tid + 256 * (i & 1)) * 8) = v2u{g[i].x, g[i].z};
        }
        bar();
        STAMP(4); STAMP3(7);
        LAS char* bb = cb + CV_KV + (2032 - 16 * i16 + 32 * p + 8 * q) * 2;
        f32x4 acc[8];
        float xs[4][4], qs[4][4];
        v4u B[32];
        v2u xg[4];
        B[0] = *(LAS v4u*)(bb + 3 * 128); B[1] = *(LAS v4u*)(bb + 2 * 128); B[2] = *(LAS v4u*)(bb + 128);
        const u32 s4 = (u32)(256 * p + 4 * i16 + q), xo = OFF_X0G + ch * 4096;
        SB();
        sfor<0, 32>([&](auto OC) {
            constexpr int oi = decltype(OC)::value, om = oi - 3;
            constexpr int nm = 8 - (om > 0 ? (om + 3) / 4 : 0) + ((om >= 0 && (om & 3) == 0) ? 1 : 0);
            if constexpr (oi % 4 == 0) STAMP3(8 + oi / 4);
            if constexpr (oi == 9) { bar(); SB(); }
            if constexpr (oi + 3 < 32) B[oi + 3] = *(LAS v4u*)(bb - (om + 3) * 128);
#pragma unroll
            for (int cb_ = 7; cb_ >= 0; --cb_)
                if (4 * cb_ - om >= 0) acc[cb_] = mfma16(A[4 * cb_ - om], B[oi], oi == 0 ? f32x4{0, 0, 0, 0} : acc[cb_]);
            if constexpr (om >= 0 && (om & 3) == 0) acc[om >> 2] = mfma16(AD, B[oi], acc[om >> 2]);
            if constexpr (oi < 2) { sg[2 * oi] = ld128_asm<true>(c.rws, tid * 16, OFF_ST0 + oi * 8192); sg[2 * oi + 1] = ld128_asm<true>(c.rws, tid * 16, OFF_ST0 + oi * 8192 + 4096); }
            if constexpr (oi == 2 || oi == 3) {
                asm volatile("buffer_load_dwordx2 %0, %2, %3, %4 offen offset:%5 sc1\n\tbuffer_load_dwordx2 %1, %2, %3, %4 offen offset:%6 sc1"
                             : "=&v"(xg[2 * oi - 4]), "=&v"(xg[2 * oi - 3]) : "v"(s4 * 8), "s"(c.rws), "s"(xo), "i"((oi - 2) * 1024), "i"((oi - 2) * 1024 + 512) : "memory");
            }
            if constexpr (oi < NW1) w_read1<oi>(cs_, wl, W1, W2);
            if constexpr (oi >= 4 && (oi & 1) == 0 && NW1 + (oi - 4) / 2 < 12) w_dma<NW1 + (oi - 4) / 2>(cs_, wt, w_soff<NW1 + (oi - 4) / 2>(cs_));
            if constexpr (oi == 7) {
                asm volatile("s_waitcnt vmcnt(4)" : "+v"(sg[0]), "+v"(sg[1]), "+v"(sg[2]), "+v"(sg[3]), "+v"(xg[0]), "+v"(xg[1]), "+v"(xg[2]), "+v"(xg[3]) :: "memory");
                auto bad = [&]() { u32 d = 0; for (int t = 0; t < 4; ++t) d = tag_bad(sg[t], c.tagA, c.tagB, d); return ANY(d != 0u); };
                if (bad()) {
                    unsigned spins = 0;
                    NOUNROLL do {
                        MEMBAR();
#pragma unroll
                        for (int t = 0; t < 4; ++t) sg[t] = ld128(c.rws, tid * 16, OFF_ST0 + t * 4096);
                    } while (bad() && !spin_fail(c, spins, 6u));
                }
#pragma unroll
                for (int t = 0; t < 4; ++t) *(LAS v2u*)(c.L + CV_ST + (tid + 256 * t) * 8) = v2u{sg[t].x, sg[t].z};
            }
            if constexpr (oi >= 9 && oi < 13) sg[oi - 9] = *(LAS v4u*)(c.L + CV_ST + s4 * 16 + (oi - 9) * 1024);
            if constexpr (oi >= 11 && oi < 19) {
                constexpr int t = (oi - 11) >> 1, hf = (oi - 11) & 1;
                LV(xg[t]); LV(sg[t]);
                const u32 xw = xg[t][hf];
                const u32 sa = sg[t][2 * hf], sb = sg[t][2 * hf + 1];
                xs[t][2 * hf] = bflo(xw); xs[t][2 * hf + 1] = bfhi(xw);
                qs[t][2 * hf] = (xs[t][2 * hf] - bflo(sa)) * __builtin_amdgcn_rcpf(bfhi(sa));
                qs[t][2 * hf + 1] = (xs[t][2 * hf + 1] - bflo(sb)) * __builtin_amdgcn_rcpf(bfhi(sb));
                asm volatile("" : "+v"(qs[t][2 * hf]), "+v"(qs[t][2 * hf + 1]));
            }
            if constexpr (oi == 23 || oi == 25) {
                const v4u ff = {~0u, ~0u, ~0u, ~0u};
                st128(ff, c.rws, lane * 16, OFF_X3 + (cs_.grp * 56 + (oi - 23) * 14 + 4 * cs_.j + cs_.w) * 1024);
            }
            ilv<nm, 2>();
            SB();
        });
        STAMP(5); STAMP3(16);
        LAS char* ex = c.L;
        f32x4 fin[4];
        if (p == 0) {
#pragma unroll
            for (int t = 0; t < 4; ++t) { *(LAS f32x4*)(ex + w * 4096 + t * 1024 + lane * 16) = acc[4 + t]; fin[t] = acc[t]; }
        } else {
#pragma unroll
            for (int t = 0; t < 4; ++t) { *(LAS f32x4*)(ex + w * 4096 + t * 1024 + lane * 16) = acc[t]; fin[t] = acc[4 + t]; }
        }
        bar();
        STAMP3(17);
        const u32 toff = (u32)((32 * p + 8 * (q & 1) + (i16 >> 1)) * 28672 + ch * 64 + (i16 & 1) * 32 + (q >> 1) * 16);
        v2u y[4], x1[4];
#pragma unroll
        for (int t = 0; t < 4; ++t) {
            fin[t] += *(LAS f32x4*)(ex + (w ^ 1) * 4096 + t * 1024 + lane * 16);
            y[t] = v2u{pk(fin[t][0], fin[t][1]), pk(fin[t][2], fin[t][3])};
            x1[t] = v2u{pk(__builtin_fmaf(qs[t][0], bflo(y[t].x), xs[t][0]), __builtin_fmaf(qs[t][1], bfhi(y[t].x), xs[t][1])),
                        pk(__builtin_fmaf(qs[t][2], bflo(y[t].y), xs[t][2]), __builtin_fmaf(qs[t][3], bfhi(y[t].y), xs[t][3]))};
        }
        auto merge = [&](const v2u& a, const v2u& b_) {
            auto r0 = __builtin_amdgcn_permlane16_swap(a.x, b_.x, false, false);
            auto r1 = __builtin_amdgcn_permlane16_swap(a.y, b_.y, false, false);
            return v4u{r0[0], r1[0], r0[1], r1[1]};
        };
        st128(merge(x1[0], x1[1]), c.rws, toff, OFF_X1);
        st128(merge(x1[2], x1[3]), c.rws, toff, OFF_X1 + 16 * 28672);
        st128(merge(y[0], y[1]), c.rws, toff, OFF_YT);
        st128(merge(y[2], y[3]), c.rws, toff, OFF_YT + 16 * 28672);
        STAMP(6); STAMP3(18);
        sfor<12, 12 + NW1>([&](auto NC) { w_dma<decltype(NC)::value>(cs_, wt, w_soff<decltype(NC)::value>(cs_)); });
        WAIT_VM(6 + 2 * NW1);
        sfor<NW1, 12>([&](auto NC) { w_read1<decltype(NC)::value>(cs_, wl, W1, W2); });
        WAIT_LGKM(0);
        sfor<12 + NW1, 20>([&](auto NC) { w_dma<decltype(NC)::value>(cs_, wt, w_soff<decltype(NC)::value>(cs_)); });
        STAMP3(19);
        WAIT_VM(16);
        bar();
        if (tid < 8) __builtin_amdgcn_raw_buffer_store_b64(v2u{c.tagA, c.tagB}, c.rws, (u32)(tid * 2048 + wb * 8), OFF_FL, AUX_SC1);
        MEMBAR();
        STAMP(7);
        sfor<20, 24>([&](auto NC) { w_dma<decltype(NC)::value>(cs_, wt, w_soff<decltype(NC)::value>(cs_)); });
        WAIT_VM(12);
        sfor<12, 18>([&](auto NC) { w_read1<decltype(NC)::value>(cs_, wl, W1, W2); });
        WAIT_LGKM(0);
        sfor<24, 28>([&](auto NC) { w_dma<decltype(NC)::value>(cs_, wt, w_soff<decltype(NC)::value>(cs_)); });
        STAMP3(21);
    }
    Ad ad;
    ad.l16 = lane_id() * 16;
    ad.hst = OFF_H + (c.grp * 224 + 16 * c.j + 4 * w) * 1024;
    ad.x3o = OFF_X3 + (c.grp * 56 + 4 * c.j + w) * 1024;
    ad.sto = OFF_ST + c.grp * 2 * ST_HG;
    asm volatile("" : "+s"(ad.hst), "+s"(ad.x3o), "+s"(ad.sto));
    u32 wso[7];
#pragma unroll
    for (int m = 0; m < 7; ++m) {
        const int mm = member(c, m);
        wso[m] = OFF_WT2 + ((c.j * 4 + w) * 7 + mm) * 8192; ad.so[m] = ad.hst + (mm - c.j) * 16384;
        asm volatile("" : "+s"(wso[m]), "+s"(ad.so[m]));
    }
    const u32 ring = c.lbase + L_W + w * 24576;
    auto w2_ld = [&](auto RC) {
        constexpr int R = decltype(RC)::value, f = w2_step_of(28 + R), sub = k_sub(f);
        const u32 so = wso[k_mem(f)] + (sub >> 1) * 4096;
        if constexpr (R < 16) {
            W2[R][0] = ld128_asm<true, (sub & 1) * 2048>(c.rws, ad.l16, so);
            if constexpr (R < 15) W2[R][1] = ld128_asm<true, (sub & 1) * 2048 + 1024>(c.rws, ad.l16, so);
            else dma16<true>(c.lbase + L_JK + w * 1024, ad.l16, c.rws, so + (sub & 1) * 2048 + 1024);
        } else {
            dma_pair_sc1<(R - 16) * 2048>(ring, ad.l16, c.rws, so + (sub & 1) * 2048);
        }
    };
    {
        v4u cg = ld128_asm<true>(c.rws, tok32(ad), OFF_CSG + c.j * 2048 + w * 512);
        v4u f2 = ld128_asm<true>(c.rws, ad.l16 & 240u, OFF_FL2);
        WAIT_VM(10);
        w_read<18, 24>(c, W1, W2);
        const u32 fo = w == 0 ? OFF_FL + (wb & 7) * 2048 : OFF_CSG + c.j * 2048;
        v4u f0 = ld128_asm<true>(c.rws, ad.l16, fo), f1 = ld128_asm<true>(c.rws, 1024 + min(ad.l16, 752u), fo);
        asm volatile("s_waitcnt vmcnt(2)" : "+v"(cg), "+v"(f2) :: "memory");
        w_read<24, 28>(c, W1, W2);
        STAMP(8); STAMP3(22);
        asm volatile("s_waitcnt vmcnt(%2)" : "+v"(f0), "+v"(f1) : "i"(2 * NW2) : "memory");
        if (w == 0) {
            auto bad = [&]() { return ANY(xor_or(f0.x, c.tagA, xor_or(f0.z, c.tagA, xor_or(f1.x, c.tagA, xor_or(f1.z, c.tagA, tag_bad(f0, c.tagB, c.tagB, tag_bad(f1, c.tagB, c.tagB, 0u)))))) != 0u); };
            if (bad()) {
                unsigned spins = 0;
                NOUNROLL do {
                    MEMBAR();
                    f0 = ld128(c.rws, ad.l16, OFF_FL + (wb & 7) * 2048); f1 = ld128(c.rws, 1024 + min(ad.l16, 752u), OFF_FL + (wb & 7) * 2048);
                } while (bad() && !spin_fail(c, spins, 7u));
            }
            if (wb == 0 && ad.l16 == 0) __builtin_amdgcn_raw_buffer_store_b64(v2u{c.tagA, c.tagB}, c.rws, 0, OFF_EP, AUX_SC1);
        }
        bar();
        STAMP(9); STAMP3(23);
        {
            const u32 xb = c.lbase + L_X + w * 7168, xs_ = OFF_X1 + c.grp * 57344 + w * 7168;
            sfor<0, 14>([&](auto IC) {
                constexpr int i = decltype(IC)::value, o = (i / 7) * 28672 + (i % 7) * 1024;
                dma16<true>(xb + o, ad.l16, c.rws, xs_ + o);
            });
        }
        STAMP3(24);
        {
            auto bad = [&]() { return ANY(xor_or(f2.x, c.tagA, xor_or(f2.z, c.tagA, tag_bad(f2, c.tagB, c.tagB, 0u))) != 0u); };
            if (bad()) {
                unsigned spins = 0;
                NOUNROLL do { MEMBAR(); f2 = ld128(c.rws, ad.l16 & 240u, OFF_FL2); } while (bad() && !spin_fail(c, spins, 12u));
            }
            if (ANY(tag_bad(cg, c.tagA, c.tagB, 0u) != 0u)) {
                unsigned spins = 0;
                NOUNROLL do { MEMBAR(); cg = ld128(c.rws, tok32(ad), OFF_CSG + c.j * 2048 + w * 512); } while (ANY(tag_bad(cg, c.tagA, c.tagB, 0u) != 0u) && !spin_fail(c, spins, 8u));
            }
            *(LAS v2u*)(c.L + L_CS + w * 256 + (tok32(ad) >> 1)) = v2u{cg.x, cg.z};
        }
        sfor<0, NW3>(w2_ld);
        WAIT_VM(7 + 2 * NW3);
        bar();
        STAMP(10); STAMP3(25);
    }
    STAMP3(26);
    pin_a<0, 25>(W1);
    v4u hb[56], xres[2];
    G1Out ga, gb;
    v2u gate;
    f32x4 csq[3];
    St1 st;
    ad.ca = L_CS + (w * 64 + (ad.l16 >> 9) * 4) * 4;
    using M0 = std::integral_constant<int, 0>; using M1 = std::integral_constant<int, 1>;
    {
        st.ad = xtr_of(ad.l16 >> 4) + w * 7168;
        auto stats = [&](auto MC, auto HC, auto FC, G1Out& o) {
            constexpr int H = decltype(HC)::value, f = decltype(FC)::value;
            constexpr bool TR = decltype(MC)::value == 0;
            if constexpr (f == 0) { st.s1 = 0.f; st.s2 = 0.f; }
            if constexpr (f >= 2 && f < 23) { constexpr int ph = (f - 2) % 3, k = (f - 2) / 3; st1_acc<k, ph * 3, ph == 2 ? 8 : ph * 3 + 3>(st); }
            if constexpr (f % 3 == 0 && f < 21) st1_read<TR, H, f / 3>(c, st);
            if constexpr (f == 23) part_put(c, ad, L_SP + H * 1024, st.s1, st.s2);
            if constexpr (f == 24) { SB(); asm volatile("s_waitcnt lgkmcnt(%0)\n\ts_barrier" :: "i"(TR ? 2 : 1) : "memory"); }
            if constexpr (f == 25) { const f32x2 t = part_get(c, ad, L_SP + H * 1024); ln_scalars(t[0], t[1], o.mb, o.r); asm volatile("" : "+v"(o.mb), "+v"(o.r)); }
        };
        constexpr unsigned E2 = 0xAAAAAAAu;
        g1_main<true, 0, PS0, 0x9AAAAAAu>(c, ad, W1, ga.acc, [&](auto FC) {
            constexpr int f = decltype(FC)::value;
            if constexpr ((f & 1) == 0) w2_ld(std::integral_constant<int, NW3 + f / 2>{});
            if constexpr (f == 22) WAIT_VM(24);
            stats(M0{}, M0{}, FC, ga);
            if constexpr (f >= 26) csq[f - 26] = epi_cs(c, ad, f - 26);
        });
        STAMP(11);
        auto side_b = [&](auto MC, auto FC) {
            constexpr int f = decltype(FC)::value, M = decltype(MC)::value;
            if constexpr (f < 6) csq[(f + 2) % 3] = epi_cs(c, ad, f + 2);
            if constexpr (f < 8) g1_epi<0, M, f>(c, ad, ga, csq[f % 3], hb[f >> 1]);
            if constexpr (f < 8 && (f & 1)) h_store<0, f / 2>(c, ad, hb[f >> 1]);
            if constexpr (f == 18) gate = gate_ld<true>(c, ad, 0);
            if constexpr (f == 19) xres[0] = xres_ld<0, M>(c, ad);
            if constexpr (f == 23) {
                SB();
                asm volatile("s_waitcnt vmcnt(%1)" : "+v"(gate) : "i"(0) : "memory");
                gate_wait<M>(c, ad, 0, gate);
            }
            if constexpr (f >= 24) hb[f - 20] = h_next<f - 20>(c, ad);
            if constexpr (f >= 26) csq[f - 26] = epi_cs(c, ad, f - 26);
        };
        g1_main<true, 1, PS0 + 7, 0x9AAAAAAu>(c, ad, W1, gb.acc, [&](auto FC) {
            constexpr int f = decltype(FC)::value;
            if constexpr ((f & 1) == 0 && NW3 + 14 + f / 2 < 28) w2_ld(std::integral_constant<int, NW3 + 14 + f / 2>{});
            stats(M0{}, M1{}, FC, gb);
            side_b(M0{}, FC);
        });
        STAMP(12);
        pin_v<0, 16>(W2);
        pin_a<0, 25>(W1);
        STAMP2(PS0 + 14);
        STAMP(13);
        g2_pair<0>(c, ad, W2, hb, gb, csq, ga.r, xres, c.rws);
        pin_a<0, 25>(W1);
        STAMP(14);
        v4u t[7];
        st.ad = ad.l16 + w * 7168;
        STAMP4(6);
        bar();
        STAMP4(7);
        STAMP(17);
        g1_main<false, 0, PS1, 0x9AAAAAAu>(c, ad, W1, ga.acc, [&](auto FC) {
            constexpr int f = decltype(FC)::value;
            stats(M1{}, M0{}, FC, ga);
            if constexpr (f >= 12 && f < 19) t[f - 12] = x3_ld1<1, f - 12>(c, ad);
            if constexpr (f == 24) x3_wait<1>(c, ad, t);
            if constexpr (f >= 25) { x3_put1<1, 2 * (f - 25)>(c, ad, t[2 * (f - 25)]); x3_put1<1, 2 * (f - 25) + 1>(c, ad, t[2 * (f - 25) + 1]); }
            if constexpr (f == 27) x3_put1<1, 6>(c, ad, t[6]);
            if constexpr (f >= 26) csq[f - 26] = epi_cs(c, ad, f - 26);
        });
        STAMP4(8);
        STAMP4(9);
        STAMP4(10);
        STAMP(18);
        bar();
        STAMP4(11);
        g1_main<false, 1, PS1 + 7, 0x9AAAAAAu>(c, ad, W1, gb.acc, [&](auto FC) {
            stats(M1{}, M1{}, FC, gb);
            side_b(M1{}, FC);
        });
        (void)E2;
        STAMP(19);
        STAMP2(PS1 + 14);
        STAMP(20);
        const __attribute__((address_space(4))) char* ka = (const __attribute__((address_space(4))) char*)__builtin_amdgcn_kernarg_segment_ptr();
        asm volatile("" : "+s"(ka));
        g2_pair<1>(c, ad, W2, hb, gb, csq, ga.r, xres, mkrs(*(void* const __attribute__((address_space(4)))*)(ka + 72), TSZ));
        STAMP(21);
    }
    asm volatile(".Lfk_end:");
}

extern "C" void kernel_launch(void* const* d_in, const int* in_sizes, int n_in,
                              void* d_out, int out_size, void* d_ws, size_t ws_size,
                              hipStream_t stream) {
    (void)hipFuncSetAttribute((const void*)fused_kernel, hipFuncAttributeMaxDynamicSharedMemorySize, L_TOT);
    fused_kernel<<<256, 256, L_TOT, stream>>>((const u16*)d_in[0], (const u16*)d_in[1], (const u16*)d_in[2], (const u16*)d_in[3], (const u16*)d_in[4],
                                              (const u16*)d_in[5], (const u16*)d_in[7], (const u16*)d_in[8], (unsigned char*)d_ws, (u16*)d_out);
}
```

```cpp
#include <hip/hip_runtime.h>
#include <stdint.h>
#include <type_traits>

typedef unsigned short u16;
typedef unsigned u32;
typedef u32 v4u __attribute__((ext_vector_type(4)));
typedef u32 v2u __attribute__((ext_vector_type(2)));
typedef float f32x2 __attribute__((ext_vector_type(2)));
typedef float f32x4 __attribute__((ext_vector_type(4)));
typedef float f32x16 __attribute__((ext_vector_type(16)));
typedef __bf16 bf16x8 __attribute__((ext_vector_type(8)));
typedef __bf16 bf16x2 __attribute__((ext_vector_type(2)));
typedef short s16x4 __attribute__((ext_vector_type(4)));
typedef short s16x2 __attribute__((ext_vector_type(2)));
typedef unsigned short u16x2 __attribute__((ext_vector_type(2)));
#define LAS __attribute__((address_space(3)))
#define GAS __attribute__((address_space(1)))
#define DI __device__ __forceinline__
#define RLX_AGENT __ATOMIC_RELAXED, __HIP_MEMORY_SCOPE_AGENT
#define SB() __builtin_amdgcn_sched_barrier(0)
#define NOUNROLL _Pragma("unroll 1")
#define MEMBAR() asm volatile("" ::: "memory")
template <class T> __device__ __forceinline__ void LV(T& x) { asm volatile("" : "+v"(x)); }
template <class T> __device__ __forceinline__ void LA(T& x) { asm volatile("" : "+a"(x)); }
#define ANY(x) (__builtin_amdgcn_ballot_w64(x) != 0ull)

constexpr int S = 2048, D = 448, F = 1792;
constexpr float EPS = 1e-5f;
constexpr u32 TSZ = S * D * 2;
constexpr u32 OFF_EP  = 0;
constexpr u32 OFF_FL  = 4096;
constexpr u32 OFF_ST0 = OFF_FL + 16384;
constexpr u32 OFF_CSG = OFF_ST0 + 16384;
constexpr u32 OFF_ST  = OFF_CSG + 16384;
constexpr u32 OFF_KVG = 1048576;
constexpr u32 OFF_X0G = OFF_KVG + 2 * TSZ;
constexpr u32 OFF_X1  = OFF_X0G + 2 * TSZ;
constexpr u32 OFF_YT  = OFF_X1 + TSZ;
constexpr u32 OFF_X3  = OFF_YT + TSZ;
constexpr u32 OFF_H   = OFF_X3 + TSZ;
constexpr u32 OFF_WT2 = OFF_H + 64 * 112 * 1024;
constexpr u32 OFF_FL2 = 2048;
constexpr u32 WS_BYTES = OFF_WT2 + D * F * 2;
static_assert(OFF_ST + 64 * 24 * 512 <= OFF_KVG, "ws map");
constexpr int RSRC_W3 = 0x00020000;
constexpr int AUX_SC1 = 16;
constexpr unsigned SPIN_CAP = 1u << 14;
constexpr u32 SGN = 0x80008000u;

template <int I, int N, class Fn> DI void sfor(Fn&& fn) { if constexpr (I < N) { fn(std::integral_constant<int, I>{}); sfor<I + 1, N>(fn); } }

DI float bflo(u32 x) { return __uint_as_float(x << 16); }
DI float bfhi(u32 x) { return __uint_as_float(x & 0xffff0000u); }
DI u32 pk(float a, float b) { f32x2 v = {a, b}; return __builtin_bit_cast(u32, __builtin_convertvector(v, bf16x2)); }
DI float rbf(float a) { return bflo(pk(a, a)); }
DI __amdgpu_buffer_rsrc_t mkrs(const void* p, u32 bytes) { return __builtin_amdgcn_make_buffer_rsrc((void*)p, 0, (int)bytes, RSRC_W3); }
DI f32x4 mfma16(v4u a, v4u b, f32x4 c) { return __builtin_amdgcn_mfma_f32_16x16x32_bf16(__builtin_bit_cast(bf16x8, a), __builtin_bit_cast(bf16x8, b), c, 0, 0, 0); }
DI f32x16 mfma32(v4u a, v4u b, f32x16 c) { return __builtin_amdgcn_mfma_f32_32x32x16_bf16(__builtin_bit_cast(bf16x8, a), __builtin_bit_cast(bf16x8, b), c, 0, 0, 0); }
DI f32x16 zero16() { f32x16 z; for (int i = 0; i < 16; ++i) z[i] = 0.f; return z; }
DI v2u trd(LAS char* p) { return __builtin_bit_cast(v2u, __builtin_amdgcn_ds_read_tr16_b64_v4i16((LAS s16x4*)p)); }
DI v4u trd2(LAS char* p, int second) { v2u a = trd(p), b = trd(p + second); return v4u{a.x, a.y, b.x, b.y}; }
DI float dot2(u32 a, u32 b, float c) { return __builtin_amdgcn_fdot2_f32_bf16(__builtin_bit_cast(bf16x2, a), __builtin_bit_cast(bf16x2, b), c, false); }
DI v4u ld128(__amdgpu_buffer_rsrc_t rs, u32 vo, u32 so) { return __builtin_amdgcn_raw_buffer_load_b128(rs, vo, so, AUX_SC1); }
template <bool SC1, int OFF = 0> DI v4u ld128_asm(__amdgpu_buffer_rsrc_t rs, u32 vo, u32 so) {
    v4u r;
    if constexpr (SC1) asm volatile("buffer_load_dwordx4 %0, %1, %2, %3 offen offset:%4 sc1" : "=v"(r) : "v"(vo), "s"(rs), "s"(so), "i"(OFF) : "memory");
    else asm volatile("buffer_load_dwordx4 %0, %1, %2, %3 offen offset:%4" : "=v"(r) : "v"(vo), "s"(rs), "s"(so), "i"(OFF) : "memory");
    return r;
}
template <int OFF = 0> DI void st128(v4u v, __amdgpu_buffer_rsrc_t rs, u32 vo, u32 so) {
    asm volatile("buffer_store_dwordx4 %0, %1, %2, %3 offen offset:%4 sc1\n\ts_nop 1" :: "v"(v), "v"(vo), "s"(rs), "s"(so), "i"(OFF) : "memory");
}
DI int lane_id() { int l; asm volatile("v_mbcnt_lo_u32_b32 %0, -1, 0\n\tv_mbcnt_hi_u32_b32 %0, -1, %0" : "=v"(l)); return l; }
DI float hsum(float a) {
    const u32 v = __float_as_uint(a);
    auto t = __builtin_amdgcn_permlane32_swap(v, v, false, false);
    return __uint_as_float(t[0]) + __uint_as_float(t[1]);
}
DI float ln_sd(float var) { return rbf(__builtin_amdgcn_sqrtf(rbf(rbf(var) + EPS))); }
DI void ln_scalars(float s1, float s2, float& m, float& r) {
    m = s1 * (1.0f / D);
    r = __builtin_amdgcn_rsqf(fmaxf(__builtin_fmaf(-m, m, s2 * (1.0f / D)), 0.f) + EPS);
}
DI u32 xor_or(u32 a, u32 b, u32 c) { return __builtin_amdgcn_bitop3_b32(a, b, c, 0xBE); }
DI u32 tag_bad(const v4u& g, u32 tA, u32 tB, u32 acc) { return xor_or(g.w, tB, xor_or(g.y, tA, acc)); }
template <bool SC1> DI void dma16(u32 ldsaddr, u32 voff, __amdgpu_buffer_rsrc_t rs, u32 soff) {
    if constexpr (SC1) asm volatile("s_mov_b32 m0, %0\n\ts_nop 0\n\tbuffer_load_dwordx4 %1, %2, %3 offen sc1 lds" :: "s"(ldsaddr), "v"(voff), "s"(rs), "s"(soff) : "memory", "m0");
    else asm volatile("s_mov_b32 m0, %0\n\ts_nop 0\n\tbuffer_load_dwordx4 %1, %2, %3 offen lds" :: "s"(ldsaddr), "v"(voff), "s"(rs), "s"(soff) : "memory", "m0");
}
template <int LI> DI void dma_pair(u32 ldsbase, u32 vo0, u32 vo1, __amdgpu_buffer_rsrc_t rs, u32 soff) {
    asm volatile("s_add_u32 m0, %0, %5\n\ts_nop 0\n\tbuffer_load_dwordx4 %1, %3, %4 offen lds\n\tbuffer_load_dwordx4 %2, %3, %4 offen offset:1024 lds"
                 :: "s"(ldsbase), "v"(vo0), "v"(vo1), "s"(rs), "s"(soff), "i"(LI) : "memory", "m0", "scc");
}
template <int LI> DI void dma_pair_sc1(u32 ldsbase, u32 vo, __amdgpu_buffer_rsrc_t rs, u32 soff) {
    asm volatile("s_add_u32 m0, %0, %4\n\ts_nop 0\n\tbuffer_load_dwordx4 %1, %2, %3 offen sc1 lds\n\tbuffer_load_dwordx4 %1, %2, %3 offen offset:1024 sc1 lds"
                 :: "s"(ldsbase), "v"(vo), "s"(rs), "s"(soff), "i"(LI) : "memory", "m0", "scc");
}
DI void touch(u32 junk, u32 voff, __amdgpu_buffer_rsrc_t rs, u32 soff) {
    asm volatile("s_mov_b32 m0, %0\n\ts_nop 0\n\tbuffer_load_dword %1, %2, %3 offen lds" :: "s"(junk), "v"(voff), "s"(rs), "s"(soff) : "memory", "m0");
}
#define WAIT_VM(n) asm volatile("s_waitcnt vmcnt(%0)" :: "i"(n) : "memory")
#define WAIT_LGKM(n) asm volatile("s_waitcnt lgkmcnt(%0)" :: "i"(n) : "memory")
DI void bar() { asm volatile("s_waitcnt lgkmcnt(0)\n\ts_barrier" ::: "memory"); }
DI int wrap7(int x) { return x >= 7 ? x - 7 : x; }
template <int N, int K> DI void ilv() { if constexpr (N > 0) { __builtin_amdgcn_sched_group_barrier(0x8, 1, 0); __builtin_amdgcn_sched_group_barrier(0x366, K, 0); ilv<N - 1, K>(); } }

constexpr int L_X = 0;
constexpr int L_W = 57344;
constexpr int L_M = L_W + 98304;
constexpr int L_SP = L_M;
constexpr int L_CS = L_M + 2048;
constexpr int L_S2 = L_M + 3072;
constexpr int L_JK = L_M + 4096;
constexpr int L_TOT = L_M + 8192;
constexpr int T_K = 0, T_V = 9216, T_X = 17408, T_S = L_JK;
constexpr int CV_B = 25600;
constexpr int CV_HE = 0, CV_HO = 4160, CV_KV = CV_HO + 4160, CV_CH = CV_KV + 4608;
constexpr int CV_ST = 16384;
static_assert(CV_B + 2 * CV_CH <= L_W, "lds map");

struct Ctx {
    LAS char* L;
    __amdgpu_buffer_rsrc_t rws, rw1, rw2;
    GAS u32* tmo;
    int w, j, grp, kb0, gi;
    u32 lbase, tagA, tagB;
};
DI Ctx fresh(const Ctx& c0) { Ctx c = c0; asm volatile("" : "+s"(c.w), "+s"(c.j), "+s"(c.grp), "+s"(c.kb0), "+s"(c.gi)); return c; }
DI bool spin_fail(const Ctx& c, unsigned& spins, u32 code) {
    if (++spins > SPIN_CAP) { __hip_atomic_store(c.tmo, code, RLX_AGENT); return true; }
    __builtin_amdgcn_s_sleep(1);
    return false;
}
DI u32 xtr_of(int lane) { return (u32)(((lane >> 5) * 4 + ((lane >> 2) & 3)) * 64 + ((lane >> 4) & 1) * 32 + (lane & 3) * 8); }
DI u32 wl1_of(const Ctx& c, int lane) {
    const int rr = (lane >> 5) * 4 + ((lane >> 2) & 3);
    return (u32)(L_W + c.w * 24576 + rr * 128 + ((rr >> 1) & 1) * 64 + ((lane >> 4) & 1) * 32 + (lane & 3) * 8);
}
DI u32 wl2_of(const Ctx& c, int lane) { return wl1_of(c, lane) ^ (u32)((c.w >> 1) * 64 + (c.w & 1) * 32); }
DI int member(const Ctx& c, int m) {
    if (m == 0) return c.j;
    int t = m - 1 + 2 * c.gi; t = t >= 6 ? t - 6 : t; t = t >= 6 ? t - 6 : t;
    return wrap7(c.j + 1 + t);
}
typedef v4u W1T[28][2];
typedef v4u W2T[16][2];
typedef f32x16 AccT[2];

DI constexpr int k_mem(int f) { return f < 4 ? 0 : 1 + (f - 4) % 6; }
DI constexpr int k_sub(int f) { return f < 4 ? f : (f - 4) / 6; }
DI constexpr int w2_step_of(int n) { return n < 40 ? 2 * (n - 28) : (n < 44 ? n - 16 : 2 * (n - 44) + 1); }
template <int N> DI u32 w_soff(const Ctx& c) {
    if constexpr (N < 28) return (N % 7 + 7 * ((N / 7 + c.kb0) & 3)) * 16 * (F * 2) + (256 * c.j + 64 * c.w) * 2;
    else return (256 * member(c, k_mem(w2_step_of(N))) + 64 * c.w + 16 * k_sub(w2_step_of(N))) * (D * 2) + 128 * c.j;
}
template <int N> DI u32 w_voff(int lane) { return (lane >> 3) * ((N < 28 ? F : D) * 2) + ((lane & 7) ^ (((lane >> 4) & 1) << 2)) * 16; }
struct WSt { u32 ring, vo0, vo1; };
template <int N> DI WSt w_st(const Ctx& c, int lane) { WSt t; t.ring = c.lbase + L_W + c.w * 24576; t.vo0 = w_voff<N>(lane); t.vo1 = t.vo0 + 8 * ((N < 28 ? F : D) * 2) - 1024; return t; }
template <int N> DI void w_dma(const Ctx& c, const WSt& t, u32 so) { dma_pair<(N % 12) * 2048>(t.ring, t.vo0, t.vo1, N < 28 ? c.rw1 : c.rw2, so); }
DI v4u w_frag(const Ctx& c, u32 wl, int slot, int t) { return trd2(c.L + (wl ^ (u32)(t * 64)) + slot * 2048, 1024); }
template <int N> DI void w_read1(const Ctx& c, u32 wl, W1T& W1, W2T& W2) {
#pragma unroll
    for (int t = 0; t < 2; ++t) {
        if constexpr (N < 28) W1[N][t] = w_frag(c, wl, N % 12, t);
        else W2[N - 28][t] = w_frag(c, wl, N % 12, t);
    }
}
template <int N0, int N1> DI void w_read(const Ctx& c0, W1T& W1, W2T& W2) {
    const Ctx c = fresh(c0);
    const u32 wl = wl1_of(c, lane_id());
    sfor<N0, N1>([&](auto NC) { w_read1<decltype(NC)::value>(c, wl, W1, W2); });
}

template <int N0, int N1> DI void pin_a(W1T& W1) {
    if constexpr (N0 < N1) { asm volatile("" : "+a"(W1[N0][0]), "+a"(W1[N0][1])); pin_a<N0 + 1, N1>(W1); }
}
template <int N0, int N1> DI void pin_v(W2T& W2) { if constexpr (N0 < N1) { LV(W2[N0][0]); if constexpr (N0 < 15) LV(W2[N0][1]); pin_v<N0 + 1, N1>(W2); } }
struct G1Out { AccT acc; float mb, r; };
DI constexpr bool reg_start(unsigned long long end, int f) { return f == 0 || ((end >> (f - 1)) & 1ull); }
DI constexpr int reg_len(unsigned long long end, int f) { int n = 1; while (!reg_start(end, f - n + 1)) ++n; return n; }
struct Ad {
    u32 l16;
    u32 ca;
    u32 hst;
    u32 x3o;
    u32 sto;
    u32 so[7];
};
constexpr u32 ST_HG = 3 * 4096;
DI u32 tok32(const Ad& a) { return a.l16 & 496u; }

template <bool TR, int HALF, unsigned END, class Side>
DI void g1_main(const Ctx& c, Ad& a, const W1T& W1, AccT& acc, Side&& side) {
    const u32 lp = TR ? xtr_of(a.l16 >> 4) : a.l16;
    int kb = c.kb0; asm volatile("" : "+s"(kb));
    u32 la = 0;
    auto ldb = [&](int f) {
        if (f % 7 == 0) la = lp + ((f / 7 + kb) & 3) * 7168;
        LAS char* p = c.L + la + (L_X + HALF * 28672 + (f % 7) * 1024);
        if constexpr (TR) return trd2(p, 512);
        else return *(LAS v4u*)p;
    };
    v4u B[28];
    B[0] = ldb(0); B[1] = ldb(1); B[2] = ldb(2);
    SB();
    sfor<0, 28>([&](auto FC) {
        constexpr int f = decltype(FC)::value;
        if constexpr (reg_start(END, f)) asm volatile("" : "+v"(a.l16), "+v"(a.ca));
        acc[0] = mfma32(W1[f][0], B[f], f == 0 ? zero16() : acc[0]);
        acc[1] = mfma32(W1[f][1], B[f], f == 0 ? zero16() : acc[1]);
        if constexpr (f + 3 < 28) B[f + 3] = ldb(f + 3);
        side(FC);
        if constexpr ((END >> f) & 1u) { ilv<2 * reg_len(END, f), 5>(); SB(); }
    });
}
struct St1 { u32 ad; v4u fr[2]; float s1, s2; };
template <bool TR, int HALF, int K> DI void st1_read(const Ctx& c, St1& s) {
    if constexpr (TR) s.fr[K & 1] = trd2(c.L + s.ad + (L_X + HALF * 28672 + K * 1024), 512);
    else s.fr[K & 1] = *(LAS v4u*)(c.L + s.ad + (L_X + HALF * 28672 + K * 1024));
}
template <int K, int E0, int E1> DI void st1_acc(St1& s) {
#pragma unroll
    for (int e = E0; e < E1; ++e) { if (e == E0) LV(s.fr[K & 1]); const u32 x = s.fr[K & 1][e >> 1]; if (e & 1) s.s2 = dot2(x, x, s.s2); else s.s1 = dot2(x, 0x3f803f80u, s.s1); }
}
DI void part_put(const Ctx& c, const Ad& a, int off, float s1, float s2) { *(LAS f32x2*)(c.L + off + tok32(a) * 2 + c.w * 8) = f32x2{hsum(s1), hsum(s2)}; }
DI f32x2 part_get(const Ctx& c, const Ad& a, int off) {
    const f32x4 u = *(LAS f32x4*)(c.L + off + tok32(a) * 2), v = *(LAS f32x4*)(c.L + off + tok32(a) * 2 + 16);
    return f32x2{(u[0] + u[2]) + (v[0] + v[2]), (u[1] + u[3]) + (v[1] + v[3])};
}
DI void frag_sums(const v4u& p, float& s1, float& s2) {
    s1 = 0.f; s2 = 0.f;
#pragma unroll
    for (int e = 0; e < 4; ++e) { s1 = dot2(p[e], 0x3f803f80u, s1); s2 = dot2(p[e], p[e], s2); }
}
template <int HALF, int KIND> DI void stat_pub(const Ctx& c, const Ad& a, int off, int who) {
    if (c.w == who) {
        f32x2 t = part_get(c, a, off);
        const bool z = c.j == 6 && a.l16 >= 512u;
        st128(v4u{z ? 0u : __float_as_uint(t[0]), c.tagA, z ? 0u : __float_as_uint(t[1]), c.tagB}, c.rws, z ? a.l16 : tok32(a), a.sto + HALF * ST_HG + KIND * 4096 + c.j * 512);
    }
}
template <bool DO> DI void stat_pub_if(const Ctx& c, const Ad& a) { if constexpr (DO) stat_pub<0, 0>(c, a, L_S2, 0); }
struct Gath { v4u g[4]; };
template <int HALF, int KIND, int I> DI void gath_ld(const Ctx& c, const Ad& a, Gath& g) {
    g.g[I] = ld128(c.rws, a.l16 + I * 1024, a.sto + HALF * ST_HG + KIND * 4096);
}
DI bool gath_bad(const Ctx& c, const Gath& g) {
    u32 d = 0;
#pragma unroll
    for (int i = 0; i < 4; ++i) d = tag_bad(g.g[i], c.tagA, c.tagB, d);
    return ANY(d != 0u);
}
template <int HALF, int KIND> DI void gath_wait(const Ctx& c, const Ad& a, Gath& g) {
    if (gath_bad(c, g)) {
        unsigned spins = 0;
        NOUNROLL do { MEMBAR(); sfor<0, 4>([&](auto IC) { gath_ld<HALF, KIND, decltype(IC)::value>(c, a, g); }); } while (gath_bad(c, g) && !spin_fail(c, spins, 2u));
    }
}
DI void gath_ln(const Gath& g, float& m, float& r) {
    const float t1 = (__uint_as_float(g.g[0].x) + __uint_as_float(g.g[1].x)) + (__uint_as_float(g.g[2].x) + __uint_as_float(g.g[3].x));
    const float t2 = (__uint_as_float(g.g[0].z) + __uint_as_float(g.g[1].z)) + (__uint_as_float(g.g[2].z) + __uint_as_float(g.g[3].z));
    ln_scalars(hsum(t1), hsum(t2), m, r);
}
DI f32x4 epi_cs(const Ctx& c, const Ad& a, int E) { return *(LAS f32x4*)(c.L + a.ca + E * 32); }
template <int HALF, int MLP, int E>
DI void g1_epi(const Ctx& c, const Ad& a, G1Out& o, f32x4& cs, v4u& fr) {
    constexpr int t = E >> 2, r0 = 4 * (E & 3), g = E & 1;
    constexpr bool neg = (g ^ MLP) != 0;
    float h[4];
    LV(cs); LA(o.acc[t]);
#pragma unroll
    for (int e = 0; e < 4; ++e) h[e] = __builtin_fmaf(-o.mb, cs[e], o.acc[t][r0 + e]);
#pragma unroll
    for (int e = 0; e < 2; ++e) {
        const u32 p = __builtin_bit_cast(u32, __builtin_elementwise_max(__builtin_bit_cast(s16x2, pk(h[2 * e], h[2 * e + 1])), s16x2{0, 0}));
        fr[2 * g + e] = neg ? (p | SGN) : p;
    }
    if (g == 0) LV(fr);
}
template <int HALF, int K> DI void h_store(const Ctx& c, const Ad& a, const v4u& fr) { st128<K * 1024>(fr, c.rws, a.l16, a.hst + HALF * 114688); }

constexpr int NW1 = 2;
constexpr int NW3 = 4;
constexpr int SLP1 = 16;
constexpr int HR = 10, GL = 14, GC = 20;
template <int MLP> DI bool h_bad(const v4u& a, const v4u& b) {
    const u32 lo = MLP == 0 ? (a.x | a.y | b.x | b.y) : ~(a.x & a.y & b.x & b.y);
    const u32 hi = MLP == 0 ? ~(a.z & a.w & b.z & b.w) : (a.z | a.w | b.z | b.w);
    return ((lo | hi) & SGN) != 0u;
}
DI u32 h_base(const Ctx& c, int half) { return OFF_H + ((c.grp * 2 + half) * 112 + 4 * c.w) * 1024; }
template <bool ASM> DI v2u gate_ld(const Ctx& c, const Ad& a, int half) {
    const u32 vo = min(a.l16, 96u) * 1024, so = a.hst - c.j * 16384 + half * 114688 + 4;
    if constexpr (ASM) { v2u r; asm volatile("buffer_load_dwordx2 %0, %1, %2, %3 offen sc1" : "=v"(r) : "v"(vo), "s"(c.rws), "s"(so) : "memory"); return r; }
    else return __builtin_amdgcn_raw_buffer_load_b64(c.rws, vo, so, AUX_SC1);
}
template <int MLP> DI bool gate_bad(v2u x) { return ((MLP == 0 ? (x.x | ~x.y) : (~x.x | x.y)) & SGN) != 0u; }
template <int MLP> DI void gate_wait(const Ctx& c, const Ad& a, int half, v2u x) {
    if (ANY(gate_bad<MLP>(x))) {
        unsigned spins = 0;
        NOUNROLL do { MEMBAR(); x = gate_ld<false>(c, a, half); } while (ANY(gate_bad<MLP>(x)) && !spin_fail(c, spins, 1u));
    }
}
template <int N> DI v4u h_next(const Ctx& c, Ad& a) {
    constexpr int f = N >= 28 ? N - 28 : N;
    if constexpr (N >= 32 && N < 38) a.so[k_mem(f)] += 114688u;
    v4u r = __builtin_amdgcn_raw_buffer_load_b128(c.rws, a.l16 + k_sub(f) * 1024, a.so[k_mem(f)], AUX_SC1);
    if constexpr (N >= 50) a.so[k_mem(f)] -= 114688u;
    return r;
}
template <int MLP, int F_> DI void h_check(const Ctx& c0, v4u& a, v4u& b) {
    if (__builtin_expect(ANY(h_bad<MLP>(a, b)), 0)) {
        const Ctx c = fresh(c0);
        constexpr int half = F_ >= 28, f = F_ - 28 * half;
        const u32 vo = lane_id() * 16 + k_sub(f) * 1024, s0 = h_base(c, half) + member(c, k_mem(f)) * 16384, s1 = h_base(c, half) + member(c, k_mem(f + 1)) * 16384;
        unsigned spins = 0;
        NOUNROLL do {
            MEMBAR();
            a = ld128(c.rws, vo, s0); b = ld128(c.rws, vo, s1);
        } while (ANY(h_bad<MLP>(a, b)) && !spin_fail(c, spins, 3u));
    }
}
template <int HALF, int P> DI void red_write(const Ctx& c, const Ad& a, const AccT& acc) {
    LAS char* dst = c.L + (a.l16 + (P ^ c.w) * 6144) + (L_X + (1 - HALF) * 28672 + (P - 1) * 2048);
    const f32x16& x = acc[P >> 1];
    constexpr int r0 = (P & 1) * 8;
    *(LAS f32x4*)dst = f32x4{x[r0], x[r0 + 1], x[r0 + 2], x[r0 + 3]};
    *(LAS f32x4*)(dst + 1024) = f32x4{x[r0 + 4], x[r0 + 5], x[r0 + 6], x[r0 + 7]};
}
template <int HALF> DI void red_read(const Ctx& c, const Ad& a, f32x4 (&t)[6]) {
    LAS char* src = c.L + (a.l16 + c.w * 6144) + (L_X + (1 - HALF) * 28672);
#pragma unroll
    for (int i = 0; i < 6; ++i) t[i] = *(LAS f32x4*)(src + i * 1024);
}
template <int K> DI void post_half(const f32x4& o, float r, v4u& xres) {
#pragma unroll
    for (int e = 0; e < 2; ++e) xres[2 * K + e] = pk(__builtin_fmaf(o[2 * e], r, bflo(xres[2 * K + e])), __builtin_fmaf(o[2 * e + 1], r, bfhi(xres[2 * K + e])));
}
template <int HALF> DI void out_store(const Ctx& c, const Ad& a, const v4u& x, __amdgpu_buffer_rsrc_t rout) {
    auto r0 = __builtin_amdgcn_permlane32_swap(x.x, x.z, false, false);
    auto r1 = __builtin_amdgcn_permlane32_swap(x.y, x.w, false, false);
    const v4u v = {r0[0], r1[0], r0[1], r1[1]};
    const u32 vo = tok32(a) * (D / 8) + (a.l16 >> 9) * 16, so = (u32)((c.grp * 64 + HALF * 32) * D + 64 * c.j + 16 * c.w) * 2;
    asm volatile("buffer_store_dwordx4 %0, %1, %2, %3 offen\n\ts_nop 1" :: "v"(v), "v"(vo), "s"(rout), "s"(so) : "memory");
}
template <int HALF, int MLP> DI v4u xres_ld(const Ctx& c, const Ad& a) {
    if constexpr (MLP == 0) return trd2(c.L + (xtr_of(a.l16 >> 4) + (4 * c.j + c.w) * 1024) + (L_X + HALF * 28672), 512);
    else return *(LAS v4u*)(c.L + (a.l16 + (4 * c.j + c.w) * 1024) + (L_X + HALF * 28672));
}
template <int HALF> DI v4u y_ld(const Ctx& c, const Ad& a) { return trd2(c.L + (xtr_of(a.l16 >> 4) + c.w * 1024) + (L_X + HALF * 28672 + 24576), 512); }
DI void x3_make(v4u& x, const v4u& yf, float m, float r) {
    const float n = -m * r;
#pragma unroll
    for (int e = 0; e < 4; ++e) {
        const float p = bflo(x[e]), q = bfhi(x[e]);
        x[e] = pk(__builtin_fmaf(__builtin_fmaf(p, r, n), bflo(yf[e]), p), __builtin_fmaf(__builtin_fmaf(q, r, n), bfhi(yf[e]), q));
    }
}
template <int HALF> DI void x3_store(const Ctx& c, const Ad& a, const v4u& x) { st128(x, c.rws, a.l16, a.x3o + HALF * 28672); }
template <int HALF, int I> DI v4u x3_ld1(const Ctx& c, const Ad& a) {
    return ld128(c.rws, a.l16 + (I & 3) * 1024, a.x3o + (6 * c.w - 4 * c.j + (I & 4)) * 1024 + HALF * 28672);
}
DI u32 max3u(u32 a, u32 b, u32 c) { u32 r; asm("v_max3_u32 %0, %1, %2, %3" : "=v"(r) : "v"(a), "v"(b), "v"(c)); return r; }
DI bool x3_bad(const v4u (&t)[7]) {
    u32 mx = 0;
#pragma unroll
    for (int i = 0; i < 7; ++i) mx = max3u(max3u(mx, t[i].x, t[i].y), t[i].z, t[i].w);
    return ANY(mx == ~0u);
}
template <int HALF> DI void x3_wait(const Ctx& c, const Ad& a, v4u (&t)[7]) {
    if (x3_bad(t)) {
        unsigned spins = 0;
        NOUNROLL do { MEMBAR(); sfor<0, 7>([&](auto IC) { t[decltype(IC)::value] = x3_ld1<HALF, decltype(IC)::value>(c, a); }); } while (x3_bad(t) && !spin_fail(c, spins, 4u));
    }
}
template <int HALF, int I> DI void x3_put1(const Ctx& c, const Ad& a, const v4u& t) { *(LAS v4u*)(c.L + (a.l16 + c.w * 7168) + (L_X + HALF * 28672 + I * 1024)) = t; }

template <int MLP>
DI void g2_pair(const Ctx& c, Ad& a, const W2T& W2, v4u (&hb)[56], G1Out& gb, f32x4 (&csq)[3], float r0, v4u (&xres)[2], __amdgpu_buffer_rsrc_t out) {
    const u32 wl0 = a.l16 + L_W + c.w * 24576;
    AccT acc[2];
    v2u gate = {0, 0};
    v4u wl[2], fr, yf;
    f32x4 o[2], u[2];
    float s1, s2;
    Gath gs;
    const float ra_ = MLP ? -r0 : r0, rb_ = MLP ? -gb.r : gb.r;
    auto wres = [&](int slot, int k) { return *(LAS v4u*)(c.L + wl0 + (slot * 2048 + k * 1024)); };
    auto fetch = [&](auto NC) {
        constexpr int N = decltype(NC)::value;
        if constexpr (N < 56) {
            if constexpr (N >= 28 && N < 32) hb[N] = *(LAS v4u*)(c.L + (a.l16 + c.w * 4096) + (L_X + (N - 28) * 1024));
            else hb[N] = h_next<N>(c, a);
        }
    };
    auto rd = [&](int i) { return *(LAS f32x4*)(c.L + (a.l16 + c.w * 6144) + (L_X + 28672 + i * 1024)); };
    auto own = [&](int k) { return f32x4{acc[0][0][4 * k], acc[0][0][4 * k + 1], acc[0][0][4 * k + 2], acc[0][0][4 * k + 3]}; };
    wl[0] = wres(0, 0); wl[1] = wres(0, 1);
    SB();
    sfor<0, 28>([&](auto PC) {
        constexpr int F_ = 2 * decltype(PC)::value, H = F_ >= 28, f = F_ - 28 * H;
        constexpr bool res = f < 24;
        constexpr int r0_ = f < 24 ? f >> 1 : f - 12;
        if constexpr (F_ == 24 || F_ == 30 || (MLP == 0 && F_ == 36)) { bar(); SB(); stat_pub_if<MLP == 0 && F_ == 36>(c, a); }
        asm volatile("" : "+v"(a.l16), "+v"(a.ca));
        acc[H][0] = mfma32(W2[r0_][0], hb[F_], f == 0 ? zero16() : acc[H][0]);
        acc[H][1] = mfma32(W2[r0_][1], hb[F_], f == 0 ? zero16() : acc[H][1]);
        fetch(std::integral_constant<int, F_ + (F_ < 8 ? HR - 2 : HR)>{});
        fetch(std::integral_constant<int, F_ + 1 + (F_ < 8 ? HR - 2 : HR)>{});
        if constexpr (F_ == 6) { fetch(std::integral_constant<int, F_ + HR>{}); fetch(std::integral_constant<int, F_ + HR + 1>{}); }
        if constexpr (F_ < 6) csq[(F_ + 2) % 3] = epi_cs(c, a, F_ + 2);
        if constexpr (F_ < 8) g1_epi<1, MLP, F_>(c, a, gb, csq[F_ % 3], fr);
        if constexpr (F_ < 6) csq[F_ % 3] = epi_cs(c, a, F_ + 3);
        if constexpr (F_ < 8) g1_epi<1, MLP, F_ + 1>(c, a, gb, csq[(F_ + 1) % 3], fr);
        if constexpr (F_ == 32) { LA(acc[0][0]); LV(o[0]); LV(u[0]); LV(u[1]); o[0] = (o[0] + own(0)) + (u[0] + u[1]); post_half<0>(o[0], ra_, xres[0]); LV(xres[0]); }
        if constexpr (F_ == 34) { LA(acc[0][0]); LV(o[0]); LV(u[0]); LV(u[1]); o[0] = (o[0] + own(1)) + (u[0] + u[1]); post_half<1>(o[0], ra_, xres[0]); LV(xres[0]); }
        acc[H][0] = mfma32(res ? wl[0] : W2[r0_ + 1][0], hb[F_ + 1], acc[H][0]);
        acc[H][1] = mfma32((res || f == 26) ? wl[1] : W2[r0_ + 1][1], hb[F_ + 1], acc[H][1]);
        if constexpr (f < 22) { wl[0] = wres(1 + (f >> 1), 0); wl[1] = wres(1 + (f >> 1), 1); }
        if constexpr (f == 24) wl[1] = *(LAS v4u*)(c.L + (a.l16 + c.w * 1024) + L_JK);
        if constexpr (F_ == 26) { wl[0] = wres(0, 0); wl[1] = wres(0, 1); }
        if constexpr (F_ == 22) xres[1] = xres_ld<1, MLP>(c, a);
        if constexpr (F_ == 30) { u[0] = rd(0); u[1] = rd(2); o[0] = rd(4); }
        if constexpr (F_ == 32) { u[0] = rd(1); u[1] = rd(3); o[0] = rd(5); }
        if constexpr (MLP == 0 && F_ == 54) yf = y_ld<0>(c, a);
        if constexpr (F_ == GL) gate = gate_ld<false>(c, a, 1);
        if constexpr (F_ < 8) { *(LAS v4u*)(c.L + (a.l16 + c.w * 4096) + (L_X + (F_ / 2) * 1024)) = fr; h_store<1, F_ / 2>(c, a, fr); }
        if constexpr (MLP == 0 && (F_ == 24 || F_ == 26))
            dma16<true>(c.lbase + L_X + (F_ - 24) / 2 * 28672 + 24576 + c.w * 1024, a.l16, c.rws, OFF_YT + (c.grp * 2 + (F_ - 24) / 2) * 28672 + c.j * 4096 + c.w * 1024);
        if constexpr (F_ == 28) { red_write<0, 1>(c, a, acc[0]); red_write<0, 2>(c, a, acc[0]); red_write<0, 3>(c, a, acc[0]); }
        if constexpr (MLP == 0 && F_ == 34) { frag_sums(xres[0], s1, s2); part_put(c, a, L_S2, s1, s2); }
        if constexpr (MLP == 1 && F_ == 36) out_store<0>(c, a, xres[0], out);
        ilv<4, 5>();
        if constexpr (F_ + 2 < 56 && f + 2 != 28 && f + 2 >= 4) h_check<MLP, F_ + 2>(c, hb[F_ + 2], hb[F_ + 3]);
        if constexpr (F_ == GC) gate_wait<MLP>(c, a, 1, gate);
        SB();
    });
    static_assert(GC + 2 + HR <= 32 && (GC & 1) == 0 && (HR & 1) == 0, "half B's first foreign fragment is requested behind the gate");

    red_write<1, 1>(c, a, acc[1]); red_write<1, 2>(c, a, acc[1]); red_write<1, 3>(c, a, acc[1]);
    if constexpr (MLP == 0) sfor<0, 4>([&](auto IC) { gath_ld<0, 0, decltype(IC)::value>(c, a, gs); });
    bar();
    f32x4 t[6];
    red_read<1>(c, a, t);
    o[0] = (f32x4{acc[1][0][0], acc[1][0][1], acc[1][0][2], acc[1][0][3]} + t[0]) + (t[2] + t[4]);
    o[1] = (f32x4{acc[1][0][4], acc[1][0][5], acc[1][0][6], acc[1][0][7]} + t[1]) + (t[3] + t[5]);
    post_half<0>(o[0], rb_, xres[1]); post_half<1>(o[1], rb_, xres[1]);
    if constexpr (MLP == 0) {
        frag_sums(xres[1], s1, s2); part_put(c, a, L_S2, s1, s2);

        bar();
        stat_pub<1, 0>(c, a, L_S2, 1);

        gath_wait<0, 0>(c, a, gs);
        gath_ln(gs, s1, s2);
        x3_make(xres[0], yf, s1, s2);
        x3_store<0>(c, a, xres[0]);

        yf = y_ld<1>(c, a);
        __builtin_amdgcn_s_sleep(SLP1);
        v4u x[7];
        unsigned spins = 0;
        NOUNROLL do {
            MEMBAR();
            sfor<0, 4>([&](auto IC) { gath_ld<1, 0, decltype(IC)::value>(c, a, gs); });
            sfor<0, 7>([&](auto IC) { x[decltype(IC)::value] = x3_ld1<0, decltype(IC)::value>(c, a); });
        } while ((gath_bad(c, gs) || x3_bad(x)) && !spin_fail(c, spins, 4u));

        gath_ln(gs, s1, s2);
        x3_make(xres[1], yf, s1, s2);
        x3_store<1>(c, a, xres[1]);
        sfor<0, 7>([&](auto IC) { x3_put1<0, decltype(IC)::value>(c, a, x[decltype(IC)::value]); });
    } else out_store<1>(c, a, xres[1], out);
}

__global__ void __launch_bounds__(256, 1) fused_kernel(const u16* __restrict__ x0, const u16* __restrict__ kin, const u16* __restrict__ vin,
                                                       const u16* __restrict__ sk, const u16* __restrict__ hk, const u16* __restrict__ Dv,
                                                       const u16* __restrict__ w1, const u16* __restrict__ w2,
                                                       unsigned char* __restrict__ ws, u16* __restrict__ out) {
    extern __shared__ __attribute__((aligned(16))) char smem[];
    Ctx c;
    c.L = (LAS char*)smem;
    c.lbase = (u32)(uintptr_t)c.L;
    const int b = blockIdx.x, tid = threadIdx.x;
    c.w = __builtin_amdgcn_readfirstlane(tid >> 6);
    c.rws = mkrs(ws, WS_BYTES);
    c.tmo = (GAS u32*)(ws + 256);

    if (b < 32) {
        const int lane = tid & 63;
        const __amdgpu_buffer_rsrc_t rx = mkrs(x0, TSZ), rw1 = mkrs(w1, D * F * 2), rw2 = mkrs(w2, D * F * 2);
        v4u ep = ld128_asm<true>(c.rws, 0u, OFF_EP);
        v4u xr[2][7];
        sfor<0, 14>([&](auto IC) { constexpr int i = decltype(IC)::value; xr[i / 7][i % 7] = ld128_asm<false, (i / 7) * 896 + (i % 7) * 128>(rx, (u32)((tid >> 3) * 1792 + (tid & 7) * 16), (u32)b * 64 * 896); });
        {
            unsigned long long pc; u32 back, len;
            asm volatile("s_getpc_b64 %0\n.Lfk_here:\n\ts_mov_b32 %1, .Lfk_here-.Lfk_begin\n\ts_mov_b32 %2, .Lfk_end-.Lfk_begin" : "=s"(pc), "=s"(back), "=s"(len));
            touch(c.lbase + L_JK, (u32)(lane & 31) * 128, mkrs((const void*)(pc - back), len), (u32)((b >> 3) * 4 + c.w) * 4096);
        }
        const int blk = min(b * 4 + c.w, 111);
        const u32 vo0 = w_voff<28>(lane), base = c.lbase + 16384 + c.w * 14336;
        asm volatile("s_waitcnt vmcnt(1)" : "+v"(ep), "+v"(xr[0][0]), "+v"(xr[0][1]), "+v"(xr[0][2]), "+v"(xr[0][3]), "+v"(xr[0][4]), "+v"(xr[0][5]), "+v"(xr[0][6]),
                     "+v"(xr[1][0]), "+v"(xr[1][1]), "+v"(xr[1][2]), "+v"(xr[1][3]), "+v"(xr[1][4]), "+v"(xr[1][5]), "+v"(xr[1][6]) :: "memory");
        const u32 tA = ep.x + 1u, tB = ep.y + 1u;
        u32 res[2];
#pragma unroll
        for (int h = 0; h < 2; ++h) {
            float s1 = 0.f, s2 = 0.f;
#pragma unroll
            for (int i = 0; i < 7; ++i)
#pragma unroll
                for (int e = 0; e < 4; ++e) { s1 = dot2(xr[h][i][e], 0x3f803f80u, s1); s2 = dot2(xr[h][i][e], xr[h][i][e], s2); }
#pragma unroll
            for (int o = 1; o < 8; o <<= 1) { s1 += __shfl_xor(s1, o); s2 += __shfl_xor(s2, o); }
            const float m = s1 * (1.0f / D);
            res[h] = pk(m, ln_sd(fmaxf(__builtin_fmaf(-m, m, s2 * (1.0f / D)), 0.f)));
        }
        if ((tid & 7) == 0) st128(v4u{res[0], tA, res[1], tB}, c.rws, (u32)(b * 32 + (tid >> 3)) * 16, OFF_ST0);
        sfor<0, 7>([&](auto JC) { dma_pair<decltype(JC)::value * 2048>(base, vo0, vo0 + 8 * (D * 2) - 1024, rw2, (u32)(blk * 16 * (D * 2) + decltype(JC)::value * 128)); });
        const int rg = tid >> 3, c8 = min(tid & 7, 6);
        v4u wr[14];
        sfor<0, 14>([&](auto IC) { wr[decltype(IC)::value] = ld128_asm<false>(rw1, (u32)(rg * 14 * (F * 2) + c8 * 16), (u32)(b * 112 + decltype(IC)::value * (F * 2))); });
        {
            Ctx h = c; h.w = (blk >> 2) & 3;
            const u32 wl = wl2_of(h, lane) - L_W - h.w * 24576 + 16384 + c.w * 14336;
            WAIT_VM(14);
#pragma unroll
            for (int jj = 0; jj < 7; ++jj)
#pragma unroll
                for (int t = 0; t < 2; ++t) {
                    v4u x = trd2(c.L + (wl ^ (u32)(t * 64)) + jj * 2048, 1024);
                    x.z ^= SGN; x.w ^= SGN;
                    st128(x, c.rws, (u32)lane * 16, OFF_WT2 + (u32)((((jj * 4 + h.w) * 7 + (blk >> 4)) * 4 + (blk & 3)) * 2 + t) * 1024);
                }
        }
        asm volatile("s_waitcnt vmcnt(14)" : "+v"(wr[0]), "+v"(wr[1]), "+v"(wr[2]), "+v"(wr[3]), "+v"(wr[4]), "+v"(wr[5]), "+v"(wr[6]),
                     "+v"(wr[7]), "+v"(wr[8]), "+v"(wr[9]), "+v"(wr[10]), "+v"(wr[11]), "+v"(wr[12]), "+v"(wr[13]) :: "memory");
        float cs[8] = {0, 0, 0, 0, 0, 0, 0, 0};
#pragma unroll
        for (int i = 0; i < 14; ++i)
#pragma unroll
            for (int e = 0; e < 4; ++e) { cs[2 * e] += bflo(wr[i][e]); cs[2 * e + 1] += bfhi(wr[i][e]); }
        LAS float* sf = (LAS float*)c.L;
#pragma unroll
        for (int e = 0; e < 8; ++e) sf[rg * 65 + (tid & 7) * 8 + e] = cs[e];
        WAIT_VM(0);
        bar();
        if (tid == 64) __builtin_amdgcn_raw_buffer_store_b64(v2u{tA, tB}, c.rws, (u32)b * 8, OFF_FL2, AUX_SC1);
        if (tid < 56) {
            float t = 0.f;
#pragma unroll
            for (int i = 0; i < 32; ++i) t += sf[i * 65 + tid];
            const float t2 = __shfl_down(t, 1);
            if ((tid & 1) == 0) st128(v4u{__float_as_uint(t), tA, __float_as_uint(t2), tB}, c.rws, (u32)(b * 28 + (tid >> 1)) * 16, OFF_CSG);
        }
        return;
    }
    asm volatile(".Lfk_begin:");
    const int wb = b - 32;
    { const int xcd = wb & 7, idx = wb >> 3; c.grp = xcd * 4 + idx / 7; c.j = idx % 7; }
    c.gi = c.grp & 3;
    const int w = c.w;
    c.rw1 = mkrs(w1, D * F * 2); c.rw2 = mkrs(w2, D * F * 2);
    c.kb0 = (w + c.gi + 2) & 3;
    const int cp = (wb & 7) * 28 + (wb >> 3);
    W1T W1; W2T W2;

    u32 dval;
    {
        const int g = wb % 7, rb = wb / 7, lane = tid & 63;
        const int rg = (lane & 7) * 2 + ((lane >> 4) & 1), cl = ((lane >> 3) & 1) + 2 * (lane >> 5), cq = 4 * w + cl;
        const __amdgpu_buffer_rsrc_t rk = mkrs(kin, TSZ), rv = mkrs(vin, TSZ), rx = mkrs(x0, TSZ), rh = mkrs(hk, TSZ), rs_ = mkrs(sk, D * 8), rd = mkrs(Dv, D * 2);

        const u32 tb = (u32)((rb * 64) * 896 + g * 128);
        const u32 tv0 = (u32)((lane >> 3) * 896) + (((lane & 7) ^ ((lane >> 5) + 2 * w)) & 7) * 16;
        dma16<false>(c.lbase + T_X + w * 1024, tv0, rx, tb + w * 7168);
        dma16<false>(c.lbase + T_X + (w + 4) * 1024, tv0, rx, tb + (w + 4) * 7168);
        dma16<false>(c.lbase + T_K + w * 1024, tv0 + tb + (w - 1) * 7168, rk, 0u);
        dma16<false>(c.lbase + T_K + (w + 4) * 1024, tv0, rk, tb + (w + 3) * 7168);
        dma16<false>(c.lbase + T_V + w * 1024, tv0, rv, tb + w * 7168);
        dma16<false>(c.lbase + T_V + (w + 4) * 1024, tv0, rv, tb + (w + 4) * 7168);
        if (w == 0) dma16<false>(c.lbase + T_K + 8192, tv0, rk, tb + 7 * 7168);
        else dma16<false>(c.lbase + T_S + (w - 1) * 1024, (u32)lane * 16, rs_, g * 512);
        v4u ep4 = ld128_asm<true>(c.rws, 0u, OFF_EP);
        v4u he[2];
        he[0] = ld128_asm<false>(rh, tid * 16, cp * 8192); he[1] = ld128_asm<false>(rh, tid * 16, cp * 8192 + 4096);
        v4u dv4 = ld128_asm<false>(rd, 0u, cp * 4);
        {
            if (w == 0) touch(c.lbase + L_JK + 3072, (u32)lane * 524288u, c.rws, 4096u);
            else touch(c.lbase + L_JK + 3072, (u32)lane * 262144u, w == 1 ? c.rw1 : w == 2 ? c.rw2 : mkrs(out, TSZ), 0u);
        }

        asm volatile("s_waitcnt vmcnt(4)" : "+v"(ep4) :: "memory");
        c.tagA = __builtin_amdgcn_readfirstlane(ep4.x) + 1u; c.tagB = __builtin_amdgcn_readfirstlane(ep4.y) + 1u;
        bar();

        const u32 a1 = (u32)(rg * 512 + (((cq >> 1) ^ rg) & 7) * 16 + (cq & 1) * 8);
        const u32 ak1 = (u32)(rg * 512 + 1024 + (((cq >> 1) ^ (rg + 2)) & 7) * 16 + (cq & 1) * 8);
        const u32 ak0 = (u32)(rg * 512 + 512 + (((cq >> 1) ^ (rg + 1)) & 7) * 16 + (cq & 1) * 8);
        v2u kr[7], vr[4], xr[4];
#pragma unroll
        for (int i = 0; i < 4; ++i) xr[i] = *(LAS v2u*)(c.L + T_X + a1 + i * 128);
        const v4u w01 = *(LAS v4u*)(c.L + T_S + cq * 32), w23 = *(LAS v4u*)(c.L + T_S + cq * 32 + 16);
#pragma unroll
        for (int i = 0; i < 3; ++i) kr[i] = *(LAS v2u*)(c.L + T_K + ak0 + (i + 1) * 128);
#pragma unroll
        for (int i = 0; i < 4; ++i) { kr[3 + i] = *(LAS v2u*)(c.L + T_K + ak1 + i * 128); vr[i] = *(LAS v2u*)(c.L + T_V + a1 + i * 128); }
#pragma unroll
        for (int h = 0; h < 2; ++h) {
            auto r0 = __builtin_amdgcn_permlane16_swap(__builtin_amdgcn_perm(xr[1][h], xr[0][h], 0x05040100u), __builtin_amdgcn_perm(xr[1][h], xr[0][h], 0x07060302u), false, false);
            auto r1 = __builtin_amdgcn_permlane16_swap(__builtin_amdgcn_perm(xr[3][h], xr[2][h], 0x05040100u), __builtin_amdgcn_perm(xr[3][h], xr[2][h], 0x07060302u), false, false);
            st128(v4u{r0[0], r1[0], r0[1], r1[1]}, c.rws, (u32)((cl * 4 + ((lane >> 4) & 1)) * 4096 + (lane & 7) * 16), OFF_X0G + (g * 64 + 16 * w + 2 * h) * 4096 + rb * 128);
        }

        v4u kg[4];
#pragma unroll
        for (int e = 0; e < 4; ++e) {
            const v4u& wq = e < 2 ? w01 : w23;
            const float w0 = bflo(wq[(e & 1) * 2]), w1_ = bfhi(wq[(e & 1) * 2]), w2_ = bflo(wq[(e & 1) * 2 + 1]), w3 = bfhi(wq[(e & 1) * 2 + 1]);
            float kk[7], o[4];
#pragma unroll
            for (int i = 0; i < 7; ++i) kk[i] = (e & 1) ? bfhi(kr[i][e >> 1]) : bflo(kr[i][e >> 1]);
#pragma unroll
            for (int i = 0; i < 4; i += 2) {
                u32 a_ = pk(kk[i] * w3, kk[i + 1] * w3), b_ = pk(kk[i + 1] * w2_, kk[i + 2] * w2_);
                a_ = pk(bflo(a_) + bflo(b_), bfhi(a_) + bfhi(b_)); b_ = pk(kk[i + 2] * w1_, kk[i + 3] * w1_);
                a_ = pk(bflo(a_) + bflo(b_), bfhi(a_) + bfhi(b_)); b_ = pk(kk[i + 3] * w0, kk[i + 4] * w0);
                a_ = pk(bflo(a_) + bflo(b_), bfhi(a_) + bfhi(b_));
                o[i] = bflo(a_) * ((e & 1) ? bfhi(vr[i][e >> 1]) : bflo(vr[i][e >> 1]));
                o[i + 1] = bfhi(a_) * ((e & 1) ? bfhi(vr[i + 1][e >> 1]) : bflo(vr[i + 1][e >> 1]));
            }
            kg[e] = v4u{pk(o[3], o[2]), c.tagA, pk(o[1], o[0]), c.tagB};
        }

        asm volatile("s_waitcnt vmcnt(0)\n\ts_barrier" : "+v"(he[0]), "+v"(he[1]), "+v"(dv4), "+v"(kg[0]), "+v"(kg[1]), "+v"(kg[2]), "+v"(kg[3]) :: "memory");

#pragma unroll
        for (int e = 0; e < 4; ++e) st128(kg[e], c.rws, (u32)(cl * 4 * 8192 + (15 - rg) * 16), OFF_KVG + ((g * 64 + 16 * w + e) * 512 + 496 - 16 * rb) * 16);
        {
            const WSt wt = w_st<0>(c, lane);
            sfor<0, NW1>([&](auto NC) { w_dma<decltype(NC)::value>(c, wt, w_soff<decltype(NC)::value>(c)); });
        }

        dval = (w >> 1) ? dv4.x >> 16 : dv4.x & 0xffffu;
#pragma unroll
        for (int cc = 0; cc < 2; ++cc) {
            LAS char* cb = c.L + CV_B + cc * CV_CH;
            *(LAS v4u*)(cb + CV_HE + 32 + tid * 16) = he[cc];
            if (tid < 2) { *(LAS v4u*)(cb + CV_HE + tid * 16) = v4u{0, 0, 0, 0}; *(LAS v4u*)(cb + CV_HO + tid * 16) = v4u{0, 0, 0, 0}; }
            if (tid < 32) *(LAS v4u*)(cb + CV_KV + 4096 + tid * 16) = v4u{0, 0, 0, 0};
        }
        bar();
#pragma unroll
        for (int cc = 0; cc < 2; ++cc) {
            const v4u e = he[cc];
            const u32 pv = *(LAS u32*)(c.L + CV_B + cc * CV_CH + CV_HE + 28 + tid * 16);
            *(LAS v4u*)(c.L + CV_B + cc * CV_CH + CV_HO + 32 + tid * 16) = v4u{__builtin_amdgcn_alignbit(e.x, pv, 16), __builtin_amdgcn_alignbit(e.y, e.x, 16),
                                                                            __builtin_amdgcn_alignbit(e.z, e.y, 16), __builtin_amdgcn_alignbit(e.w, e.z, 16)};
        }
        bar();

        SB();
    }
    {
        const int lane = lane_id(), i16 = lane & 15, q = lane >> 4, cc = w >> 1, p = w & 1, ch = 2 * cp + cc;
        LAS char* cb = c.L + CV_B + cc * CV_CH;
        const int o0 = i16 + 1 + 32 * p + 8 * q;
        LAS char* ab = cb + ((o0 & 1) ? CV_HO + 4 : CV_HE) + (o0 >> 1) * 4;
        v4u A[32];
        v4u AD;
        {
            const int jj = p ? -1 : 15 - i16 - 8 * q;
            const u32 dv = dval << ((jj & 1) * 16);
#pragma unroll
            for (int d = 0; d < 4; ++d) AD[d] = (jj >= 0 && (jj >> 1) == d) ? dv : 0u;
        }
        const Ctx cs_ = fresh(c);
        const u32 wl = wl1_of(cs_, lane);
        const WSt wt = w_st<0>(cs_, lane);
        v4u sg[4], g[4];
        SB();
        sfor<0, 8>([&](auto NC) {
            constexpr int n = decltype(NC)::value;
#pragma unroll
            for (int t = n * 4; t < n * 4 + 4; ++t) { LAS u32* pp = (LAS u32*)(ab + t * 128); A[t] = v4u{pp[0], pp[1], pp[2], pp[3]}; }
            SB();
        });

        {
            unsigned spins = 0;
            auto bad4 = [&](const v4u (&x)[4]) { u32 d = 0; for (int t = 0; t < 4; ++t) d = tag_bad(x[t], c.tagA, c.tagB, d); return ANY(d != 0u); };
            NOUNROLL do {
                MEMBAR();
#pragma unroll
                for (int i = 0; i < 4; ++i) g[i] = ld128(c.rws, tid * 16, OFF_KVG + cp * 16384 + i * 4096);
            } while (bad4(g) && !spin_fail(c, spins, 5u));

#pragma unroll
            for (int i = 0; i < 4; ++i) *(LAS v2u*)(c.L + CV_B + (i >> 1) * CV_CH + CV_KV + (tid + 256 * (i & 1)) * 8) = v2u{g[i].x, g[i].z};
        }
        bar();

        LAS char* bb = cb + CV_KV + (2032 - 16 * i16 + 32 * p + 8 * q) * 2;
        f32x4 acc[8];
        float xs[4][4], qs[4][4];
        v4u B[32];
        v2u xg[4];
        B[0] = *(LAS v4u*)(bb + 3 * 128); B[1] = *(LAS v4u*)(bb + 2 * 128); B[2] = *(LAS v4u*)(bb + 128);
        const u32 s4 = (u32)(256 * p + 4 * i16 + q), xo = OFF_X0G + ch * 4096;
        SB();
        sfor<0, 32>([&](auto OC) {
            constexpr int oi = decltype(OC)::value, om = oi - 3;
            constexpr int nm = 8 - (om > 0 ? (om + 3) / 4 : 0) + ((om >= 0 && (om & 3) == 0) ? 1 : 0);
            if constexpr (oi == 9) { bar(); SB(); }
            if constexpr (oi + 3 < 32) B[oi + 3] = *(LAS v4u*)(bb - (om + 3) * 128);
#pragma unroll
            for (int cb_ = 7; cb_ >= 0; --cb_)
                if (4 * cb_ - om >= 0) acc[cb_] = mfma16(A[4 * cb_ - om], B[oi], oi == 0 ? f32x4{0, 0, 0, 0} : acc[cb_]);
            if constexpr (om >= 0 && (om & 3) == 0) acc[om >> 2] = mfma16(AD, B[oi], acc[om >> 2]);
            if constexpr (oi < 2) { sg[2 * oi] = ld128_asm<true>(c.rws, tid * 16, OFF_ST0 + oi * 8192); sg[2 * oi + 1] = ld128_asm<true>(c.rws, tid * 16, OFF_ST0 + oi * 8192 + 4096); }
            if constexpr (oi == 2 || oi == 3) {
                asm volatile("buffer_load_dwordx2 %0, %2, %3, %4 offen offset:%5 sc1\n\tbuffer_load_dwordx2 %1, %2, %3, %4 offen offset:%6 sc1"
                             : "=&v"(xg[2 * oi - 4]), "=&v"(xg[2 * oi - 3]) : "v"(s4 * 8), "s"(c.rws), "s"(xo), "i"((oi - 2) * 1024), "i"((oi - 2) * 1024 + 512) : "memory");
            }
            if constexpr (oi < NW1) w_read1<oi>(cs_, wl, W1, W2);
            if constexpr (oi >= 4 && (oi & 1) == 0 && NW1 + (oi - 4) / 2 < 12) w_dma<NW1 + (oi - 4) / 2>(cs_, wt, w_soff<NW1 + (oi - 4) / 2>(cs_));
            if constexpr (oi == 7) {
                asm volatile("s_waitcnt vmcnt(4)" : "+v"(sg[0]), "+v"(sg[1]), "+v"(sg[2]), "+v"(sg[3]), "+v"(xg[0]), "+v"(xg[1]), "+v"(xg[2]), "+v"(xg[3]) :: "memory");
                auto bad = [&]() { u32 d = 0; for (int t = 0; t < 4; ++t) d = tag_bad(sg[t], c.tagA, c.tagB, d); return ANY(d != 0u); };
                if (bad()) {
                    unsigned spins = 0;
                    NOUNROLL do {
                        MEMBAR();
#pragma unroll
                        for (int t = 0; t < 4; ++t) sg[t] = ld128(c.rws, tid * 16, OFF_ST0 + t * 4096);
                    } while (bad() && !spin_fail(c, spins, 6u));
                }
#pragma unroll
                for (int t = 0; t < 4; ++t) *(LAS v2u*)(c.L + CV_ST + (tid + 256 * t) * 8) = v2u{sg[t].x, sg[t].z};
            }
            if constexpr (oi >= 9 && oi < 13) sg[oi - 9] = *(LAS v4u*)(c.L + CV_ST + s4 * 16 + (oi - 9) * 1024);
            if constexpr (oi >= 11 && oi < 19) {
                constexpr int t = (oi - 11) >> 1, hf = (oi - 11) & 1;
                LV(xg[t]); LV(sg[t]);
                const u32 xw = xg[t][hf];
                const u32 sa = sg[t][2 * hf], sb = sg[t][2 * hf + 1];
                xs[t][2 * hf] = bflo(xw); xs[t][2 * hf + 1] = bfhi(xw);
                qs[t][2 * hf] = (xs[t][2 * hf] - bflo(sa)) * __builtin_amdgcn_rcpf(bfhi(sa));
                qs[t][2 * hf + 1] = (xs[t][2 * hf + 1] - bflo(sb)) * __builtin_amdgcn_rcpf(bfhi(sb));
                asm volatile("" : "+v"(qs[t][2 * hf]), "+v"(qs[t][2 * hf + 1]));
            }
            if constexpr (oi == 23 || oi == 25) {
                const v4u ff = {~0u, ~0u, ~0u, ~0u};
                st128(ff, c.rws, lane * 16, OFF_X3 + (cs_.grp * 56 + (oi - 23) * 14 + 4 * cs_.j + cs_.w) * 1024);
            }
            ilv<nm, 2>();
            SB();
        });

        LAS char* ex = c.L;
        f32x4 fin[4];
        if (p == 0) {
#pragma unroll
            for (int t = 0; t < 4; ++t) { *(LAS f32x4*)(ex + w * 4096 + t * 1024 + lane * 16) = acc[4 + t]; fin[t] = acc[t]; }
        } else {
#pragma unroll
            for (int t = 0; t < 4; ++t) { *(LAS f32x4*)(ex + w * 4096 + t * 1024 + lane * 16) = acc[t]; fin[t] = acc[4 + t]; }
        }
        bar();

        const u32 toff = (u32)((32 * p + 8 * (q & 1) + (i16 >> 1)) * 28672 + ch * 64 + (i16 & 1) * 32 + (q >> 1) * 16);
        v2u y[4], x1[4];
#pragma unroll
        for (int t = 0; t < 4; ++t) {
            fin[t] += *(LAS f32x4*)(ex + (w ^ 1) * 4096 + t * 1024 + lane * 16);
            y[t] = v2u{pk(fin[t][0], fin[t][1]), pk(fin[t][2], fin[t][3])};
            x1[t] = v2u{pk(__builtin_fmaf(qs[t][0], bflo(y[t].x), xs[t][0]), __builtin_fmaf(qs[t][1], bfhi(y[t].x), xs[t][1])),
                        pk(__builtin_fmaf(qs[t][2], bflo(y[t].y), xs[t][2]), __builtin_fmaf(qs[t][3], bfhi(y[t].y), xs[t][3]))};
        }
        auto merge = [&](const v2u& a, const v2u& b_) {
            auto r0 = __builtin_amdgcn_permlane16_swap(a.x, b_.x, false, false);
            auto r1 = __builtin_amdgcn_permlane16_swap(a.y, b_.y, false, false);
            return v4u{r0[0], r1[0], r0[1], r1[1]};
        };
        st128(merge(x1[0], x1[1]), c.rws, toff, OFF_X1);
        st128(merge(x1[2], x1[3]), c.rws, toff, OFF_X1 + 16 * 28672);
        st128(merge(y[0], y[1]), c.rws, toff, OFF_YT);
        st128(merge(y[2], y[3]), c.rws, toff, OFF_YT + 16 * 28672);

        sfor<12, 12 + NW1>([&](auto NC) { w_dma<decltype(NC)::value>(cs_, wt, w_soff<decltype(NC)::value>(cs_)); });
        WAIT_VM(6 + 2 * NW1);
        sfor<NW1, 12>([&](auto NC) { w_read1<decltype(NC)::value>(cs_, wl, W1, W2); });
        WAIT_LGKM(0);
        sfor<12 + NW1, 20>([&](auto NC) { w_dma<decltype(NC)::value>(cs_, wt, w_soff<decltype(NC)::value>(cs_)); });

        WAIT_VM(16);
        bar();
        if (tid < 8) __builtin_amdgcn_raw_buffer_store_b64(v2u{c.tagA, c.tagB}, c.rws, (u32)(tid * 2048 + wb * 8), OFF_FL, AUX_SC1);
        MEMBAR();

        sfor<20, 24>([&](auto NC) { w_dma<decltype(NC)::value>(cs_, wt, w_soff<decltype(NC)::value>(cs_)); });
        WAIT_VM(12);
        sfor<12, 18>([&](auto NC) { w_read1<decltype(NC)::value>(cs_, wl, W1, W2); });
        WAIT_LGKM(0);
        sfor<24, 28>([&](auto NC) { w_dma<decltype(NC)::value>(cs_, wt, w_soff<decltype(NC)::value>(cs_)); });

    }
    Ad ad;
    ad.l16 = lane_id() * 16;
    ad.hst = OFF_H + (c.grp * 224 + 16 * c.j + 4 * w) * 1024;
    ad.x3o = OFF_X3 + (c.grp * 56 + 4 * c.j + w) * 1024;
    ad.sto = OFF_ST + c.grp * 2 * ST_HG;
    asm volatile("" : "+s"(ad.hst), "+s"(ad.x3o), "+s"(ad.sto));
    u32 wso[7];
#pragma unroll
    for (int m = 0; m < 7; ++m) {
        const int mm = member(c, m);
        wso[m] = OFF_WT2 + ((c.j * 4 + w) * 7 + mm) * 8192; ad.so[m] = ad.hst + (mm - c.j) * 16384;
        asm volatile("" : "+s"(wso[m]), "+s"(ad.so[m]));
    }
    const u32 ring = c.lbase + L_W + w * 24576;
    auto w2_ld = [&](auto RC) {
        constexpr int R = decltype(RC)::value, f = w2_step_of(28 + R), sub = k_sub(f);
        const u32 so = wso[k_mem(f)] + (sub >> 1) * 4096;
        if constexpr (R < 16) {
            W2[R][0] = ld128_asm<true, (sub & 1) * 2048>(c.rws, ad.l16, so);
            if constexpr (R < 15) W2[R][1] = ld128_asm<true, (sub & 1) * 2048 + 1024>(c.rws, ad.l16, so);
            else dma16<true>(c.lbase + L_JK + w * 1024, ad.l16, c.rws, so + (sub & 1) * 2048 + 1024);
        } else {
            dma_pair_sc1<(R - 16) * 2048>(ring, ad.l16, c.rws, so + (sub & 1) * 2048);
        }
    };
    {
        v4u cg = ld128_asm<true>(c.rws, tok32(ad), OFF_CSG + c.j * 2048 + w * 512);
        v4u f2 = ld128_asm<true>(c.rws, ad.l16 & 240u, OFF_FL2);
        WAIT_VM(10);
        w_read<18, 24>(c, W1, W2);
        const u32 fo = w == 0 ? OFF_FL + (wb & 7) * 2048 : OFF_CSG + c.j * 2048;
        v4u f0 = ld128_asm<true>(c.rws, ad.l16, fo), f1 = ld128_asm<true>(c.rws, 1024 + min(ad.l16, 752u), fo);
        asm volatile("s_waitcnt vmcnt(2)" : "+v"(cg), "+v"(f2) :: "memory");
        w_read<24, 28>(c, W1, W2);

        asm volatile("s_waitcnt vmcnt(0)" : "+v"(f0), "+v"(f1) :: "memory");
        if (w == 0) {
            auto bad = [&]() { return ANY(xor_or(f0.x, c.tagA, xor_or(f0.z, c.tagA, xor_or(f1.x, c.tagA, xor_or(f1.z, c.tagA, tag_bad(f0, c.tagB, c.tagB, tag_bad(f1, c.tagB, c.tagB, 0u)))))) != 0u); };
            if (bad()) {
                unsigned spins = 0;
                NOUNROLL do {
                    MEMBAR();
                    f0 = ld128(c.rws, ad.l16, OFF_FL + (wb & 7) * 2048); f1 = ld128(c.rws, 1024 + min(ad.l16, 752u), OFF_FL + (wb & 7) * 2048);
                } while (bad() && !spin_fail(c, spins, 7u));
            }
            if (wb == 0 && ad.l16 == 0) __builtin_amdgcn_raw_buffer_store_b64(v2u{c.tagA, c.tagB}, c.rws, 0, OFF_EP, AUX_SC1);
        }
        bar();

        {
            const u32 xb = c.lbase + L_X + w * 7168, xs_ = OFF_X1 + c.grp * 57344 + w * 7168;
            sfor<0, 14>([&](auto IC) {
                constexpr int i = decltype(IC)::value, o = (i / 7) * 28672 + (i % 7) * 1024;
                dma16<true>(xb + o, ad.l16, c.rws, xs_ + o);
            });
        }

        {
            auto bad = [&]() { return ANY(xor_or(f2.x, c.tagA, xor_or(f2.z, c.tagA, tag_bad(f2, c.tagB, c.tagB, 0u))) != 0u); };
            if (bad()) {
                unsigned spins = 0;
                NOUNROLL do { MEMBAR(); f2 = ld128(c.rws, ad.l16 & 240u, OFF_FL2); } while (bad() && !spin_fail(c, spins, 12u));
            }
            if (ANY(tag_bad(cg, c.tagA, c.tagB, 0u) != 0u)) {
                unsigned spins = 0;
                NOUNROLL do { MEMBAR(); cg = ld128(c.rws, tok32(ad), OFF_CSG + c.j * 2048 + w * 512); } while (ANY(tag_bad(cg, c.tagA, c.tagB, 0u) != 0u) && !spin_fail(c, spins, 8u));
            }
            *(LAS v2u*)(c.L + L_CS + w * 256 + (tok32(ad) >> 1)) = v2u{cg.x, cg.z};
        }
        sfor<0, NW3>(w2_ld);
        WAIT_VM(7 + 2 * NW3);
        bar();

    }

    pin_a<0, 25>(W1);
    v4u hb[56], xres[2];
    G1Out ga, gb;
    v2u gate;
    f32x4 csq[3];
    St1 st;
    ad.ca = L_CS + (w * 64 + (ad.l16 >> 9) * 4) * 4;
    using M0 = std::integral_constant<int, 0>; using M1 = std::integral_constant<int, 1>;
    {
        st.ad = xtr_of(ad.l16 >> 4) + w * 7168;
        auto stats = [&](auto MC, auto HC, auto FC, G1Out& o) {
            constexpr int H = decltype(HC)::value, f = decltype(FC)::value;
            constexpr bool TR = decltype(MC)::value == 0;
            if constexpr (f == 0) { st.s1 = 0.f; st.s2 = 0.f; }
            if constexpr (f >= 2 && f < 23) { constexpr int ph = (f - 2) % 3, k = (f - 2) / 3; st1_acc<k, ph * 3, ph == 2 ? 8 : ph * 3 + 3>(st); }
            if constexpr (f % 3 == 0 && f < 21) st1_read<TR, H, f / 3>(c, st);
            if constexpr (f == 23) part_put(c, ad, L_SP + H * 1024, st.s1, st.s2);
            if constexpr (f == 24) { SB(); asm volatile("s_waitcnt lgkmcnt(%0)\n\ts_barrier" :: "i"(TR ? 2 : 1) : "memory"); }
            if constexpr (f == 25) { const f32x2 t = part_get(c, ad, L_SP + H * 1024); ln_scalars(t[0], t[1], o.mb, o.r); asm volatile("" : "+v"(o.mb), "+v"(o.r)); }
        };
        constexpr unsigned REG = 0x9AAAAAAu;
        g1_main<true, 0, REG>(c, ad, W1, ga.acc, [&](auto FC) {
            constexpr int f = decltype(FC)::value;
            if constexpr ((f & 1) == 0) w2_ld(std::integral_constant<int, NW3 + f / 2>{});
            if constexpr (f == 22) WAIT_VM(24);
            stats(M0{}, M0{}, FC, ga);
            if constexpr (f >= 26) csq[f - 26] = epi_cs(c, ad, f - 26);
        });

        auto side_b = [&](auto MC, auto FC) {
            constexpr int f = decltype(FC)::value, M = decltype(MC)::value;
            if constexpr (f < 6) csq[(f + 2) % 3] = epi_cs(c, ad, f + 2);
            if constexpr (f < 8) g1_epi<0, M, f>(c, ad, ga, csq[f % 3], hb[f >> 1]);
            if constexpr (f < 8 && (f & 1)) h_store<0, f / 2>(c, ad, hb[f >> 1]);
            if constexpr (f == 18) gate = gate_ld<true>(c, ad, 0);
            if constexpr (f == 19) xres[0] = xres_ld<0, M>(c, ad);
            if constexpr (f == 23) {
                SB();
                asm volatile("s_waitcnt vmcnt(%1)" : "+v"(gate) : "i"(0) : "memory");
                gate_wait<M>(c, ad, 0, gate);
            }
            if constexpr (f >= 24) hb[f - 20] = h_next<f - 20>(c, ad);
            if constexpr (f >= 26) csq[f - 26] = epi_cs(c, ad, f - 26);
        };
        g1_main<true, 1, REG>(c, ad, W1, gb.acc, [&](auto FC) {
            constexpr int f = decltype(FC)::value;
            if constexpr ((f & 1) == 0 && NW3 + 14 + f / 2 < 28) w2_ld(std::integral_constant<int, NW3 + 14 + f / 2>{});
            stats(M0{}, M1{}, FC, gb);
            side_b(M0{}, FC);
        });

        pin_v<0, 16>(W2);
        pin_a<0, 25>(W1);

        g2_pair<0>(c, ad, W2, hb, gb, csq, ga.r, xres, c.rws);
        pin_a<0, 25>(W1);

        v4u t[7];
        st.ad = ad.l16 + w * 7168;

        bar();

        g1_main<false, 0, REG>(c, ad, W1, ga.acc, [&](auto FC) {
            constexpr int f = decltype(FC)::value;
            stats(M1{}, M0{}, FC, ga);
            if constexpr (f >= 12 && f < 19) t[f - 12] = x3_ld1<1, f - 12>(c, ad);
            if constexpr (f == 24) x3_wait<1>(c, ad, t);
            if constexpr (f >= 25) { x3_put1<1, 2 * (f - 25)>(c, ad, t[2 * (f - 25)]); x3_put1<1, 2 * (f - 25) + 1>(c, ad, t[2 * (f - 25) + 1]); }
            if constexpr (f == 27) x3_put1<1, 6>(c, ad, t[6]);
            if constexpr (f >= 26) csq[f - 26] = epi_cs(c, ad, f - 26);
        });

        bar();

        g1_main<false, 1, REG>(c, ad, W1, gb.acc, [&](auto FC) {
            stats(M1{}, M1{}, FC, gb);
            side_b(M1{}, FC);
        });

        const __attribute__((address_space(4))) char* ka = (const __attribute__((address_space(4))) char*)__builtin_amdgcn_kernarg_segment_ptr();
        asm volatile("" : "+s"(ka));
        g2_pair<1>(c, ad, W2, hb, gb, csq, ga.r, xres, mkrs(*(void* const __attribute__((address_space(4)))*)(ka + 72), TSZ));

    }
    asm volatile(".Lfk_end:");
}

extern "C" void kernel_launch(void* const* d_in, const int* in_sizes, int n_in,
                              void* d_out, int out_size, void* d_ws, size_t ws_size,
                              hipStream_t stream) {
    (void)hipFuncSetAttribute((const void*)fused_kernel, hipFuncAttributeMaxDynamicSharedMemorySize, L_TOT);
    fused_kernel<<<256, 256, L_TOT, stream>>>((const u16*)d_in[0], (const u16*)d_in[1], (const u16*)d_in[2], (const u16*)d_in[3], (const u16*)d_in[4],
                                              (const u16*)d_in[5], (const u16*)d_in[7], (const u16*)d_in[8], (unsigned char*)d_ws, (u16*)d_out);
}
```
